```python
import torch

def _softmax(x, axis=-1):
    return torch.softmax(x, dim=axis)
def _gelu(x):
    return torch.nn.functional.gelu(x, approximate="tanh")
def _silu(x):
    return torch.nn.functional.silu(x)
def _sigmoid(x):
    return torch.sigmoid(x)
def _layernorm(x, axis=-1, eps=1e-5):
    m = x.mean(dim=axis, keepdim=True)
    v = x.var(dim=axis, keepdim=True, unbiased=False)
    return (x - m) / torch.sqrt(v + eps)
def _scatter_add(tgt, idx, src, axis=0):
    return tgt.clone().index_add_(axis, idx, src)
def _segment_sum(src, seg, n):
    out = torch.zeros((n,) + src.shape[1:], dtype=src.dtype)
    return out.index_add_(0, seg, src)
def _pad(x, pw):
    flat = tuple(v for pair in reversed(pw) for v in pair)
    return torch.nn.functional.pad(x, flat)

kernel_name = 'parallel_sum__mha_block_cached__geglu_ffn__decode'

def setup_inputs():

    _w = (lambda a: (lambda t: t.to(torch.bfloat16) if t.is_floating_point() else t)(torch.as_tensor(a)))
    def _A_setup():
        B, Sq, D = 2, 1, 576
        H, Sk, Dh = 32, 8192, 128
        return {
            "x": _w((lambda *s: torch.randn(*s, dtype=torch.bfloat16) * 0.05)(B, Sq, D)),
            "k_cache": _w((lambda *s: torch.randn(*s, dtype=torch.bfloat16) * 3.2)(B, H, Sk, Dh)),
            "v_cache": _w((lambda *s: torch.randn(*s, dtype=torch.bfloat16) * 1.6)(B, H, Sk, Dh)),
            "wq": _w((lambda *s: torch.randn(*s, dtype=torch.bfloat16) * 3.2)(D, H * Dh) * (D ** -0.5)),
            "wo": _w((lambda *s: torch.randn(*s, dtype=torch.bfloat16) * 1.6)(H * Dh, D) * ((H * Dh) ** -0.5)),
        }
    _A_in = _A_setup()

    def _B_setup():
        B, S, D, F = 2, 1, 576, 4608
        return {
            "x": _w((lambda *s: torch.randn(*s, dtype=torch.bfloat16) * 0.05)(B, S, D)),
            "w_gate": _w((lambda *s: torch.randn(*s, dtype=torch.bfloat16) * 0.05)(D, F)),
            "w_up": _w((lambda *s: torch.randn(*s, dtype=torch.bfloat16) * 0.05)(D, F)),
            "w_down": _w((lambda *s: torch.randn(*s, dtype=torch.bfloat16) * 0.05)(F, D)),
        }
    _B_in = _B_setup()

    return {
        "A_x": _A_in["x"],
        "A_k_cache": _A_in["k_cache"],
        "A_v_cache": _A_in["v_cache"],
        "A_wq": _A_in["wq"],
        "A_wo": _A_in["wo"],
        "B_x": _B_in["x"],
        "B_w_gate": _B_in["w_gate"],
        "B_w_up": _B_in["w_up"],
        "B_w_down": _B_in["w_down"],
    }

def reference(A_x, A_k_cache, A_v_cache, A_wq, A_wo, B_x, B_w_gate, B_w_up, B_w_down):
    def _A_ref(x, k_cache, v_cache, wq, wo):
        B, Sq, D = x.shape
        H, Dh = 32, 128
        q = (lambda x, *d: x.permute(*d))((x @ wq).reshape(B, Sq, H, Dh), 0, 2, 1, 3)
        scores = torch.einsum("bhqd,bhkd->bhqk", q, k_cache) * (Dh ** -0.5)
        p = _softmax(scores, axis=-1)
        o = torch.einsum("bhqk,bhkd->bhqd", p, v_cache)
        return (lambda x, *d: x.permute(*d))(o, 0, 2, 1, 3).reshape(B, Sq, H * Dh) @ wo

    def _B_ref(x, w_gate, w_up, w_down):
        return (_gelu(x @ w_gate) * (x @ w_up)) @ w_down

    x = A_x
    xn = _layernorm(x, axis=-1)
    return x + _A_ref(xn, A_k_cache, A_v_cache, A_wq, A_wo) + _B_ref(xn, B_w_gate, B_w_up, B_w_down)
```

```cpp
#include <hip/hip_runtime.h>
#include <hip/hip_bf16.h>
#include <stdint.h>

#define MIMIC 0

typedef unsigned short u16;
typedef unsigned v4u __attribute__((ext_vector_type(4)));

constexpr int B = 2, D = 576, H = 32, SK = 8192, DH = 128, F = 4608;
constexpr int NSPLIT = 8;

__device__ __forceinline__ float bf2f(u16 b) { return __builtin_bit_cast(float, (unsigned)b << 16); }
__device__ __forceinline__ float rbf(float f) {
  unsigned u = __builtin_bit_cast(unsigned, f);
  u += 0x7FFFu + ((u >> 16) & 1u);
  return __builtin_bit_cast(float, u & 0xffff0000u);
}
__device__ __forceinline__ float R(float f) { return MIMIC ? rbf(f) : f; }

__device__ void layernorm(const u16* x, float* xn  , float* red) {
  for (int b = 0; b < B; ++b) {
    float s = 0.f;
    for (int i = threadIdx.x; i < D; i += blockDim.x) s += bf2f(x[b * D + i]);
    red[threadIdx.x] = s; __syncthreads();
    for (int o = blockDim.x / 2; o > 0; o >>= 1) { if ((int)threadIdx.x < o) red[threadIdx.x] += red[threadIdx.x + o]; __syncthreads(); }
    float mean = red[0] / D; __syncthreads();
    float s2 = 0.f;
    for (int i = threadIdx.x; i < D; i += blockDim.x) { float d = bf2f(x[b * D + i]) - mean; s2 += d * d; }
    red[threadIdx.x] = s2; __syncthreads();
    for (int o = blockDim.x / 2; o > 0; o >>= 1) { if ((int)threadIdx.x < o) red[threadIdx.x] += red[threadIdx.x + o]; __syncthreads(); }
    float var = red[0] / D; __syncthreads();
    float m = R(mean), v = R(var);
    float sd = R(sqrtf(R(v + 1e-5f)));
    for (int i = threadIdx.x; i < D; i += blockDim.x) xn[b * D + i] = R(R(bf2f(x[b * D + i]) - m) / sd);
  }
  __syncthreads();
}

__global__ void k1(const u16* x, const u16* wq, const u16* wg, const u16* wu, float* q, float* hbuf) {
  __shared__ float xn[B * D]; __shared__ float red[256]; __shared__ float part[4][4][64];
  layernorm(x, xn, red);
  int col = threadIdx.x & 63, kg = threadIdx.x >> 6;
  int blk = blockIdx.x;
  if (blk < 64) {
    int n = blk * 64 + col;
    float a0 = 0, a1 = 0;
    for (int k = kg; k < D; k += 4) { float w = bf2f(wq[(size_t)k * 4096 + n]); a0 += xn[k] * w; a1 += xn[D + k] * w; }
    part[0][kg][col] = a0; part[1][kg][col] = a1; __syncthreads();
    if (kg < 2) { float s = part[kg][0][col] + part[kg][1][col] + part[kg][2][col] + part[kg][3][col]; q[kg * 4096 + n] = rbf(s); }
  } else {
    int n = (blk - 64) * 64 + col;
    float g0 = 0, g1 = 0, u0 = 0, u1 = 0;
    for (int k = kg; k < D; k += 4) { float w = bf2f(wg[(size_t)k * F + n]); float v = bf2f(wu[(size_t)k * F + n]); g0 += xn[k] * w; g1 += xn[D + k] * w; u0 += xn[k] * v; u1 += xn[D + k] * v; }
    part[0][kg][col] = g0; part[1][kg][col] = g1; part[2][kg][col] = u0; part[3][kg][col] = u1; __syncthreads();
    if (kg < 2) {
      float g = R(part[kg][0][col] + part[kg][1][col] + part[kg][2][col] + part[kg][3][col]);
      float u = R(part[2 + kg][0][col] + part[2 + kg][1][col] + part[2 + kg][2][col] + part[2 + kg][3][col]);
      float t = 0.7978845608028654f * (g + 0.044715f * g * g * g);
      float ge = R(0.5f * g * (1.f + tanhf(t)));
      hbuf[kg * F + n] = R(ge * u);
    }
  }
}

__global__ void k2(const float* q, const u16* kc, const u16* vc, float* pm, float* pl, float* po) {
  constexpr int KEYS = SK / NSPLIT;
  __shared__ float sc[KEYS]; __shared__ float red[256]; __shared__ float oacc[16][DH];
  int s = blockIdx.x, h = blockIdx.y, b = blockIdx.z;
  int tid = threadIdx.x, c = tid & 15, r = tid >> 4;
  const u16* kb = kc + ((size_t)(b * H + h) * SK + s * KEYS) * DH;
  const u16* vb = vc + ((size_t)(b * H + h) * SK + s * KEYS) * DH;
  float qv[8];
  for (int j = 0; j < 8; ++j) qv[j] = q[b * 4096 + h * DH + c * 8 + j];
  for (int k = r; k < KEYS; k += 16) {
    v4u kk = __builtin_nontemporal_load((const v4u*)(kb + (size_t)k * DH + c * 8));
    float d = 0;
    for (int j = 0; j < 4; ++j) { d += qv[2 * j] * __builtin_bit_cast(float, kk[j] << 16) + qv[2 * j + 1] * __builtin_bit_cast(float, kk[j] & 0xffff0000u); }
    for (int o = 8; o > 0; o >>= 1) d += __shfl_xor(d, o);
    if (c == 0) sc[k] = R(R(d) * 0.08838834764831845f);
  }
  __syncthreads();
  float mx = -INFINITY;
  for (int k = tid; k < KEYS; k += 256) mx = fmaxf(mx, sc[k]);
  red[tid] = mx; __syncthreads();
  for (int o = 128; o > 0; o >>= 1) { if (tid < o) red[tid] = fmaxf(red[tid], red[tid + o]); __syncthreads(); }
  mx = red[0]; __syncthreads();
  float l = 0;
  for (int k = tid; k < KEYS; k += 256) { float p = __expf(sc[k] - mx); sc[k] = p; l += p; }
  red[tid] = l; __syncthreads();
  for (int o = 128; o > 0; o >>= 1) { if (tid < o) red[tid] += red[tid + o]; __syncthreads(); }
  l = red[0];
  float acc[8] = {0, 0, 0, 0, 0, 0, 0, 0};
  for (int k = r; k < KEYS; k += 16) {
    v4u vv = __builtin_nontemporal_load((const v4u*)(vb + (size_t)k * DH + c * 8));
    float p = sc[k];
    for (int j = 0; j < 4; ++j) { acc[2 * j] += p * __builtin_bit_cast(float, vv[j] << 16); acc[2 * j + 1] += p * __builtin_bit_cast(float, vv[j] & 0xffff0000u); }
  }
  for (int j = 0; j < 8; ++j) oacc[r][c * 8 + j] = acc[j];
  __syncthreads();
  int slot = (b * H + h) * NSPLIT + s;
  if (tid < DH) { float o = 0; for (int i = 0; i < 16; ++i) o += oacc[i][tid]; po[slot * DH + tid] = o; }
  if (tid == 0) { pm[slot] = mx; pl[slot] = l; }
}

__global__ void k3(const float* pm, const float* pl, const float* po, float* o) {
  int bh = blockIdx.x, d = threadIdx.x;
  float M = -INFINITY;
  for (int s = 0; s < NSPLIT; ++s) M = fmaxf(M, pm[bh * NSPLIT + s]);
  float L = 0, a = 0;
  for (int s = 0; s < NSPLIT; ++s) { float w = __expf(pm[bh * NSPLIT + s] - M); L += w * pl[bh * NSPLIT + s]; a += w * po[(bh * NSPLIT + s) * DH + d]; }
  o[bh * DH + d] = R(a / L);
}

__global__ void k4(const float* o, const float* hbuf, const u16* wo, const u16* wd, float* part) {
  __shared__ float sp[2][4][64];
  int col = threadIdx.x & 63, kg = threadIdx.x >> 6;
  int n = blockIdx.x * 64 + col, kb = blockIdx.y;
  float a0 = 0, a1 = 0;
  if (kb < 16) {
    for (int k = kb * 256 + kg; k < kb * 256 + 256; k += 4) { float w = bf2f(wo[(size_t)k * D + n]); a0 += o[k] * w; a1 += o[4096 + k] * w; }
  } else {
    int k0 = (kb - 16) * 256;
    for (int k = k0 + kg; k < k0 + 256; k += 4) { float w = bf2f(wd[(size_t)k * D + n]); a0 += hbuf[k] * w; a1 += hbuf[F + k] * w; }
  }
  sp[0][kg][col] = a0; sp[1][kg][col] = a1; __syncthreads();
  if (kg < 2) part[(kb * 2 + kg) * D + n] = sp[kg][0][col] + sp[kg][1][col] + sp[kg][2][col] + sp[kg][3][col];
}
__global__ void k5(const u16* x, const float* part, __hip_bfloat16* out) {
  int i = blockIdx.x * 64 + threadIdx.x;
  int b = i / D, n = i % D;
  float a = 0, f = 0;
  for (int kb = 0; kb < 16; ++kb) a += part[(kb * 2 + b) * D + n];
  for (int kb = 16; kb < 34; ++kb) f += part[(kb * 2 + b) * D + n];
  float r = R(bf2f(x[i]) + R(a)) + R(f);
  out[i] = __float2bfloat16(r);
}

extern "C" void kernel_launch(void* const* d_in, const int* in_sizes, int n_in, void* d_out, int out_size, void* d_ws, size_t ws_size, hipStream_t stream) {
  const u16* x = (const u16*)d_in[0];
  const u16* kc = (const u16*)d_in[1];
  const u16* vc = (const u16*)d_in[2];
  const u16* wq = (const u16*)d_in[3];
  const u16* wo = (const u16*)d_in[4];
  const u16* wg = (const u16*)d_in[6];
  const u16* wu = (const u16*)d_in[7];
  const u16* wd = (const u16*)d_in[8];
  float* ws = (float*)d_ws;
  float* q = ws;
  float* hb = q + 8192;
  float* pm = hb + 9216;
  float* pl = pm + 512;
  float* po = pl + 512;
  float* o = po + 65536;
  float* part = o + 8192;
  k1<<<64 + 72, 256, 0, stream>>>(x, wq, wg, wu, q, hb);
  k2<<<dim3(NSPLIT, H, B), 256, 0, stream>>>(q, kc, vc, pm, pl, po);
  k3<<<64, 128, 0, stream>>>(pm, pl, po, o);
  k4<<<dim3(9, 34), 256, 0, stream>>>(o, hb, wo, wd, part);
  k5<<<18, 64, 0, stream>>>(x, part, (__hip_bfloat16*)d_out);
}
```

```cpp
#include <hip/hip_runtime.h>
#include <hip/hip_bf16.h>

#define WIN 8
#define QSLEEP 20
#define LATE_CNT 4
#define CODE_PF 1
#define DIAG 0

#define GAS __attribute__((address_space(1)))
#define LAS __attribute__((address_space(3)))
typedef unsigned v4u __attribute__((ext_vector_type(4)));
typedef unsigned v3u __attribute__((ext_vector_type(3)));
typedef unsigned v2u __attribute__((ext_vector_type(2)));
typedef float f32x4 __attribute__((ext_vector_type(4)));
typedef float f32x2 __attribute__((ext_vector_type(2)));
typedef __bf16 bf16x8 __attribute__((ext_vector_type(8)));
typedef __bf16 bf16x2 __attribute__((ext_vector_type(2)));
typedef __amdgpu_buffer_rsrc_t rsrc_t;

constexpr int E_OFF = 0;
constexpr int QG_OFF = 4096;
constexpr int ML_OFF = QG_OFF + 524288;
constexpr int GU_OFF = ML_OFF + 8192;
constexpr int HG_OFF = GU_OFF + 589824;
constexpr int OG_OFF = HG_OFF + 73728;
constexpr int YG_OFF = OG_OFF + 262144;
constexpr int WS_USED = YG_OFF + 294912;
constexpr int TS_OFF = 4 << 20;

constexpr int L_XN = 0, XN_S = 580;
constexpr int L_HD = 1160, HD_S = 148;
constexpr int L_OD = L_HD + 2 * HD_S;
constexpr int L_QB = L_OD + 2 * HD_S;
constexpr int L_OW = L_QB + 64;
constexpr int L_ZB = L_OW + 512;
constexpr int L_ML = L_ZB + 256;
constexpr int L_FL = L_ML + 8;
constexpr int F_KD = 0, F_LATE = 4, F_OD = 8, F_Q = 12, F_GO = 13, F_T = 14, F_Z = 15, F_XN = 16;
static_assert(L_FL % 4 == 0 && L_OW % 4 == 0 && L_ZB % 4 == 0 && L_QB % 4 == 0, "16-byte aligned LDS pieces");
constexpr int LDS_BYTES = 84 * 1024;

constexpr int AUX_NT = 2, AUX_SC1 = 16;
constexpr unsigned OOB = 0x80000000u;
constexpr unsigned CODE_BYTES = 13 * 1024;
constexpr int SPIN_CAP = 1 << 18;
constexpr float LOG2E = 1.4426950408889634f;

__device__ __forceinline__ rsrc_t mk(const void* p, unsigned bytes) { return __builtin_amdgcn_make_buffer_rsrc((void*)p, 0, bytes, 0x00020000); }
__device__ __forceinline__ v4u ld_nt(rsrc_t r, unsigned vo, unsigned so) { return __builtin_amdgcn_raw_buffer_load_b128(r, vo, so, AUX_NT); }
__device__ __forceinline__ v4u ld_sc(rsrc_t r, unsigned vo, unsigned so) { return __builtin_amdgcn_raw_buffer_load_b128(r, vo, so, AUX_SC1); }
__device__ __forceinline__ void st_sc(rsrc_t r, unsigned vo, unsigned so, v4u v) { __builtin_amdgcn_raw_buffer_store_b128(v, r, vo, so, AUX_SC1); }
__device__ __forceinline__ float u2f(unsigned u) { return __builtin_bit_cast(float, u); }
__device__ __forceinline__ unsigned f2u(float f) { return __builtin_bit_cast(unsigned, f); }
__device__ __forceinline__ unsigned cvtpk(float lo, float hi) {
  bf16x2 r = {(__bf16)lo, (__bf16)hi};
  return __builtin_bit_cast(unsigned, r);
}
__device__ __forceinline__ float rbf(float f) { return (float)(__bf16)f; }
__device__ __forceinline__ float ex2(float f) { return __builtin_amdgcn_exp2f(f); }
__device__ __forceinline__ float dot2(unsigned a, unsigned b, float c) {
  return __builtin_amdgcn_fdot2_f32_bf16(__builtin_bit_cast(bf16x2, a), __builtin_bit_cast(bf16x2, b), c, false);
}
__device__ __forceinline__ f32x4 mfma(v4u a, v4u b, f32x4 c) {
  return __builtin_amdgcn_mfma_f32_16x16x32_bf16(__builtin_bit_cast(bf16x8, a), __builtin_bit_cast(bf16x8, b), c, 0, 0, 0);
}
template <int CTRL> __device__ __forceinline__ float dpp(float x) { return u2f(__builtin_amdgcn_mov_dpp(f2u(x), CTRL, 0xf, 0xf, true)); }
template <int CTRL> __device__ __forceinline__ unsigned dppu(unsigned x) { return __builtin_amdgcn_mov_dpp(x, CTRL, 0xf, 0xf, true); }
constexpr int XOR1 = 0xB1, XOR2 = 0x4E, XOR7 = 0x141, XOR8 = 0x128, ROR4 = 0x124;
__device__ __forceinline__ float sum16x(float x) { auto s = __builtin_amdgcn_permlane16_swap(f2u(x), f2u(x), false, false); return u2f(s[0]) + u2f(s[1]); }
__device__ __forceinline__ float sum32x(float x) { auto s = __builtin_amdgcn_permlane32_swap(f2u(x), f2u(x), false, false); return u2f(s[0]) + u2f(s[1]); }
__device__ __forceinline__ float max16x(float x) { auto s = __builtin_amdgcn_permlane16_swap(f2u(x), f2u(x), false, false); return fmaxf(u2f(s[0]), u2f(s[1])); }
__device__ __forceinline__ float max32x(float x) { auto s = __builtin_amdgcn_permlane32_swap(f2u(x), f2u(x), false, false); return fmaxf(u2f(s[0]), u2f(s[1])); }
__device__ __forceinline__ float rowsum(float x) { x += dpp<XOR1>(x); x += dpp<XOR2>(x); x += dpp<XOR7>(x); x += dpp<XOR8>(x); return x; }
__device__ __forceinline__ float rowmax(float x) { x = fmaxf(x, dpp<XOR1>(x)); x = fmaxf(x, dpp<XOR2>(x)); x = fmaxf(x, dpp<XOR7>(x)); x = fmaxf(x, dpp<XOR8>(x)); return x; }
#define SB() __builtin_amdgcn_sched_barrier(0)
#define LDS_FENCE() asm volatile("s_waitcnt lgkmcnt(0)" ::: "memory")
#define BARRIER() do { SB(); asm volatile("s_barrier" ::: "memory"); SB(); } while (0)
__device__ __forceinline__ unsigned lds_ld(LAS unsigned* p) { return __hip_atomic_load(p, __ATOMIC_RELAXED, __HIP_MEMORY_SCOPE_WORKGROUP); }
__device__ __forceinline__ void lds_wait(LAS unsigned* p) {
  while (__builtin_amdgcn_readfirstlane(lds_ld(p)) == 0) __builtin_amdgcn_s_sleep(1);
  asm volatile("" ::: "memory");
}
__device__ __forceinline__ void lds_wait4(LAS unsigned* p, int lane, int n) {
  while (__builtin_popcountll(__builtin_amdgcn_ballot_w64(lds_ld(p + (lane & 3)) != 0)) < 16 * n) __builtin_amdgcn_s_sleep(1);
  asm volatile("" ::: "memory");
}
__device__ __forceinline__ void lds_signal(LAS unsigned* p, int lane) {
  LDS_FENCE();
  if (lane == 0) __hip_atomic_store(p, 1u, __ATOMIC_RELAXED, __HIP_MEMORY_SCOPE_WORKGROUP);
}
__device__ __forceinline__ bool none_bad(unsigned bad) { return __builtin_amdgcn_ballot_w64(bad != 0u) == 0ull; }
__device__ __forceinline__ unsigned tagbad(const v4u& v, unsigned T) { return (v.y ^ T) | (v.w ^ T); }
__device__ __forceinline__ v4u gran(float a, float b, unsigned T) { return v4u{f2u(a), T, f2u(b), T}; }
__device__ __forceinline__ v4u afrag(unsigned dup, const v4u& m) { return v4u{dup & m.x, dup & m.y, dup & m.z, dup & m.w}; }

#define STAMP(slot) do {} while (0)

template <int LO, int HI> __device__ __forceinline__ void gemv(f32x4& a0, f32x4& a1, const LAS unsigned* xs, const v4u& am, const v4u (&w)[36]) {
  unsigned xv[HI - LO];
#pragma unroll
  for (int i = LO; i < HI; ++i) xv[i - LO] = xs[4 * i];
  SB();
#pragma unroll
  for (int i = LO; i < HI; i += 2) {
    a0 = mfma(afrag(xv[i - LO], am), w[i], a0);
    a1 = mfma(afrag(xv[i + 1 - LO], am), w[i + 1], a1);
  }
}

__global__ void __launch_bounds__(384, 2)
fused_decode(const char* xg, const char* kc, const char* vc, const char* wq, const char* wo, const char* wg, const char* wu, const char* wd,
             char* out, char* ws) {
  extern __shared__ __attribute__((aligned(16))) unsigned smem[];
  LAS unsigned* L = (LAS unsigned*)smem;
  LAS float* Lf = (LAS float*)smem;
  unsigned long long pc0 = __builtin_amdgcn_s_getpc();
  asm volatile("" : "+s"(pc0));
  asm volatile("" : "+s"(xg), "+s"(kc), "+s"(vc), "+s"(wq), "+s"(wo), "+s"(wg), "+s"(wu), "+s"(wd), "+s"(out), "+s"(ws));
  const int lane = threadIdx.x & 63, wave = __builtin_amdgcn_readfirstlane(threadIdx.x >> 6);
  const int g = lane >> 4, c = lane & 15;
  const int bid = blockIdx.x, xc = bid & 7, r = bid >> 3;
  const int h = xc + 8 * (r & 3), j = r >> 2, s = j & 3, b = j >> 2;
  const unsigned hm = (c & 1) ? 0xffff0000u : 0x0000ffffu;
  const int mt = (c & 7) >> 1;

  if (wave >= 2) {
    const int sw = wave - 2;
    const size_t cb = (size_t)((b * 32 + h) * 4 + s) * 524288;
    const char* kb = kc + cb;
    const char* vb = vc + cb;
    const rsrc_t rK = mk(kb, 524288), rV = mk(vb, 524288);
    const unsigned vo = sw * 1024 + lane * 16;
    BARRIER();
    v4u a[WIN];
#pragma unroll
    for (int i = 0; i < WIN; ++i) a[i] = ld_nt(rK, vo, i * 4096);
    SB();
    if (sw == 0) STAMP(16);
    lds_wait(L + L_FL + F_Q);
    const v4u q = *(LAS v4u*)(L + L_QB + c * 4);
    if (sw == 0) STAMP(17);
    float sc[8];
#pragma unroll
    for (int i = 0; i < 8; ++i) sc[i] = 0.f;
#pragma unroll 1
    for (int blk = 0; blk < 8; ++blk) {
      const rsrc_t rN = mk(blk < 7 ? kb : vb, 524288);
      const unsigned sC = blk * 65536, sN = blk < 7 ? sC + 65536 : 0;
      float x[16];
#pragma unroll
      for (int i = 0; i < 16; ++i) {
        const v4u k = a[i % WIN];
        float d = dot2(k.x, q.x, 0.f); d = dot2(k.y, q.y, d); d = dot2(k.z, q.z, d); d = dot2(k.w, q.w, d);
        x[i] = d;
        asm volatile("" : "+v"(x[i]));
        SB();
        if (i + WIN < 16) a[i % WIN] = ld_nt(rK, vo, sC + (i + WIN) * 4096);
        else a[i % WIN] = ld_nt(rN, vo, sN + (i + WIN - 16) * 4096);
        SB();
      }
#define TR_STEP(HALF, CTRL) _Pragma("unroll") for (int u = 0; u < (HALF); ++u) { const float lo_ = x[u], hi_ = x[u + (HALF)]; \
        const float t1_ = lo_ + dpp<CTRL>(lo_), t2_ = hi_ + dpp<CTRL>(hi_); x[u] = (c & (HALF)) ? t2_ : t1_; }
      TR_STEP(8, XOR8) TR_STEP(4, XOR7) TR_STEP(2, XOR2) TR_STEP(1, XOR1)
#undef TR_STEP
#pragma unroll
      for (int i = 0; i < 7; ++i) sc[i] = sc[i + 1];
      sc[7] = rbf(rbf(x[0]) * 0.08838834764831845f);
    }
    float m = sc[0];
#pragma unroll
    for (int i = 1; i < 8; ++i) m = fmaxf(m, sc[i]);
    m = max32x(max16x(rowmax(m)));
    float l = 0.f;
    unsigned pp[8];
#pragma unroll
    for (int i = 0; i < 8; ++i) { const float p = ex2((sc[i] - m) * LOG2E); l += p; pp[i] = cvtpk(p, p); }
    l = sum32x(sum16x(rowsum(l)));
    if (lane == 0) { Lf[L_ML + 2 * sw] = m; Lf[L_ML + 2 * sw + 1] = l; }
    lds_signal(L + L_FL + F_KD + sw, lane);
    if (sw == 0) STAMP(18);
    const v4u am = {(c < 8 && mt == 0) ? hm : 0u, (c < 8 && mt == 1) ? hm : 0u, (c < 8 && mt == 2) ? hm : 0u, (c < 8 && mt == 3) ? hm : 0u};
    f32x4 acc = {0.f, 0.f, 0.f, 0.f};
#define PV(I, REFILL) { acc = mfma(afrag(dppu<0x150 + (I)>(pp[0]), am), a[(I) % WIN], acc); asm volatile("" : "+v"(acc)); SB(); \
      if (REFILL) a[(I) % WIN] = ld_nt(rV, vo, sC + ((I) + WIN) * 4096); SB(); }
#pragma unroll 1
    for (int blk = 0; blk < 7; ++blk) {
      const unsigned sC = blk * 65536;
      PV(0, 1) PV(1, 1) PV(2, 1) PV(3, 1) PV(4, 1) PV(5, 1) PV(6, 1) PV(7, 1)
      PV(8, 1) PV(9, 1) PV(10, 1) PV(11, 1) PV(12, 1) PV(13, 1) PV(14, 1) PV(15, 1)
#pragma unroll
      for (int i = 0; i < 7; ++i) pp[i] = pp[i + 1];
    }
    {
      const unsigned sC = 7 * 65536;
      if (WIN == 16) lds_signal(L + L_FL + F_LATE + sw, lane);
      PV(0, WIN < 16) PV(1, WIN < 15) PV(2, WIN < 14) PV(3, WIN < 13) PV(4, WIN < 12) PV(5, WIN < 11) PV(6, WIN < 10) PV(7, WIN < 9)
      if (WIN == 8) lds_signal(L + L_FL + F_LATE + sw, lane);
      PV(8, 0) PV(9, 0) PV(10, 0) PV(11, 0) PV(12, 0) PV(13, 0) PV(14, 0) PV(15, 0)
    }
#undef PV
    if (lane < 32) *(LAS f32x4*)(Lf + L_OW + sw * 128 + c * 8 + g * 4) = acc;
    lds_signal(L + L_FL + F_OD + sw, lane);
    if (sw == 0) STAMP(19);
    return;
  }

  const bool isA = wave == 0;
  STAMP(wave * 8 + 0);
  const rsrc_t rW = mk(ws, WS_USED);
  const v4u am = {mt == 0 ? hm : 0u, mt == 1 ? hm : 0u, mt == 2 ? hm : 0u, mt == 3 ? hm : 0u};
  v4u w[36];
  v4u pl[8];
  unsigned T;
  if (isA) {
    const int hq = (r & 7) + 8 * (xc & 3), jq = (r >> 3) + 4 * (xc >> 2);
    const rsrc_t rQ = mk(wq + (size_t)(72 * jq) * 8192 + hq * 256, 72 * 8192 - hq * 256);
    const unsigned E = __builtin_amdgcn_raw_buffer_load_b32(rW, E_OFF, 0, AUX_SC1);
#pragma unroll
    for (int i = 0; i < 18; ++i) w[i] = ld_nt(rQ, g * 8192 + c * 16, i * 32768);
    BARRIER();
    T = __builtin_amdgcn_readfirstlane(E) + 0x9E3779B9u;
    if (lane == 0) L[L_FL + F_T] = T;
    lds_wait(L + L_FL + F_XN);
    STAMP(1);
    f32x4 a0 = {0.f, 0.f, 0.f, 0.f}, a1 = a0;
    gemv<0, 18>(a0, a1, L + L_XN + (c >> 3) * XN_S + 72 * jq + g, am, w);
    a0 += a1;
    const unsigned o = QG_OFF + ((hq * 2 + (g >> 1)) * 8 + jq) * 1024 + (c * 8 + (g & 1) * 4) * 8;
    st_sc(rW, o, 0, gran(a0[0], a0[1], T));
    st_sc(rW, o + 16, 0, gran(a0[2], a0[3], T));
    STAMP(2);
    __builtin_amdgcn_s_sleep(QSLEEP);
#pragma unroll
    for (int i = 0; i < 8; ++i) pl[i] = ld_sc(rW, lane * 16, QG_OFF + ((h * 2 + b) * 8 + i) * 1024);
    SB();
    lds_signal(L + L_FL + F_GO, lane);
  } else {
    v3u xr[3];
    const rsrc_t rX = mk(xg, 2304);
#pragma unroll
    for (int k = 0; k < 3; ++k) xr[k] = __builtin_amdgcn_raw_buffer_load_b96(rX, (lane >> 5) * 1152 + (lane & 31) * 12, k * 384, 0);
    if (lane < 20) L[L_FL + lane] = 0u;
    LDS_FENCE();
    BARRIER();
    const unsigned cv = __builtin_amdgcn_raw_buffer_load_b32(mk((const void*)(pc0 & ~255ull), CODE_BYTES), (r * 4 + (lane & 3)) * 128, 0, 0);
    SB();
    float xv[18];
#pragma unroll
    for (int k = 0; k < 3; ++k)
#pragma unroll
      for (int t = 0; t < 3; ++t) { xv[k * 6 + 2 * t] = u2f(xr[k][t] << 16); xv[k * 6 + 2 * t + 1] = u2f(xr[k][t] & 0xffff0000u); }
    float sum = 0.f;
#pragma unroll
    for (int i = 0; i < 18; ++i) sum += xv[i];
    const float mean = sum16x(rowsum(sum)) * (1.0f / 576.0f);
    float sq = 0.f;
#pragma unroll
    for (int i = 0; i < 18; ++i) { const float d = xv[i] - mean; sq = fmaf(d, d, sq); }
    const float var = sum16x(rowsum(sq)) * (1.0f / 576.0f);
    const float mb = rbf(mean), sd = rbf(__builtin_sqrtf(rbf(rbf(var) + 1e-5f))), rs = 1.0f / sd;
    LAS v2u* dst = (LAS v2u*)(L + L_XN + (lane >> 5) * XN_S + (lane & 31) * 6);
#pragma unroll
    for (int k = 0; k < 3; ++k)
#pragma unroll
      for (int t = 0; t < 3; ++t) {
        float n[2];
#pragma unroll
        for (int e = 0; e < 2; ++e) { const float d = rbf(xv[k * 6 + 2 * t + e] - mb); const float q0 = d * rs; n[e] = fmaf(fmaf(-q0, sd, d), rs, q0); }
        dst[k * 96 + t] = v2u{cvtpk(n[0], n[0]), cvtpk(n[1], n[1])};
      }
    lds_signal(L + L_FL + F_XN, lane);
    STAMP(9);
    lds_wait(L + L_FL + F_GO);
    asm volatile("" :: "v"(cv));
    T = __builtin_amdgcn_readfirstlane(L[L_FL + F_T]);
  }
  const int hp = 4 * xc + (r & 3), qd = j >> 1, half = j & 1;
  const unsigned tvo = c < 9 ? (unsigned)(c * 16) : OOB;
  {
    const unsigned cofs = hp * 288 + half * 144;
    const rsrc_t rG = mk((isA ? wu : wg) + (size_t)(144 * qd) * 9216 + cofs, 144 * 9216 - cofs);
#pragma unroll
    for (int i = 0; i < 36; ++i) w[i] = ld_nt(rG, tvo + g * 9216, i * 36864);
    SB();
  }
  if (isA) {
    unsigned bad = 0;
    float q0 = 0.f, q1 = 0.f;
#pragma unroll
    for (int i = 0; i < 8; ++i) { bad |= tagbad(pl[i], T); q0 += u2f(pl[i].x); q1 += u2f(pl[i].z); }
    if (!none_bad(bad)) {
      _Pragma("unroll 1") for (int spin = 0; spin < SPIN_CAP; ++spin) {
      asm volatile("" ::: "memory");
#pragma unroll
        for (int i = 0; i < 8; ++i) pl[i] = ld_sc(rW, lane * 16, QG_OFF + ((h * 2 + b) * 8 + i) * 1024);
        bad = 0; q0 = 0.f; q1 = 0.f;
#pragma unroll
        for (int i = 0; i < 8; ++i) { bad |= tagbad(pl[i], T); q0 += u2f(pl[i].x); q1 += u2f(pl[i].z); }
        if (none_bad(bad)) break;
      }
      STAMP(21);
    }
    L[L_QB + lane] = cvtpk(q0, q1);
    lds_signal(L + L_FL + F_Q, lane);
    STAMP(3);
  }
  {
    f32x4 a0 = {0.f, 0.f, 0.f, 0.f}, a1 = a0;
    gemv<0, 36>(a0, a1, L + L_XN + (c >> 3) * XN_S + 144 * qd + g, am, w);
    a0 += a1;
    const unsigned o = c < 9 ? (unsigned)(GU_OFF + ((((hp * 2 + half) * 4 + qd) * 2 + (isA ? 1 : 0)) * 2 + (g >> 1)) * 576 + (c * 8 + (g & 1) * 4) * 8) : OOB;
    st_sc(rW, o, 0, gran(a0[0], a0[1], T));
    st_sc(rW, o + 16, 0, gran(a0[2], a0[3], T));
  }
  STAMP(wave * 8 + 4);
  float fw[4] = {0.f, 0.f, 0.f, 0.f};
  unsigned xres = 0;
  const int kcons = h;
  if (isA) {
    lds_wait4(L + L_FL + F_KD, lane, 4);
    float mw[4], lw[4];
#pragma unroll
    for (int i = 0; i < 4; ++i) { mw[i] = Lf[L_ML + 2 * i]; lw[i] = Lf[L_ML + 2 * i + 1]; }
    const float mg = fmaxf(fmaxf(mw[0], mw[1]), fmaxf(mw[2], mw[3]));
    float lg = 0.f;
#pragma unroll
    for (int i = 0; i < 4; ++i) lg += lw[i] * ex2((mw[i] - mg) * LOG2E);
    const unsigned mlb = ML_OFF + (b * 32 + h) * 128;
    st_sc(rW, lane == 0 ? mlb + s * 16 : OOB, 0, gran(mg, lg, T));
    v4u v;
    _Pragma("unroll 1") for (int spin = 0; spin < SPIN_CAP; ++spin) {
      asm volatile("" ::: "memory");
      v = ld_sc(rW, (lane & 3) * 16, mlb);
      if (none_bad(tagbad(v, T))) break;
      __builtin_amdgcn_s_sleep(8);
    }
    const float ms = u2f(v.x);
    float M = fmaxf(ms, dpp<XOR1>(ms)); M = fmaxf(M, dpp<XOR2>(M));
    float Ls = u2f(v.z) * ex2((ms - M) * LOG2E);
    Ls += dpp<XOR1>(Ls); Ls += dpp<XOR2>(Ls);
    const float inv = 1.0f / Ls;
#pragma unroll
    for (int i = 0; i < 4; ++i) fw[i] = ex2((mw[i] - M) * LOG2E) * inv;
  } else {
    const unsigned gb = GU_OFF + (hp * 2 + (j >> 2)) * (4 * 4 * 576);
    const unsigned o = c < 9 ? (unsigned)(g * 576 + (j & 3) * 144 + c * 16) : OOB;
    _Pragma("unroll 1") for (int spin = 0; spin < SPIN_CAP; ++spin) {
      asm volatile("" ::: "memory");
      unsigned bad = 0;
#pragma unroll
      for (int i = 0; i < 4; ++i) pl[i] = ld_sc(rW, o, gb + i * (4 * 576));
#pragma unroll
      for (int i = 0; i < 4; ++i) bad |= tagbad(pl[i], T);
      if (none_bad(c < 9 ? bad : 0u)) break;
      __builtin_amdgcn_s_sleep(8);
    }
    float hv[2];
#pragma unroll
    for (int e = 0; e < 2; ++e) {
      const float sm = rbf((u2f(pl[0][2 * e]) + u2f(pl[1][2 * e])) + (u2f(pl[2][2 * e]) + u2f(pl[3][2 * e])));
      auto sw2 = __builtin_amdgcn_permlane32_swap(f2u(sm), f2u(sm), false, false);
      const float gt = u2f(sw2[0]), up = u2f(sw2[1]);
      const float t2 = 1.5957691216057308f * (gt + 0.044715f * gt * gt * gt);
      const float ge = rbf(gt / (1.0f + ex2(-t2 * LOG2E)));
      hv[e] = ge * up;
    }
    const unsigned ho = (c < 9 && g < 2) ? (unsigned)(HG_OFF + hp * 2304 + g * 1152 + j * 144 + c * 16) : OOB;
    st_sc(rW, ho, 0, v4u{cvtpk(hv[0], hv[0]), T, cvtpk(hv[1], hv[1]), T});
    if (kcons < 18) xres = *(const GAS unsigned*)(xg + (kcons / 9) * 1152 + (72 * j + 8 * (kcons % 9)) * 2 + (lane & 3) * 4);
    _Pragma("unroll 1") for (int spin = 0; spin < SPIN_CAP; ++spin) {
      asm volatile("" ::: "memory");
#pragma unroll
      for (int i = 0; i < 3; ++i) pl[i] = ld_sc(rW, (i < 2 || lane < 16) ? (unsigned)(lane * 16) : OOB, HG_OFF + hp * 2304 + i * 1024);
      if (none_bad(tagbad(pl[0], T) | tagbad(pl[1], T) | (lane < 16 ? tagbad(pl[2], T) : 0u))) break;
      __builtin_amdgcn_s_sleep(8);
    }
#pragma unroll
    for (int i = 0; i < 3; ++i) {
      const int e = (i * 64 + lane) * 2;
      const int bb = e >= 144 ? 1 : 0;
      if (i < 2 || lane < 16) *(LAS v2u*)(L + L_HD + bb * HD_S + e - bb * 144) = v2u{pl[i].x, pl[i].z};
    }
    LDS_FENCE();
  }
  STAMP(wave * 8 + 5);
  lds_wait4(L + L_FL + F_LATE, lane, LATE_CNT);
  {
    const rsrc_t rO = mk(isA ? wo + (size_t)(128 * h) * 1152 + j * 144 : wd + (size_t)(144 * hp) * 1152 + j * 144, (isA ? 128 : 144) * 1152 - j * 144);
#pragma unroll
    for (int i = 0; i < 32; ++i) w[i] = ld_nt(rO, tvo + g * 1152, i * 4608);
    if (!isA) {
#pragma unroll
      for (int i = 32; i < 36; ++i) w[i] = ld_nt(rO, tvo + g * 1152, i * 4608);
    }
    SB();
  }
  if (isA) {
    lds_wait4(L + L_FL + F_OD, lane, 4);
    float o0 = 0.f, o1 = 0.f;
#pragma unroll
    for (int i = 0; i < 4; ++i) { const f32x2 t = *(LAS f32x2*)(Lf + L_OW + i * 128 + lane * 2); o0 = fmaf(fw[i], t.x, o0); o1 = fmaf(fw[i], t.y, o1); }
    st_sc(rW, lane * 16, OG_OFF + (h * 8 + j) * 1024, gran(o0, o1, T));
    STAMP(6);
    _Pragma("unroll 1") for (int spin = 0; spin < SPIN_CAP; ++spin) {
      asm volatile("" ::: "memory");
      unsigned bad = 0;
#pragma unroll
      for (int i = 0; i < 8; ++i) pl[i] = ld_sc(rW, lane * 16, OG_OFF + (h * 8 + i) * 1024);
#pragma unroll
      for (int i = 0; i < 8; ++i) bad |= tagbad(pl[i], T);
      if (none_bad(bad)) break;
    }
#pragma unroll
    for (int bb = 0; bb < 2; ++bb) {
      const float a0 = (u2f(pl[4 * bb].x) + u2f(pl[4 * bb + 1].x)) + (u2f(pl[4 * bb + 2].x) + u2f(pl[4 * bb + 3].x));
      const float a1 = (u2f(pl[4 * bb].z) + u2f(pl[4 * bb + 1].z)) + (u2f(pl[4 * bb + 2].z) + u2f(pl[4 * bb + 3].z));
      *(LAS v2u*)(L + L_OD + bb * HD_S + lane * 2) = v2u{cvtpk(a0, a0), cvtpk(a1, a1)};
    }
    LDS_FENCE();
    STAMP(7);
  }
  f32x4 acc = {0.f, 0.f, 0.f, 0.f};
  {
    f32x4 a1 = acc;
    const LAS unsigned* xs = L + (isA ? L_OD : L_HD) + (c >> 3) * HD_S + g;
    gemv<0, 32>(acc, a1, xs, am, w);
    if (!isA) gemv<32, 36>(acc, a1, xs, am, w);
    acc += a1;
  }
  if (isA) {
    lds_wait(L + L_FL + F_Z);
    acc += *(LAS f32x4*)(Lf + L_ZB + lane * 4);
    const unsigned o = c < 9 ? (unsigned)(YG_OFF + ((j * 18 + (g >> 1) * 9 + c) * 32 + h) * 64 + (g & 1) * 32) : OOB;
    st_sc(rW, o, 0, gran(acc[0], acc[1], T));
    st_sc(rW, o + 16, 0, gran(acc[2], acc[3], T));
    STAMP(20);
  } else {
    *(LAS f32x4*)(Lf + L_ZB + lane * 4) = acc;
    lds_signal(L + L_FL + F_Z, lane);
    STAMP(14);
    if (kcons < 18) {
      const unsigned yb = YG_OFF + (j * 18 + kcons) * 2048;
      _Pragma("unroll 1") for (int spin = 0; spin < SPIN_CAP; ++spin) {
      asm volatile("" ::: "memory");
        pl[0] = ld_sc(rW, lane * 16, yb);
        pl[1] = ld_sc(rW, lane * 16, yb + 1024);
        if (none_bad(tagbad(pl[0], T) | tagbad(pl[1], T))) break;
      }
      float y0 = u2f(pl[0].x) + u2f(pl[1].x), y1 = u2f(pl[0].z) + u2f(pl[1].z);
      y0 += dpp<ROR4>(y0); y1 += dpp<ROR4>(y1);
      y0 += dpp<XOR8>(y0); y1 += dpp<XOR8>(y1);
      y0 = sum32x(sum16x(y0)); y1 = sum32x(sum16x(y1));
      if (lane < 4) *(GAS unsigned*)(out + (kcons / 9) * 1152 + (72 * j + 8 * (kcons % 9)) * 2 + lane * 4) = cvtpk(u2f(xres << 16) + y0, u2f(xres & 0xffff0000u) + y1);
      if (bid == 0 && lane == 0) __builtin_amdgcn_raw_buffer_store_b32(T, rW, E_OFF, 0, AUX_SC1);
      STAMP(15);
    }
  }
}

extern "C" void kernel_launch(void* const* d_in, const int* in_sizes, int n_in, void* d_out, int out_size, void* d_ws, size_t ws_size,
                              hipStream_t stream) {
  (void)hipFuncSetAttribute((const void*)fused_decode, hipFuncAttributeMaxDynamicSharedMemorySize, LDS_BYTES);
  fused_decode<<<256, 384, LDS_BYTES, stream>>>((const char*)d_in[0], (const char*)d_in[1], (const char*)d_in[2], (const char*)d_in[3],
                                                (const char*)d_in[4], (const char*)d_in[6], (const char*)d_in[7], (const char*)d_in[8],
                                                (char*)d_out, (char*)d_ws);
}
```

```cpp
#define WIN 8
#define MAP 4
#include <hip/hip_runtime.h>
#include <hip/hip_bf16.h>
#include <cstdio>

#define QSLEEP 20
#define LATE_CNT 4
#define CODE_PF 1
#define KVAUX 2
#define PRINTPTR 0
#define DIAG 0

#define GAS __attribute__((address_space(1)))
#define LAS __attribute__((address_space(3)))
typedef unsigned v4u __attribute__((ext_vector_type(4)));
typedef unsigned v3u __attribute__((ext_vector_type(3)));
typedef unsigned v2u __attribute__((ext_vector_type(2)));
typedef float f32x4 __attribute__((ext_vector_type(4)));
typedef float f32x2 __attribute__((ext_vector_type(2)));
typedef __bf16 bf16x8 __attribute__((ext_vector_type(8)));
typedef __bf16 bf16x2 __attribute__((ext_vector_type(2)));
typedef __amdgpu_buffer_rsrc_t rsrc_t;

constexpr int E_OFF = 0;
constexpr int QG_OFF = 4096;
constexpr int ML_OFF = QG_OFF + 524288;
constexpr int GU_OFF = ML_OFF + 8192;
constexpr int HG_OFF = GU_OFF + 589824;
constexpr int OG_OFF = HG_OFF + 73728;
constexpr int YG_OFF = OG_OFF + 262144;
constexpr int WS_USED = YG_OFF + 294912;
constexpr int TS_OFF = 4 << 20;

constexpr int L_XN = 0, XN_S = 580;
constexpr int L_HD = 1160, HD_S = 148;
constexpr int L_OD = L_HD + 2 * HD_S;
constexpr int L_QB = L_OD + 2 * HD_S;
constexpr int L_OW = L_QB + 64;
constexpr int L_ZB = L_OW + 512;
constexpr int L_ML = L_ZB + 256;
constexpr int L_FL = L_ML + 8;
constexpr int F_KD = 0, F_LATE = 4, F_OD = 8, F_Q = 12, F_GO = 13, F_T = 14, F_Z = 15, F_XN = 16;
static_assert(L_FL % 4 == 0 && L_OW % 4 == 0 && L_ZB % 4 == 0 && L_QB % 4 == 0, "16-byte aligned LDS pieces");
constexpr int LDS_BYTES = 84 * 1024;

constexpr int AUX_NT = 2, AUX_SC1 = 16;
constexpr unsigned OOB = 0x80000000u;
constexpr unsigned CODE_BYTES = 13 * 1024;
constexpr int SPIN_CAP = 1 << 18;
constexpr float LOG2E = 1.4426950408889634f;

__device__ __forceinline__ rsrc_t mk(const void* p, unsigned bytes) { return __builtin_amdgcn_make_buffer_rsrc((void*)p, 0, bytes, 0x00020000); }
__device__ __forceinline__ v4u ld_nt(rsrc_t r, unsigned vo, unsigned so) { return __builtin_amdgcn_raw_buffer_load_b128(r, vo, so, AUX_NT); }
__device__ __forceinline__ v4u ld_kv(rsrc_t r, unsigned vo, unsigned so) { return __builtin_amdgcn_raw_buffer_load_b128(r, vo, so, KVAUX); }
__device__ __forceinline__ v4u ld_sc(rsrc_t r, unsigned vo, unsigned so) { return __builtin_amdgcn_raw_buffer_load_b128(r, vo, so, AUX_SC1); }
__device__ __forceinline__ void st_sc(rsrc_t r, unsigned vo, unsigned so, v4u v) { __builtin_amdgcn_raw_buffer_store_b128(v, r, vo, so, AUX_SC1); }
__device__ __forceinline__ float u2f(unsigned u) { return __builtin_bit_cast(float, u); }
__device__ __forceinline__ unsigned f2u(float f) { return __builtin_bit_cast(unsigned, f); }
__device__ __forceinline__ unsigned cvtpk(float lo, float hi) {
  bf16x2 r = {(__bf16)lo, (__bf16)hi};
  return __builtin_bit_cast(unsigned, r);
}
__device__ __forceinline__ float rbf(float f) { return (float)(__bf16)f; }
__device__ __forceinline__ float ex2(float f) { return __builtin_amdgcn_exp2f(f); }
__device__ __forceinline__ float dot2(unsigned a, unsigned b, float c) {
  return __builtin_amdgcn_fdot2_f32_bf16(__builtin_bit_cast(bf16x2, a), __builtin_bit_cast(bf16x2, b), c, false);
}
__device__ __forceinline__ f32x4 mfma(v4u a, v4u b, f32x4 c) {
  return __builtin_amdgcn_mfma_f32_16x16x32_bf16(__builtin_bit_cast(bf16x8, a), __builtin_bit_cast(bf16x8, b), c, 0, 0, 0);
}
template <int CTRL> __device__ __forceinline__ float dpp(float x) { return u2f(__builtin_amdgcn_mov_dpp(f2u(x), CTRL, 0xf, 0xf, true)); }
template <int CTRL> __device__ __forceinline__ unsigned dppu(unsigned x) { return __builtin_amdgcn_mov_dpp(x, CTRL, 0xf, 0xf, true); }
constexpr int XOR1 = 0xB1, XOR2 = 0x4E, XOR7 = 0x141, XOR8 = 0x128, ROR4 = 0x124;
__device__ __forceinline__ float sum16x(float x) { auto s = __builtin_amdgcn_permlane16_swap(f2u(x), f2u(x), false, false); return u2f(s[0]) + u2f(s[1]); }
__device__ __forceinline__ float sum32x(float x) { auto s = __builtin_amdgcn_permlane32_swap(f2u(x), f2u(x), false, false); return u2f(s[0]) + u2f(s[1]); }
__device__ __forceinline__ float max16x(float x) { auto s = __builtin_amdgcn_permlane16_swap(f2u(x), f2u(x), false, false); return fmaxf(u2f(s[0]), u2f(s[1])); }
__device__ __forceinline__ float max32x(float x) { auto s = __builtin_amdgcn_permlane32_swap(f2u(x), f2u(x), false, false); return fmaxf(u2f(s[0]), u2f(s[1])); }
__device__ __forceinline__ float rowsum(float x) { x += dpp<XOR1>(x); x += dpp<XOR2>(x); x += dpp<XOR7>(x); x += dpp<XOR8>(x); return x; }
__device__ __forceinline__ float rowmax(float x) { x = fmaxf(x, dpp<XOR1>(x)); x = fmaxf(x, dpp<XOR2>(x)); x = fmaxf(x, dpp<XOR7>(x)); x = fmaxf(x, dpp<XOR8>(x)); return x; }
#define SB() __builtin_amdgcn_sched_barrier(0)
#define LDS_FENCE() asm volatile("s_waitcnt lgkmcnt(0)" ::: "memory")
#define BARRIER() do { SB(); asm volatile("s_barrier" ::: "memory"); SB(); } while (0)
__device__ __forceinline__ unsigned lds_ld(LAS unsigned* p) { return __hip_atomic_load(p, __ATOMIC_RELAXED, __HIP_MEMORY_SCOPE_WORKGROUP); }
__device__ __forceinline__ void lds_wait(LAS unsigned* p) {
  while (__builtin_amdgcn_readfirstlane(lds_ld(p)) == 0) __builtin_amdgcn_s_sleep(1);
  asm volatile("" ::: "memory");
}
__device__ __forceinline__ void lds_wait4(LAS unsigned* p, int lane, int n) {
  while (__builtin_popcountll(__builtin_amdgcn_ballot_w64(lds_ld(p + (lane & 3)) != 0)) < 16 * n) __builtin_amdgcn_s_sleep(1);
  asm volatile("" ::: "memory");
}
__device__ __forceinline__ void lds_signal(LAS unsigned* p, int lane) {
  LDS_FENCE();
  if (lane == 0) __hip_atomic_store(p, 1u, __ATOMIC_RELAXED, __HIP_MEMORY_SCOPE_WORKGROUP);
}
__device__ __forceinline__ bool none_bad(unsigned bad) { return __builtin_amdgcn_ballot_w64(bad != 0u) == 0ull; }
__device__ __forceinline__ unsigned tagbad(const v4u& v, unsigned T) { return (v.y ^ T) | (v.w ^ T); }
__device__ __forceinline__ v4u gran(float a, float b, unsigned T) { return v4u{f2u(a), T, f2u(b), T}; }
__device__ __forceinline__ v4u afrag(unsigned dup, const v4u& m) { return v4u{dup & m.x, dup & m.y, dup & m.z, dup & m.w}; }

#define STAMP(slot) do {} while (0)

template <int LO, int HI> __device__ __forceinline__ void gemv(f32x4& a0, f32x4& a1, const LAS unsigned* xs, const v4u& am, const v4u (&w)[36]) {
  unsigned xv[HI - LO];
#pragma unroll
  for (int i = LO; i < HI; ++i) xv[i - LO] = xs[4 * i];
  SB();
#pragma unroll
  for (int i = LO; i < HI; i += 2) {
    a0 = mfma(afrag(xv[i - LO], am), w[i], a0);
    a1 = mfma(afrag(xv[i + 1 - LO], am), w[i + 1], a1);
  }
}

__global__ void __launch_bounds__(384, 2)
fused_decode(const char* xg, const char* kc, const char* vc, const char* wq, const char* wo, const char* wg, const char* wu, const char* wd,
             char* out, char* ws) {
  extern __shared__ __attribute__((aligned(16))) unsigned smem[];
  LAS unsigned* L = (LAS unsigned*)smem;
  LAS float* Lf = (LAS float*)smem;
  unsigned long long pc0 = __builtin_amdgcn_s_getpc();
  asm volatile("" : "+s"(pc0));
  asm volatile("" : "+s"(xg), "+s"(kc), "+s"(vc), "+s"(wq), "+s"(wo), "+s"(wg), "+s"(wu), "+s"(wd), "+s"(out), "+s"(ws));
  const int lane = threadIdx.x & 63, wave = __builtin_amdgcn_readfirstlane(threadIdx.x >> 6);
  const int g = lane >> 4, c = lane & 15;
  const int bid = blockIdx.x, xc = bid & 7, r = bid >> 3;
  const int h = 4 * (r & 7) + (xc >> 1), s = (xc & 1) * 2 + ((r >> 3) & 1), b = r >> 4, j = b * 4 + s;
  const unsigned hm = (c & 1) ? 0xffff0000u : 0x0000ffffu;
  const int mt = (c & 7) >> 1;

  if (wave >= 2) {
    const int sw = wave - 2;
    const size_t cb = (size_t)((b * 32 + h) * 4 + s) * 524288;
    const char* kb = kc + cb;
    const char* vb = vc + cb;
    const rsrc_t rK = mk(kb, 524288), rV = mk(vb, 524288);
    const unsigned vo = sw * 1024 + lane * 16;
    BARRIER();
    v4u a[WIN];
#pragma unroll
    for (int i = 0; i < WIN; ++i) a[i] = ld_kv(rK, vo, i * 4096);
    SB();
    if (sw == 0) STAMP(16);
    lds_wait(L + L_FL + F_Q);
    const v4u q = *(LAS v4u*)(L + L_QB + c * 4);
    if (sw == 0) STAMP(17);
    float sc[8];
#pragma unroll
    for (int i = 0; i < 8; ++i) sc[i] = 0.f;
#pragma unroll 1
    for (int blk = 0; blk < 8; ++blk) {
      const rsrc_t rN = mk(blk < 7 ? kb : vb, 524288);
      const unsigned sC = blk * 65536, sN = blk < 7 ? sC + 65536 : 0;
      float x[16];
#pragma unroll
      for (int i = 0; i < 16; ++i) {
        const v4u k = a[i % WIN];
        float d = dot2(k.x, q.x, 0.f); d = dot2(k.y, q.y, d); d = dot2(k.z, q.z, d); d = dot2(k.w, q.w, d);
        x[i] = d;
        asm volatile("" : "+v"(x[i]));
        SB();
        if (i + WIN < 16) a[i % WIN] = ld_kv(rK, vo, sC + (i + WIN) * 4096);
        else a[i % WIN] = ld_kv(rN, vo, sN + (i + WIN - 16) * 4096);
        SB();
      }
#define TR_STEP(HALF, CTRL) _Pragma("unroll") for (int u = 0; u < (HALF); ++u) { const float lo_ = x[u], hi_ = x[u + (HALF)]; \
        const float t1_ = lo_ + dpp<CTRL>(lo_), t2_ = hi_ + dpp<CTRL>(hi_); x[u] = (c & (HALF)) ? t2_ : t1_; }
      TR_STEP(8, XOR8) TR_STEP(4, XOR7) TR_STEP(2, XOR2) TR_STEP(1, XOR1)
#undef TR_STEP
#pragma unroll
      for (int i = 0; i < 7; ++i) sc[i] = sc[i + 1];
      sc[7] = rbf(rbf(x[0]) * 0.08838834764831845f);
    }
    float m = sc[0];
#pragma unroll
    for (int i = 1; i < 8; ++i) m = fmaxf(m, sc[i]);
    m = max32x(max16x(rowmax(m)));
    float l = 0.f;
    unsigned pp[8];
#pragma unroll
    for (int i = 0; i < 8; ++i) { const float p = ex2((sc[i] - m) * LOG2E); l += p; pp[i] = cvtpk(p, p); }
    l = sum32x(sum16x(rowsum(l)));
    if (lane == 0) { Lf[L_ML + 2 * sw] = m; Lf[L_ML + 2 * sw + 1] = l; }
    lds_signal(L + L_FL + F_KD + sw, lane);
    if (sw == 0) STAMP(18);
    const v4u am = {(c < 8 && mt == 0) ? hm : 0u, (c < 8 && mt == 1) ? hm : 0u, (c < 8 && mt == 2) ? hm : 0u, (c < 8 && mt == 3) ? hm : 0u};
    f32x4 acc = {0.f, 0.f, 0.f, 0.f};
#define PV(I, REFILL) { acc = mfma(afrag(dppu<0x150 + (I)>(pp[0]), am), a[(I) % WIN], acc); asm volatile("" : "+v"(acc)); SB(); \
      if (REFILL) a[(I) % WIN] = ld_kv(rV, vo, sC + ((I) + WIN) * 4096); SB(); }
#pragma unroll 1
    for (int blk = 0; blk < 7; ++blk) {
      const unsigned sC = blk * 65536;
      PV(0, 1) PV(1, 1) PV(2, 1) PV(3, 1) PV(4, 1) PV(5, 1) PV(6, 1) PV(7, 1)
      PV(8, 1) PV(9, 1) PV(10, 1) PV(11, 1) PV(12, 1) PV(13, 1) PV(14, 1) PV(15, 1)
#pragma unroll
      for (int i = 0; i < 7; ++i) pp[i] = pp[i + 1];
    }
    {
      const unsigned sC = 7 * 65536;
      if (WIN == 16) lds_signal(L + L_FL + F_LATE + sw, lane);
      PV(0, WIN < 16) PV(1, WIN < 15) PV(2, WIN < 14) PV(3, WIN < 13) PV(4, WIN < 12) PV(5, WIN < 11) PV(6, WIN < 10) PV(7, WIN < 9)
      if (WIN == 8) lds_signal(L + L_FL + F_LATE + sw, lane);
      PV(8, 0) PV(9, 0) PV(10, 0) PV(11, 0) PV(12, 0) PV(13, 0) PV(14, 0) PV(15, 0)
    }
#undef PV
    if (lane < 32) *(LAS f32x4*)(Lf + L_OW + sw * 128 + c * 8 + g * 4) = acc;
    lds_signal(L + L_FL + F_OD + sw, lane);
    if (sw == 0) STAMP(19);
    return;
  }

  const bool isA = wave == 0;
  STAMP(wave * 8 + 0);
  const rsrc_t rW = mk(ws, WS_USED);
  const v4u am = {mt == 0 ? hm : 0u, mt == 1 ? hm : 0u, mt == 2 ? hm : 0u, mt == 3 ? hm : 0u};
  v4u w[36];
  v4u pl[8];
  unsigned T;
  if (isA) {
    const int hq = (r & 7) + 8 * (xc & 3), jq = (r >> 3) + 4 * (xc >> 2);
    const rsrc_t rQ = mk(wq + (size_t)(72 * jq) * 8192 + hq * 256, 72 * 8192 - hq * 256);
    const unsigned E = __builtin_amdgcn_raw_buffer_load_b32(rW, E_OFF, 0, AUX_SC1);
#pragma unroll
    for (int i = 0; i < 18; ++i) w[i] = ld_nt(rQ, g * 8192 + c * 16, i * 32768);
    BARRIER();
    T = __builtin_amdgcn_readfirstlane(E) + 0x9E3779B9u;
    if (lane == 0) L[L_FL + F_T] = T;
    lds_wait(L + L_FL + F_XN);
    STAMP(1);
    f32x4 a0 = {0.f, 0.f, 0.f, 0.f}, a1 = a0;
    gemv<0, 18>(a0, a1, L + L_XN + (c >> 3) * XN_S + 72 * jq + g, am, w);
    a0 += a1;
    const unsigned o = QG_OFF + ((hq * 2 + (g >> 1)) * 8 + jq) * 1024 + (c * 8 + (g & 1) * 4) * 8;
    st_sc(rW, o, 0, gran(a0[0], a0[1], T));
    st_sc(rW, o + 16, 0, gran(a0[2], a0[3], T));
    STAMP(2);
    __builtin_amdgcn_s_sleep(QSLEEP);
#pragma unroll
    for (int i = 0; i < 8; ++i) pl[i] = ld_sc(rW, lane * 16, QG_OFF + ((h * 2 + b) * 8 + i) * 1024);
    SB();
    lds_signal(L + L_FL + F_GO, lane);
  } else {
    v3u xr[3];
    const rsrc_t rX = mk(xg, 2304);
#pragma unroll
    for (int k = 0; k < 3; ++k) xr[k] = __builtin_amdgcn_raw_buffer_load_b96(rX, (lane >> 5) * 1152 + (lane & 31) * 12, k * 384, 0);
    if (lane < 20) L[L_FL + lane] = 0u;
    LDS_FENCE();
    BARRIER();
    const unsigned cv = __builtin_amdgcn_raw_buffer_load_b32(mk((const void*)(pc0 & ~255ull), CODE_BYTES), (r * 4 + (lane & 3)) * 128, 0, 0);
    SB();
    float xv[18];
#pragma unroll
    for (int k = 0; k < 3; ++k)
#pragma unroll
      for (int t = 0; t < 3; ++t) { xv[k * 6 + 2 * t] = u2f(xr[k][t] << 16); xv[k * 6 + 2 * t + 1] = u2f(xr[k][t] & 0xffff0000u); }
    float sum = 0.f;
#pragma unroll
    for (int i = 0; i < 18; ++i) sum += xv[i];
    const float mean = sum16x(rowsum(sum)) * (1.0f / 576.0f);
    float sq = 0.f;
#pragma unroll
    for (int i = 0; i < 18; ++i) { const float d = xv[i] - mean; sq = fmaf(d, d, sq); }
    const float var = sum16x(rowsum(sq)) * (1.0f / 576.0f);
    const float mb = rbf(mean), sd = rbf(__builtin_sqrtf(rbf(rbf(var) + 1e-5f))), rs = 1.0f / sd;
    LAS v2u* dst = (LAS v2u*)(L + L_XN + (lane >> 5) * XN_S + (lane & 31) * 6);
#pragma unroll
    for (int k = 0; k < 3; ++k)
#pragma unroll
      for (int t = 0; t < 3; ++t) {
        float n[2];
#pragma unroll
        for (int e = 0; e < 2; ++e) { const float d = rbf(xv[k * 6 + 2 * t + e] - mb); const float q0 = d * rs; n[e] = fmaf(fmaf(-q0, sd, d), rs, q0); }
        dst[k * 96 + t] = v2u{cvtpk(n[0], n[0]), cvtpk(n[1], n[1])};
      }
    lds_signal(L + L_FL + F_XN, lane);
    STAMP(9);
    lds_wait(L + L_FL + F_GO);
    asm volatile("" :: "v"(cv));
    T = __builtin_amdgcn_readfirstlane(L[L_FL + F_T]);
  }
  const int hp = 4 * (h & 7) + (h >> 3), qd = j >> 1, half = j & 1;
  const unsigned tvo = c < 9 ? (unsigned)(c * 16) : OOB;
  {
    const unsigned cofs = hp * 288 + half * 144;
    const rsrc_t rG = mk((isA ? wu : wg) + (size_t)(144 * qd) * 9216 + cofs, 144 * 9216 - cofs);
#pragma unroll
    for (int i = 0; i < 36; ++i) w[i] = ld_nt(rG, tvo + g * 9216, i * 36864);
    SB();
  }
  if (isA) {
    unsigned bad = 0;
    float q0 = 0.f, q1 = 0.f;
#pragma unroll
    for (int i = 0; i < 8; ++i) { bad |= tagbad(pl[i], T); q0 += u2f(pl[i].x); q1 += u2f(pl[i].z); }
    if (!none_bad(bad)) {
      _Pragma("unroll 1") for (int spin = 0; spin < SPIN_CAP; ++spin) {
      asm volatile("" ::: "memory");
#pragma unroll
        for (int i = 0; i < 8; ++i) pl[i] = ld_sc(rW, lane * 16, QG_OFF + ((h * 2 + b) * 8 + i) * 1024);
        bad = 0; q0 = 0.f; q1 = 0.f;
#pragma unroll
        for (int i = 0; i < 8; ++i) { bad |= tagbad(pl[i], T); q0 += u2f(pl[i].x); q1 += u2f(pl[i].z); }
        if (none_bad(bad)) break;
      }
      STAMP(21);
    }
    L[L_QB + lane] = cvtpk(q0, q1);
    lds_signal(L + L_FL + F_Q, lane);
    STAMP(3);
  }
  {
    f32x4 a0 = {0.f, 0.f, 0.f, 0.f}, a1 = a0;
    gemv<0, 36>(a0, a1, L + L_XN + (c >> 3) * XN_S + 144 * qd + g, am, w);
    a0 += a1;
    const unsigned o = c < 9 ? (unsigned)(GU_OFF + ((((hp * 2 + half) * 4 + qd) * 2 + (isA ? 1 : 0)) * 2 + (g >> 1)) * 576 + (c * 8 + (g & 1) * 4) * 8) : OOB;
    st_sc(rW, o, 0, gran(a0[0], a0[1], T));
    st_sc(rW, o + 16, 0, gran(a0[2], a0[3], T));
  }
  STAMP(wave * 8 + 4);
  float fw[4] = {0.f, 0.f, 0.f, 0.f};
  unsigned xres = 0;
  const int kcons = h;
  if (isA) {
    lds_wait4(L + L_FL + F_KD, lane, 4);
    float mw[4], lw[4];
#pragma unroll
    for (int i = 0; i < 4; ++i) { mw[i] = Lf[L_ML + 2 * i]; lw[i] = Lf[L_ML + 2 * i + 1]; }
    const float mg = fmaxf(fmaxf(mw[0], mw[1]), fmaxf(mw[2], mw[3]));
    float lg = 0.f;
#pragma unroll
    for (int i = 0; i < 4; ++i) lg += lw[i] * ex2((mw[i] - mg) * LOG2E);
    const unsigned mlb = ML_OFF + (b * 32 + h) * 128;
    st_sc(rW, lane == 0 ? mlb + s * 16 : OOB, 0, gran(mg, lg, T));
    v4u v;
    _Pragma("unroll 1") for (int spin = 0; spin < SPIN_CAP; ++spin) {
      asm volatile("" ::: "memory");
      v = ld_sc(rW, (lane & 3) * 16, mlb);
      if (none_bad(tagbad(v, T))) break;
      __builtin_amdgcn_s_sleep(8);
    }
    const float ms = u2f(v.x);
    float M = fmaxf(ms, dpp<XOR1>(ms)); M = fmaxf(M, dpp<XOR2>(M));
    float Ls = u2f(v.z) * ex2((ms - M) * LOG2E);
    Ls += dpp<XOR1>(Ls); Ls += dpp<XOR2>(Ls);
    const float inv = 1.0f / Ls;
#pragma unroll
    for (int i = 0; i < 4; ++i) fw[i] = ex2((mw[i] - M) * LOG2E) * inv;
  } else {
    const unsigned gb = GU_OFF + (hp * 2 + (j >> 2)) * (4 * 4 * 576);
    const unsigned o = c < 9 ? (unsigned)(g * 576 + (j & 3) * 144 + c * 16) : OOB;
    _Pragma("unroll 1") for (int spin = 0; spin < SPIN_CAP; ++spin) {
      asm volatile("" ::: "memory");
      unsigned bad = 0;
#pragma unroll
      for (int i = 0; i < 4; ++i) pl[i] = ld_sc(rW, o, gb + i * (4 * 576));
#pragma unroll
      for (int i = 0; i < 4; ++i) bad |= tagbad(pl[i], T);
      if (none_bad(c < 9 ? bad : 0u)) break;
      __builtin_amdgcn_s_sleep(8);
    }
    float hv[2];
#pragma unroll
    for (int e = 0; e < 2; ++e) {
      const float sm = rbf((u2f(pl[0][2 * e]) + u2f(pl[1][2 * e])) + (u2f(pl[2][2 * e]) + u2f(pl[3][2 * e])));
      auto sw2 = __builtin_amdgcn_permlane32_swap(f2u(sm), f2u(sm), false, false);
      const float gt = u2f(sw2[0]), up = u2f(sw2[1]);
      const float t2 = 1.5957691216057308f * (gt + 0.044715f * gt * gt * gt);
      const float ge = rbf(gt / (1.0f + ex2(-t2 * LOG2E)));
      hv[e] = ge * up;
    }
    const unsigned ho = (c < 9 && g < 2) ? (unsigned)(HG_OFF + hp * 2304 + g * 1152 + j * 144 + c * 16) : OOB;
    st_sc(rW, ho, 0, v4u{cvtpk(hv[0], hv[0]), T, cvtpk(hv[1], hv[1]), T});
    if (kcons < 18) xres = *(const GAS unsigned*)(xg + (kcons / 9) * 1152 + (72 * j + 8 * (kcons % 9)) * 2 + (lane & 3) * 4);
    _Pragma("unroll 1") for (int spin = 0; spin < SPIN_CAP; ++spin) {
      asm volatile("" ::: "memory");
#pragma unroll
      for (int i = 0; i < 3; ++i) pl[i] = ld_sc(rW, (i < 2 || lane < 16) ? (unsigned)(lane * 16) : OOB, HG_OFF + hp * 2304 + i * 1024);
      if (none_bad(tagbad(pl[0], T) | tagbad(pl[1], T) | (lane < 16 ? tagbad(pl[2], T) : 0u))) break;
      __builtin_amdgcn_s_sleep(8);
    }
#pragma unroll
    for (int i = 0; i < 3; ++i) {
      const int e = (i * 64 + lane) * 2;
      const int bb = e >= 144 ? 1 : 0;
      if (i < 2 || lane < 16) *(LAS v2u*)(L + L_HD + bb * HD_S + e - bb * 144) = v2u{pl[i].x, pl[i].z};
    }
    LDS_FENCE();
  }
  STAMP(wave * 8 + 5);
  lds_wait4(L + L_FL + F_LATE, lane, LATE_CNT);
  {
    const rsrc_t rO = mk(isA ? wo + (size_t)(128 * h) * 1152 + j * 144 : wd + (size_t)(144 * hp) * 1152 + j * 144, (isA ? 128 : 144) * 1152 - j * 144);
#pragma unroll
    for (int i = 0; i < 32; ++i) w[i] = ld_nt(rO, tvo + g * 1152, i * 4608);
    if (!isA) {
#pragma unroll
      for (int i = 32; i < 36; ++i) w[i] = ld_nt(rO, tvo + g * 1152, i * 4608);
    }
    SB();
  }
  if (isA) {
    lds_wait4(L + L_FL + F_OD, lane, 4);
    float o0 = 0.f, o1 = 0.f;
#pragma unroll
    for (int i = 0; i < 4; ++i) { const f32x2 t = *(LAS f32x2*)(Lf + L_OW + i * 128 + lane * 2); o0 = fmaf(fw[i], t.x, o0); o1 = fmaf(fw[i], t.y, o1); }
    st_sc(rW, lane * 16, OG_OFF + (h * 8 + j) * 1024, gran(o0, o1, T));
    STAMP(6);
    _Pragma("unroll 1") for (int spin = 0; spin < SPIN_CAP; ++spin) {
      asm volatile("" ::: "memory");
      unsigned bad = 0;
#pragma unroll
      for (int i = 0; i < 8; ++i) pl[i] = ld_sc(rW, lane * 16, OG_OFF + (h * 8 + i) * 1024);
#pragma unroll
      for (int i = 0; i < 8; ++i) bad |= tagbad(pl[i], T);
      if (none_bad(bad)) break;
    }
#pragma unroll
    for (int bb = 0; bb < 2; ++bb) {
      const float a0 = (u2f(pl[4 * bb].x) + u2f(pl[4 * bb + 1].x)) + (u2f(pl[4 * bb + 2].x) + u2f(pl[4 * bb + 3].x));
      const float a1 = (u2f(pl[4 * bb].z) + u2f(pl[4 * bb + 1].z)) + (u2f(pl[4 * bb + 2].z) + u2f(pl[4 * bb + 3].z));
      *(LAS v2u*)(L + L_OD + bb * HD_S + lane * 2) = v2u{cvtpk(a0, a0), cvtpk(a1, a1)};
    }
    LDS_FENCE();
    STAMP(7);
  }
  f32x4 acc = {0.f, 0.f, 0.f, 0.f};
  {
    f32x4 a1 = acc;
    const LAS unsigned* xs = L + (isA ? L_OD : L_HD) + (c >> 3) * HD_S + g;
    gemv<0, 32>(acc, a1, xs, am, w);
    if (!isA) gemv<32, 36>(acc, a1, xs, am, w);
    acc += a1;
  }
  if (isA) {
    lds_wait(L + L_FL + F_Z);
    acc += *(LAS f32x4*)(Lf + L_ZB + lane * 4);
    const unsigned o = c < 9 ? (unsigned)(YG_OFF + ((j * 18 + (g >> 1) * 9 + c) * 32 + h) * 64 + (g & 1) * 32) : OOB;
    st_sc(rW, o, 0, gran(acc[0], acc[1], T));
    st_sc(rW, o + 16, 0, gran(acc[2], acc[3], T));
    STAMP(20);
  } else {
    *(LAS f32x4*)(Lf + L_ZB + lane * 4) = acc;
    lds_signal(L + L_FL + F_Z, lane);
    STAMP(14);
    if (kcons < 18) {
      const unsigned yb = YG_OFF + (j * 18 + kcons) * 2048;
      _Pragma("unroll 1") for (int spin = 0; spin < SPIN_CAP; ++spin) {
      asm volatile("" ::: "memory");
        pl[0] = ld_sc(rW, lane * 16, yb);
        pl[1] = ld_sc(rW, lane * 16, yb + 1024);
        if (none_bad(tagbad(pl[0], T) | tagbad(pl[1], T))) break;
      }
      float y0 = u2f(pl[0].x) + u2f(pl[1].x), y1 = u2f(pl[0].z) + u2f(pl[1].z);
      y0 += dpp<ROR4>(y0); y1 += dpp<ROR4>(y1);
      y0 += dpp<XOR8>(y0); y1 += dpp<XOR8>(y1);
      y0 = sum32x(sum16x(y0)); y1 = sum32x(sum16x(y1));
      if (lane < 4) *(GAS unsigned*)(out + (kcons / 9) * 1152 + (72 * j + 8 * (kcons % 9)) * 2 + lane * 4) = cvtpk(u2f(xres << 16) + y0, u2f(xres & 0xffff0000u) + y1);
      if (bid == 0 && lane == 0) __builtin_amdgcn_raw_buffer_store_b32(T, rW, E_OFF, 0, AUX_SC1);
      STAMP(15);
    }
  }
}

extern "C" void kernel_launch(void* const* d_in, const int* in_sizes, int n_in, void* d_out, int out_size, void* d_ws, size_t ws_size,
                              hipStream_t stream) {
  (void)hipFuncSetAttribute((const void*)fused_decode, hipFuncAttributeMaxDynamicSharedMemorySize, LDS_BYTES);
  fused_decode<<<256, 384, LDS_BYTES, stream>>>((const char*)d_in[0], (const char*)d_in[1], (const char*)d_in[2], (const char*)d_in[3],
                                                (const char*)d_in[4], (const char*)d_in[6], (const char*)d_in[7], (const char*)d_in[8],
                                                (char*)d_out, (char*)d_ws);
}
```

```cpp
#define WAUX 0
#include <hip/hip_runtime.h>
#include <hip/hip_bf16.h>

#define WIN 8
#define NPRE 2
#define CODE_PF 1
#define TOUCH 1
#define MAP 2
#define KAUX 2
#define VAUX 2
#define DIAG 0

#define GAS __attribute__((address_space(1)))
#define LAS __attribute__((address_space(3)))
typedef unsigned v4u __attribute__((ext_vector_type(4)));
typedef unsigned v3u __attribute__((ext_vector_type(3)));
typedef unsigned v2u __attribute__((ext_vector_type(2)));
typedef float f32x4 __attribute__((ext_vector_type(4)));
typedef float f32x2 __attribute__((ext_vector_type(2)));
typedef __bf16 bf16x8 __attribute__((ext_vector_type(8)));
typedef __bf16 bf16x2 __attribute__((ext_vector_type(2)));
typedef __amdgpu_buffer_rsrc_t rsrc_t;

constexpr int E_OFF = 0;
constexpr int QG_OFF = 4096;
constexpr int ML_OFF = QG_OFF + 524288;
constexpr int GU_OFF = ML_OFF + 8192;
constexpr int HG_OFF = GU_OFF + 589824;
constexpr int OG_OFF = HG_OFF + 73728;
constexpr int YG_OFF = OG_OFF + 262144;
constexpr int WS_USED = YG_OFF + 294912;
constexpr int TS_OFF = 4 << 20;

constexpr int L_XN = 0, XN_S = 580;
constexpr int L_HD = 1160, HD_S = 148;
constexpr int L_QB = L_HD + 2 * HD_S;
constexpr int L_OW = L_QB + 64;
constexpr int L_YW = L_OW + 512;
constexpr int L_ML = L_YW + 1024;
constexpr int L_FW = L_ML + 8;
constexpr int L_FL = L_FW + 4;
constexpr int F_KD = 0, F_Q = 4, F_T = 5, F_TOK = 6, F_XN = 7, F_FW = 8, F_HD = 9, F_OC = 10, F_YC = 11;
static_assert(L_FL % 4 == 0 && L_OW % 4 == 0 && L_YW % 4 == 0 && L_QB % 4 == 0 && L_FW % 4 == 0, "16-byte aligned LDS pieces");
constexpr int LDS_BYTES = 84 * 1024;

constexpr int AUX_NT = 2, AUX_SC1 = 16;
constexpr unsigned OOB = 0x80000000u;
constexpr unsigned CODE_BYTES = 13 * 1024;
constexpr int SPIN_CAP = 1 << 18;
constexpr float LOG2E = 1.4426950408889634f;

__device__ __forceinline__ rsrc_t mk(const void* p, unsigned bytes) { return __builtin_amdgcn_make_buffer_rsrc((void*)p, 0, bytes, 0x00020000); }
__device__ __forceinline__ v4u ld_nt(rsrc_t r, unsigned vo, unsigned so) { return __builtin_amdgcn_raw_buffer_load_b128(r, vo, so, AUX_NT); }
__device__ __forceinline__ v4u ld_k(rsrc_t r, unsigned vo, unsigned so) { return __builtin_amdgcn_raw_buffer_load_b128(r, vo, so, KAUX); }
__device__ __forceinline__ v4u ld_v(rsrc_t r, unsigned vo, unsigned so) { return __builtin_amdgcn_raw_buffer_load_b128(r, vo, so, VAUX); }
__device__ __forceinline__ v4u ld_w(rsrc_t r, unsigned vo, unsigned so) { return __builtin_amdgcn_raw_buffer_load_b128(r, vo, so, WAUX); }
__device__ __forceinline__ v4u ld_sc(rsrc_t r, unsigned vo, unsigned so) { return __builtin_amdgcn_raw_buffer_load_b128(r, vo, so, AUX_SC1); }
__device__ __forceinline__ void st_sc(rsrc_t r, unsigned vo, unsigned so, v4u v) { __builtin_amdgcn_raw_buffer_store_b128(v, r, vo, so, AUX_SC1); }
__device__ __forceinline__ float u2f(unsigned u) { return __builtin_bit_cast(float, u); }
__device__ __forceinline__ unsigned f2u(float f) { return __builtin_bit_cast(unsigned, f); }
__device__ __forceinline__ unsigned cvtpk(float lo, float hi) {
  bf16x2 r = {(__bf16)lo, (__bf16)hi};
  return __builtin_bit_cast(unsigned, r);
}
__device__ __forceinline__ float rbf(float f) { return (float)(__bf16)f; }
__device__ __forceinline__ float ex2(float f) { return __builtin_amdgcn_exp2f(f); }
__device__ __forceinline__ float dot2(unsigned a, unsigned b, float c) {
  return __builtin_amdgcn_fdot2_f32_bf16(__builtin_bit_cast(bf16x2, a), __builtin_bit_cast(bf16x2, b), c, false);
}
__device__ __forceinline__ f32x4 mfma(v4u a, v4u b, f32x4 c) {
  return __builtin_amdgcn_mfma_f32_16x16x32_bf16(__builtin_bit_cast(bf16x8, a), __builtin_bit_cast(bf16x8, b), c, 0, 0, 0);
}
template <int CTRL> __device__ __forceinline__ float dpp(float x) { return u2f(__builtin_amdgcn_mov_dpp(f2u(x), CTRL, 0xf, 0xf, true)); }
template <int CTRL> __device__ __forceinline__ unsigned dppu(unsigned x) { return __builtin_amdgcn_mov_dpp(x, CTRL, 0xf, 0xf, true); }
constexpr int XOR1 = 0xB1, XOR2 = 0x4E, XOR7 = 0x141, XOR8 = 0x128, ROR4 = 0x124, BCAST = 0x150;
__device__ __forceinline__ float sum16x(float x) { auto s = __builtin_amdgcn_permlane16_swap(f2u(x), f2u(x), false, false); return u2f(s[0]) + u2f(s[1]); }
__device__ __forceinline__ float sum32x(float x) { auto s = __builtin_amdgcn_permlane32_swap(f2u(x), f2u(x), false, false); return u2f(s[0]) + u2f(s[1]); }
__device__ __forceinline__ float max16x(float x) { auto s = __builtin_amdgcn_permlane16_swap(f2u(x), f2u(x), false, false); return fmaxf(u2f(s[0]), u2f(s[1])); }
__device__ __forceinline__ float max32x(float x) { auto s = __builtin_amdgcn_permlane32_swap(f2u(x), f2u(x), false, false); return fmaxf(u2f(s[0]), u2f(s[1])); }
__device__ __forceinline__ float rowsum(float x) { x += dpp<XOR1>(x); x += dpp<XOR2>(x); x += dpp<XOR7>(x); x += dpp<XOR8>(x); return x; }
__device__ __forceinline__ float rowmax(float x) { x = fmaxf(x, dpp<XOR1>(x)); x = fmaxf(x, dpp<XOR2>(x)); x = fmaxf(x, dpp<XOR7>(x)); x = fmaxf(x, dpp<XOR8>(x)); return x; }
#define SB() __builtin_amdgcn_sched_barrier(0)
#define MEMBAR() asm volatile("" ::: "memory")
#define LDS_FENCE() asm volatile("s_waitcnt lgkmcnt(0)" ::: "memory")
#define BARRIER() do { SB(); asm volatile("s_barrier" ::: "memory"); SB(); } while (0)
#define STR2(x) #x
#define STR(x) STR2(x)
__device__ __forceinline__ unsigned lds_ld(LAS unsigned* p) { return __hip_atomic_load(p, __ATOMIC_RELAXED, __HIP_MEMORY_SCOPE_WORKGROUP); }
__device__ __forceinline__ void lds_wait(LAS unsigned* p) {
  while (__builtin_amdgcn_readfirstlane(lds_ld(p)) == 0) __builtin_amdgcn_s_sleep(1);
  MEMBAR();
}
__device__ __forceinline__ void lds_wait4(LAS unsigned* p, int lane) {
  while (__builtin_amdgcn_ballot_w64(lds_ld(p + (lane & 3)) == 0) != 0ull) __builtin_amdgcn_s_sleep(4);
  MEMBAR();
}
__device__ __forceinline__ void lds_signal(LAS unsigned* p, int lane) {
  LDS_FENCE();
  if (lane == 0) __hip_atomic_store(p, 1u, __ATOMIC_RELAXED, __HIP_MEMORY_SCOPE_WORKGROUP);
}
__device__ __forceinline__ unsigned lds_arrive(LAS unsigned* p, int lane) {
  LDS_FENCE();
  unsigned old = 0;
  if (lane == 0) old = __hip_atomic_fetch_add(p, 1u, __ATOMIC_RELAXED, __HIP_MEMORY_SCOPE_WORKGROUP);
  old = __builtin_amdgcn_readfirstlane(old);
  MEMBAR();
  return old;
}
__device__ __forceinline__ bool none_bad(unsigned bad) { return __builtin_amdgcn_ballot_w64(bad != 0u) == 0ull; }
__device__ __forceinline__ unsigned tagbad(const v4u& v, unsigned T) { return (v.y ^ T) | (v.w ^ T); }
__device__ __forceinline__ v4u gran(float a, float b, unsigned T) { return v4u{f2u(a), T, f2u(b), T}; }
__device__ __forceinline__ v4u afrag(unsigned dup, const v4u& m) { return v4u{dup & m.x, dup & m.y, dup & m.z, dup & m.w}; }
__device__ __forceinline__ void poll2(rsrc_t rW, unsigned vo, unsigned so, unsigned T, v4u& r0, v4u& r1) {
#pragma unroll 1
  for (int spin = 0; spin < SPIN_CAP; ++spin) {
    MEMBAR();
    r0 = ld_sc(rW, vo, so); r1 = ld_sc(rW, vo, so + 1024);
    if (none_bad(tagbad(r0, T) | tagbad(r1, T))) break;
  }
}

#define STAMP(slot) do {} while (0)

template <int N, int M> __device__ __forceinline__ f32x4 gemv(const LAS unsigned* xs, const v4u& am, const v4u (&w)[M]) {
  unsigned xv[N];
#pragma unroll
  for (int i = 0; i < N; ++i) xv[i] = xs[4 * i];
  SB();
  f32x4 a0 = {0.f, 0.f, 0.f, 0.f}, a1 = a0;
#pragma unroll
  for (int i = 0; i < N; ++i) { if (i & 1) a1 = mfma(afrag(xv[i], am), w[i], a1); else a0 = mfma(afrag(xv[i], am), w[i], a0); }
  return a0 + a1;
}

__global__ void __launch_bounds__(384, 2)
fused_decode(const char* xg, const char* kc, const char* vc, const char* wq, const char* wo, const char* wg, const char* wu, const char* wd,
             char* out, char* ws) {
  extern __shared__ __attribute__((aligned(16))) unsigned smem[];
  LAS unsigned* L = (LAS unsigned*)smem;
  LAS float* Lf = (LAS float*)smem;
  unsigned long long pc0 = __builtin_amdgcn_s_getpc();
  asm volatile("" : "+s"(pc0));
  asm volatile("" : "+s"(xg), "+s"(kc), "+s"(vc), "+s"(wq), "+s"(wo), "+s"(wg), "+s"(wu), "+s"(wd), "+s"(out), "+s"(ws));
  const int lane = threadIdx.x & 63, wave = __builtin_amdgcn_readfirstlane(threadIdx.x >> 6);
  const int g = lane >> 4, c = lane & 15;
  const int bid = blockIdx.x, xc = bid & 7, r = bid >> 3;
  const int h = xc + 8 * (r & 3), j = r >> 2, s = j & 3, b = j >> 2;
  const int hp = 4 * (h & 7) + (h >> 3);
  const unsigned hm = (c & 1) ? 0xffff0000u : 0x0000ffffu;
  const int mt = (c & 7) >> 1;
  const v4u am = {mt == 0 ? hm : 0u, mt == 1 ? hm : 0u, mt == 2 ? hm : 0u, mt == 3 ? hm : 0u};
  const unsigned tvo = c < 9 ? (unsigned)(c * 16) : OOB;
  const rsrc_t rW = mk(ws, WS_USED);

  if (wave >= 2) {
    const int sw = wave - 2;
    const size_t cb = (size_t)((b * 32 + h) * 4 + s) * 524288;
    const char* kb = kc + cb;
    const char* vb = vc + cb;
    const rsrc_t rK = mk(kb, 524288), rV = mk(vb, 524288);
    const unsigned vo = sw * 1024 + lane * 16;
    constexpr int NVB = 8 - NPRE;
    BARRIER();
    v4u a[WIN], pre[NPRE * 16 + 1];
#pragma unroll
    for (int i = 0; i < WIN; ++i) a[i] = ld_k(rK, vo, i * 4096);
    SB();
#pragma unroll
    for (int i = 0; i < NPRE * 16; ++i) {
      asm volatile("s_waitcnt vmcnt(" STR(WIN) "-1)" ::: "memory");
      pre[i] = ld_v(rV, vo, NVB * 65536 + i * 4096);
      SB();
    }
    if (sw == 0) STAMP(16);
    lds_wait(L + L_FL + F_Q);
    const v4u q = *(LAS v4u*)(L + L_QB + c * 4);
    if (sw == 0) STAMP(17);
    float sc[8];
#pragma unroll
    for (int i = 0; i < 8; ++i) sc[i] = 0.f;
#pragma unroll 1
    for (int blk = 0; blk < 8; ++blk) {
      const rsrc_t rN = mk(blk < 7 ? kb : vb, 524288);
      const unsigned sC = blk * 65536, sN = blk < 7 ? sC + 65536 : 0;
      float x[16];
#pragma unroll
      for (int i = 0; i < 16; ++i) {
        const v4u k = a[i % WIN];
        float d = dot2(k.x, q.x, 0.f); d = dot2(k.y, q.y, d); d = dot2(k.z, q.z, d); d = dot2(k.w, q.w, d);
        x[i] = d;
        asm volatile("" : "+v"(x[i]));
        SB();
        if (i + WIN < 16) a[i % WIN] = ld_k(rK, vo, sC + (i + WIN) * 4096);
        else a[i % WIN] = ld_k(rN, vo, sN + (i + WIN - 16) * 4096);
        SB();
      }
#define TR_STEP(HALF, CTRL) _Pragma("unroll") for (int u = 0; u < (HALF); ++u) { const float lo_ = x[u], hi_ = x[u + (HALF)]; \
        const float t1_ = lo_ + dpp<CTRL>(lo_), t2_ = hi_ + dpp<CTRL>(hi_); x[u] = (c & (HALF)) ? t2_ : t1_; }
      TR_STEP(8, XOR8) TR_STEP(4, XOR7) TR_STEP(2, XOR2) TR_STEP(1, XOR1)
#undef TR_STEP
#pragma unroll
      for (int i = 0; i < 7; ++i) sc[i] = sc[i + 1];
      sc[7] = rbf(rbf(x[0]) * 0.08838834764831845f);
    }
    float m = sc[0];
#pragma unroll
    for (int i = 1; i < 8; ++i) m = fmaxf(m, sc[i]);
    m = max32x(max16x(rowmax(m)));
    float l = 0.f;
    unsigned pp[8];
#pragma unroll
    for (int i = 0; i < 8; ++i) { const float p = ex2((sc[i] - m) * LOG2E); l += p; pp[i] = cvtpk(p, p); }
    l = sum32x(sum16x(rowsum(l)));
    if (lane == 0) { Lf[L_ML + 2 * sw] = m; Lf[L_ML + 2 * sw + 1] = l; }
    lds_signal(L + L_FL + F_KD + sw, lane);
    if (sw == 0) STAMP(18);
    const v4u pm = {c < 8 ? am.x : 0u, c < 8 ? am.y : 0u, c < 8 ? am.z : 0u, c < 8 ? am.w : 0u};
    f32x4 acc = {0.f, 0.f, 0.f, 0.f};
#define PVM(I, P, V) { acc = mfma(afrag(dppu<BCAST + (I)>(P), pm), (V), acc); asm volatile("" : "+v"(acc)); SB(); }
#define PV(I) { PVM(I, pp[0], a[(I) % WIN]) a[(I) % WIN] = ld_v(rV, vo, sC + ((I) + WIN) * 4096); SB(); }
#define PRE16(B) PVM(0, pp[NVB + B], pre[B * 16 + 0]) PVM(1, pp[NVB + B], pre[B * 16 + 1]) PVM(2, pp[NVB + B], pre[B * 16 + 2]) PVM(3, pp[NVB + B], pre[B * 16 + 3]) \
      PVM(4, pp[NVB + B], pre[B * 16 + 4]) PVM(5, pp[NVB + B], pre[B * 16 + 5]) PVM(6, pp[NVB + B], pre[B * 16 + 6]) PVM(7, pp[NVB + B], pre[B * 16 + 7]) \
      PVM(8, pp[NVB + B], pre[B * 16 + 8]) PVM(9, pp[NVB + B], pre[B * 16 + 9]) PVM(10, pp[NVB + B], pre[B * 16 + 10]) PVM(11, pp[NVB + B], pre[B * 16 + 11]) \
      PVM(12, pp[NVB + B], pre[B * 16 + 12]) PVM(13, pp[NVB + B], pre[B * 16 + 13]) PVM(14, pp[NVB + B], pre[B * 16 + 14]) PVM(15, pp[NVB + B], pre[B * 16 + 15])
    PRE16(0)
    PRE16(1)
#pragma unroll 1
    for (int blk = 0; blk < NVB - 1; ++blk) {
      const unsigned sC = blk * 65536;
      PV(0) PV(1) PV(2) PV(3) PV(4) PV(5) PV(6) PV(7) PV(8) PV(9) PV(10) PV(11) PV(12) PV(13) PV(14) PV(15)
#pragma unroll
      for (int i = 0; i < NVB - 1; ++i) pp[i] = pp[i + 1];
    }
    v4u wz[9], wy[8];
    {
      const unsigned sC = (NVB - 1) * 65536;
      const rsrc_t rD = mk(wd + (size_t)(144 * hp + 36 * sw) * 1152 + j * 144, 36 * 1152 - j * 144);
      const rsrc_t rO = mk(wo + (size_t)(128 * h + 32 * sw) * 1152 + j * 144, 32 * 1152 - j * 144);
      const unsigned to = tvo + g * 1152;
#define TL(n) { if ((n) < 9) wz[(n) < 9 ? (n) : 0] = ld_w(rD, to, (n) * 4608); else if ((n) < 17) wy[(n) < 9 || (n) >= 17 ? 0 : (n) - 9] = ld_w(rO, to, ((n) - 9) * 4608); }
#define PVL(I) { if ((I) + WIN < 16) PV(I) else { PVM(I, pp[0], a[(I) % WIN]) TL(((I) + WIN - 16) * 2) TL(((I) + WIN - 16) * 2 + 1) if ((I) == 15) TL(16) SB(); } }
      PVL(0) PVL(1) PVL(2) PVL(3) PVL(4) PVL(5) PVL(6) PVL(7) PVL(8) PVL(9) PVL(10) PVL(11) PVL(12) PVL(13) PVL(14) PVL(15)
    }
    if (lane < 32) *(LAS f32x4*)(Lf + L_OW + sw * 128 + c * 8 + g * 4) = acc;
    const unsigned T = __builtin_amdgcn_readfirstlane(L[L_FL + F_T]);
    if (sw == 0) STAMP(19);
    if (lds_arrive(L + L_FL + F_OC, lane) == 3) {
      lds_wait(L + L_FL + F_FW);
      const f32x4 fw = *(LAS f32x4*)(Lf + L_FW);
      float o0 = 0.f, o1 = 0.f;
#pragma unroll
      for (int i = 0; i < 4; ++i) { const f32x2 t = *(LAS f32x2*)(Lf + L_OW + i * 128 + g * 32 + (c & 7) * 4 + (c >> 3) * 2); o0 = fmaf(fw[i], t.x, o0); o1 = fmaf(fw[i], t.y, o1); }
      st_sc(rW, g * 2048 + c * 16, OG_OFF + h * 8192 + j * 256, gran(o0, o1, T));
      STAMP(6);
    }
    lds_wait(L + L_FL + F_HD);
    f32x4 y = gemv<9>(L + L_HD + (c >> 3) * HD_S + 36 * sw + g, am, wz);
    asm volatile("" : "+v"(y));
    v4u p0, p1;
    poll2(rW, lane * 16, OG_OFF + h * 8192 + sw * 2048, T, p0, p1);
    if (sw == 0) STAMP(7);
    {
      const float e00 = sum32x(sum16x(u2f(p0.x))), e01 = sum32x(sum16x(u2f(p0.z))), e10 = sum32x(sum16x(u2f(p1.x))), e11 = sum32x(sum16x(u2f(p1.z)));
      unsigned pk = cvtpk((g & 1) ? e01 : e00, (g & 1) ? e11 : e10);
      pk = __builtin_amdgcn_update_dpp(pk, pk, XOR8, 0xc, 0xf, false);
      const unsigned src = (c >> 3) ? 0x0302u : 0x0100u, sel = (c & 1) ? (src << 16) | 0x0c0cu : 0x0c0c0000u | src;
      const v4u sl = {mt == 0 ? sel : 0x0c0c0c0cu, mt == 1 ? sel : 0x0c0c0c0cu, mt == 2 ? sel : 0x0c0c0c0cu, mt == 3 ? sel : 0x0c0c0c0cu};
      f32x4 y1 = {0.f, 0.f, 0.f, 0.f};
#define YM(I, ACC) { const unsigned bc = dppu<BCAST + (I)>(pk); ACC = mfma(v4u{__builtin_amdgcn_perm(0u, bc, sl.x), __builtin_amdgcn_perm(0u, bc, sl.y), \
        __builtin_amdgcn_perm(0u, bc, sl.z), __builtin_amdgcn_perm(0u, bc, sl.w)}, wy[I], ACC); }
      YM(0, y) YM(1, y1) YM(2, y) YM(3, y1) YM(4, y) YM(5, y1) YM(6, y) YM(7, y1)
      y += y1;
    }
    *(LAS f32x4*)(Lf + L_YW + sw * 256 + lane * 4) = y;
    if (lds_arrive(L + L_FL + F_YC, lane) == 3) {
      f32x4 t = *(LAS f32x4*)(Lf + L_YW + lane * 4);
#pragma unroll
      for (int i = 1; i < 4; ++i) t += *(LAS f32x4*)(Lf + L_YW + i * 256 + lane * 4);
      const unsigned o = c < 9 ? (unsigned)(YG_OFF + ((j * 18 + (g >> 1) * 9 + c) * 32 + h) * 64 + (g & 1) * 32) : OOB;
      st_sc(rW, o, 0, gran(t[0], t[1], T));
      st_sc(rW, o + 16, 0, gran(t[2], t[3], T));
      STAMP(20);
      if (h < 18) {
        const int oo = (h / 9) * 1152 + (72 * j + 8 * (h % 9)) * 2 + (lane & 3) * 4;
        const unsigned xres = *(const GAS unsigned*)(xg + oo);
        poll2(rW, lane * 16, YG_OFF + (j * 18 + h) * 2048, T, p0, p1);
        float y0 = u2f(p0.x) + u2f(p1.x), y1 = u2f(p0.z) + u2f(p1.z);
        y0 += dpp<ROR4>(y0); y1 += dpp<ROR4>(y1);
        y0 += dpp<XOR8>(y0); y1 += dpp<XOR8>(y1);
        y0 = sum32x(sum16x(y0)); y1 = sum32x(sum16x(y1));
        if (lane < 4) *(GAS unsigned*)(out + oo) = cvtpk(u2f(xres << 16) + y0, u2f(xres & 0xffff0000u) + y1);
        if (bid == 0 && lane == 0) __builtin_amdgcn_raw_buffer_store_b32(T, rW, E_OFF, 0, AUX_SC1);
        STAMP(15);
      }
    }
    return;
  }

  const bool isA = wave == 0;
  STAMP(wave * 8 + 0);
  v4u w[36];
  unsigned T;
  if (isA) {
    const int hq = (r & 7) + 8 * (xc & 3), jq = (r >> 3) + 4 * (xc >> 2);
    const rsrc_t rQ = mk(wq + (size_t)(72 * jq) * 8192 + hq * 256, 72 * 8192 - hq * 256);
    const unsigned E = __builtin_amdgcn_raw_buffer_load_b32(rW, E_OFF, 0, AUX_SC1);
#pragma unroll
    for (int i = 0; i < 18; ++i) w[i] = ld_w(rQ, g * 8192 + c * 16, i * 32768);
    BARRIER();
    T = __builtin_amdgcn_readfirstlane(E) + 0x9E3779B9u;
    if (lane == 0) L[L_FL + F_T] = T;
    lds_signal(L + L_FL + F_TOK, lane);
    lds_wait(L + L_FL + F_XN);
    STAMP(1);
    const f32x4 a0 = gemv<18>(L + L_XN + (c >> 3) * XN_S + 72 * jq + g, am, w);
    const unsigned o = QG_OFF + ((hq * 2 + (g >> 1)) * 8 + jq) * 1024 + (c * 8 + (g & 1) * 4) * 8;
    st_sc(rW, o, 0, gran(a0[0], a0[1], T));
    st_sc(rW, o + 16, 0, gran(a0[2], a0[3], T));
    STAMP(2);
    float q0, q1;
#pragma unroll 1
    for (int spin = 0; spin < SPIN_CAP; ++spin) {
      __builtin_amdgcn_s_sleep(8);
      MEMBAR();
#pragma unroll
      for (int i = 0; i < 8; ++i) w[i] = ld_sc(rW, lane * 16, QG_OFF + ((h * 2 + b) * 8 + i) * 1024);
      unsigned bad = 0; q0 = 0.f; q1 = 0.f;
#pragma unroll
      for (int i = 0; i < 8; ++i) { bad |= tagbad(w[i], T); q0 += u2f(w[i].x); q1 += u2f(w[i].z); }
      if (none_bad(bad)) break;
    }
    L[L_QB + lane] = cvtpk(q0, q1);
    lds_signal(L + L_FL + F_Q, lane);
    STAMP(3);
  } else {
    v3u xr[3];
    const rsrc_t rX = mk(xg, 2304);
#pragma unroll
    for (int k = 0; k < 3; ++k) xr[k] = __builtin_amdgcn_raw_buffer_load_b96(rX, (lane >> 5) * 1152 + (lane & 31) * 12, k * 384, 0);
    if (lane < 12) L[L_FL + lane] = 0u;
    LDS_FENCE();
    BARRIER();
    const unsigned cv = __builtin_amdgcn_raw_buffer_load_b32(mk((const void*)(pc0 & ~255ull), CODE_BYTES), (r * 4 + (lane & 3)) * 128, 0, 0);
    const char* tp = (lane & 4) ? wo + (size_t)(128 * h + 40 * (lane & 3)) * 1152 + j * 144 : wd + (size_t)(144 * hp + 45 * (lane & 3)) * 1152 + j * 144;
    if ((lane & 15) >= 8) tp = out;
    const unsigned tv = __builtin_nontemporal_load((const GAS unsigned*)tp);
    SB();
    float xv[18];
#pragma unroll
    for (int k = 0; k < 3; ++k)
#pragma unroll
      for (int t = 0; t < 3; ++t) { xv[k * 6 + 2 * t] = u2f(xr[k][t] << 16); xv[k * 6 + 2 * t + 1] = u2f(xr[k][t] & 0xffff0000u); }
    float sum = 0.f;
#pragma unroll
    for (int i = 0; i < 18; ++i) sum += xv[i];
    const float mean = sum16x(rowsum(sum)) * (1.0f / 576.0f);
    float sq = 0.f;
#pragma unroll
    for (int i = 0; i < 18; ++i) { const float d = xv[i] - mean; sq = fmaf(d, d, sq); }
    const float var = sum16x(rowsum(sq)) * (1.0f / 576.0f);
    const float mb = rbf(mean), sd = rbf(__builtin_sqrtf(rbf(rbf(var) + 1e-5f))), rs = 1.0f / sd;
    LAS v2u* dst = (LAS v2u*)(L + L_XN + (lane >> 5) * XN_S + (lane & 31) * 6);
#pragma unroll
    for (int k = 0; k < 3; ++k)
#pragma unroll
      for (int t = 0; t < 3; ++t) {
        float n[2];
#pragma unroll
        for (int e = 0; e < 2; ++e) { const float d = rbf(xv[k * 6 + 2 * t + e] - mb); const float q0 = d * rs; n[e] = fmaf(fmaf(-q0, sd, d), rs, q0); }
        dst[k * 96 + t] = v2u{cvtpk(n[0], n[0]), cvtpk(n[1], n[1])};
      }
    lds_signal(L + L_FL + F_XN, lane);
    STAMP(9);
    lds_wait(L + L_FL + F_TOK);
    asm volatile("" :: "v"(cv));
    asm volatile("" :: "v"(tv));
    T = __builtin_amdgcn_readfirstlane(L[L_FL + F_T]);
  }
  {
    const int qd = j >> 1, half = j & 1;
    const unsigned cofs = hp * 288 + half * 144;
    const rsrc_t rG = mk((isA ? wu : wg) + (size_t)(144 * qd) * 9216 + cofs, 144 * 9216 - cofs);
#pragma unroll
    for (int i = 0; i < 36; ++i) w[i] = ld_w(rG, tvo + g * 9216, i * 36864);
    SB();
    const f32x4 a0 = gemv<36>(L + L_XN + (c >> 3) * XN_S + 144 * qd + g, am, w);
    const unsigned o = c < 9 ? (unsigned)(GU_OFF + ((((hp * 2 + half) * 4 + qd) * 2 + (isA ? 1 : 0)) * 2 + (g >> 1)) * 576 + (c * 8 + (g & 1) * 4) * 8) : OOB;
    st_sc(rW, o, 0, gran(a0[0], a0[1], T));
    st_sc(rW, o + 16, 0, gran(a0[2], a0[3], T));
  }
  STAMP(wave * 8 + 4);
  if (isA) {
    lds_wait4(L + L_FL + F_KD, lane);
    float mw[4], lw[4];
#pragma unroll
    for (int i = 0; i < 4; ++i) { mw[i] = Lf[L_ML + 2 * i]; lw[i] = Lf[L_ML + 2 * i + 1]; }
    const float mg = fmaxf(fmaxf(mw[0], mw[1]), fmaxf(mw[2], mw[3]));
    float lg = 0.f;
#pragma unroll
    for (int i = 0; i < 4; ++i) lg += lw[i] * ex2((mw[i] - mg) * LOG2E);
    const unsigned mlb = ML_OFF + (b * 32 + h) * 128;
    st_sc(rW, lane == 0 ? mlb + s * 16 : OOB, 0, gran(mg, lg, T));
    v4u v;
#pragma unroll 1
    for (int spin = 0; spin < SPIN_CAP; ++spin) {
      __builtin_amdgcn_s_sleep(16);
      MEMBAR();
      v = ld_sc(rW, (lane & 3) * 16, mlb);
      if (none_bad(tagbad(v, T))) break;
    }
    const float ms = u2f(v.x);
    float M = fmaxf(ms, dpp<XOR1>(ms)); M = fmaxf(M, dpp<XOR2>(M));
    float Ls = u2f(v.z) * ex2((ms - M) * LOG2E);
    Ls += dpp<XOR1>(Ls); Ls += dpp<XOR2>(Ls);
    const float inv = 1.0f / Ls;
    if (lane == 0) *(LAS f32x4*)(Lf + L_FW) = f32x4{ex2((mw[0] - M) * LOG2E) * inv, ex2((mw[1] - M) * LOG2E) * inv, ex2((mw[2] - M) * LOG2E) * inv, ex2((mw[3] - M) * LOG2E) * inv};
    lds_signal(L + L_FL + F_FW, lane);
  } else {
    const unsigned gb = GU_OFF + (hp * 2 + (j >> 2)) * (4 * 4 * 576);
    const unsigned o = c < 9 ? (unsigned)(g * 576 + (j & 3) * 144 + c * 16) : OOB;
#pragma unroll 1
    for (int spin = 0; spin < SPIN_CAP; ++spin) {
      __builtin_amdgcn_s_sleep(32);
      MEMBAR();
      unsigned bad = 0;
#pragma unroll
      for (int i = 0; i < 4; ++i) w[i] = ld_sc(rW, o, gb + i * (4 * 576));
#pragma unroll
      for (int i = 0; i < 4; ++i) bad |= tagbad(w[i], T);
      if (none_bad(c < 9 ? bad : 0u)) break;
    }
    float hv[2];
#pragma unroll
    for (int e = 0; e < 2; ++e) {
      const float sm = rbf((u2f(w[0][2 * e]) + u2f(w[1][2 * e])) + (u2f(w[2][2 * e]) + u2f(w[3][2 * e])));
      auto sw2 = __builtin_amdgcn_permlane32_swap(f2u(sm), f2u(sm), false, false);
      const float gt = u2f(sw2[0]), up = u2f(sw2[1]);
      const float t2 = 1.5957691216057308f * (gt + 0.044715f * gt * gt * gt);
      const float ge = rbf(gt / (1.0f + ex2(-t2 * LOG2E)));
      hv[e] = ge * up;
    }
    const unsigned ho = (c < 9 && g < 2) ? (unsigned)(HG_OFF + hp * 2304 + g * 1152 + j * 144 + c * 16) : OOB;
    st_sc(rW, ho, 0, v4u{cvtpk(hv[0], hv[0]), T, cvtpk(hv[1], hv[1]), T});
#pragma unroll 1
    for (int spin = 0; spin < SPIN_CAP; ++spin) {
      __builtin_amdgcn_s_sleep(32);
      MEMBAR();
#pragma unroll
      for (int i = 0; i < 3; ++i) w[i] = ld_sc(rW, (i < 2 || lane < 16) ? (unsigned)(lane * 16) : OOB, HG_OFF + hp * 2304 + i * 1024);
      if (none_bad(tagbad(w[0], T) | tagbad(w[1], T) | (lane < 16 ? tagbad(w[2], T) : 0u))) break;
    }
#pragma unroll
    for (int i = 0; i < 3; ++i) {
      const int e = (i * 64 + lane) * 2;
      const int bb = e >= 144 ? 1 : 0;
      if (i < 2 || lane < 16) *(LAS v2u*)(L + L_HD + bb * HD_S + e - bb * 144) = v2u{w[i].x, w[i].z};
    }
    lds_signal(L + L_FL + F_HD, lane);
  }
  STAMP(wave * 8 + 5);
}

extern "C" void kernel_launch(void* const* d_in, const int* in_sizes, int n_in, void* d_out, int out_size, void* d_ws, size_t ws_size,
                              hipStream_t stream) {
  (void)hipFuncSetAttribute((const void*)fused_decode, hipFuncAttributeMaxDynamicSharedMemorySize, LDS_BYTES);
  fused_decode<<<256, 384, LDS_BYTES, stream>>>((const char*)d_in[0], (const char*)d_in[1], (const char*)d_in[2], (const char*)d_in[3],
                                                (const char*)d_in[4], (const char*)d_in[6], (const char*)d_in[7], (const char*)d_in[8],
                                                (char*)d_out, (char*)d_ws);
}
```

```cpp
#define PH 0
#include <hip/hip_runtime.h>
#include <hip/hip_bf16.h>

#define WIN 8
#define NPRE 2
#define CODE_PF 1
#define TOUCH 1
#define MAP 2
#define KAUX 2
#define VAUX 2
#define WAUX 2
#define DIAG 0

#define GAS __attribute__((address_space(1)))
#define LAS __attribute__((address_space(3)))
typedef unsigned v4u __attribute__((ext_vector_type(4)));
typedef unsigned v3u __attribute__((ext_vector_type(3)));
typedef unsigned v2u __attribute__((ext_vector_type(2)));
typedef float f32x4 __attribute__((ext_vector_type(4)));
typedef float f32x2 __attribute__((ext_vector_type(2)));
typedef __bf16 bf16x8 __attribute__((ext_vector_type(8)));
typedef __bf16 bf16x2 __attribute__((ext_vector_type(2)));
typedef __amdgpu_buffer_rsrc_t rsrc_t;

constexpr int E_OFF = 0;
constexpr int QG_OFF = 4096;
constexpr int ML_OFF = QG_OFF + 524288;
constexpr int GU_OFF = ML_OFF + 8192;
constexpr int HG_OFF = GU_OFF + 589824;
constexpr int OG_OFF = HG_OFF + 73728;
constexpr int YG_OFF = OG_OFF + 262144;
constexpr int WS_USED = YG_OFF + 294912;
constexpr int TS_OFF = 4 << 20;

constexpr int L_XN = 0, XN_S = 580;
constexpr int L_HD = 1160, HD_S = 148;
constexpr int L_QB = L_HD + 2 * HD_S;
constexpr int L_OW = L_QB + 64;
constexpr int L_YW = L_OW + 512;
constexpr int L_ML = L_YW + 1024;
constexpr int L_FW = L_ML + 8;
constexpr int L_FL = L_FW + 4;
constexpr int F_KD = 0, F_Q = 4, F_T = 5, F_TOK = 6, F_XN = 7, F_FW = 8, F_HD = 9, F_OC = 10, F_YC = 11;
static_assert(L_FL % 4 == 0 && L_OW % 4 == 0 && L_YW % 4 == 0 && L_QB % 4 == 0 && L_FW % 4 == 0, "16-byte aligned LDS pieces");
constexpr int LDS_BYTES = 84 * 1024;

constexpr int AUX_NT = 2, AUX_SC1 = 16;
constexpr unsigned OOB = 0x80000000u;
constexpr unsigned CODE_BYTES = 13 * 1024;
constexpr int SPIN_CAP = 1 << 18;
constexpr float LOG2E = 1.4426950408889634f;

__device__ __forceinline__ rsrc_t mk(const void* p, unsigned bytes) { return __builtin_amdgcn_make_buffer_rsrc((void*)p, 0, bytes, 0x00020000); }
__device__ __forceinline__ v4u ld_nt(rsrc_t r, unsigned vo, unsigned so) { return __builtin_amdgcn_raw_buffer_load_b128(r, vo, so, AUX_NT); }
__device__ __forceinline__ v4u ld_k(rsrc_t r, unsigned vo, unsigned so) { return __builtin_amdgcn_raw_buffer_load_b128(r, vo, so, KAUX); }
__device__ __forceinline__ v4u ld_v(rsrc_t r, unsigned vo, unsigned so) { return __builtin_amdgcn_raw_buffer_load_b128(r, vo, so, VAUX); }
__device__ __forceinline__ v4u ld_w(rsrc_t r, unsigned vo, unsigned so) { return __builtin_amdgcn_raw_buffer_load_b128(r, vo, so, WAUX); }
__device__ __forceinline__ v4u ld_sc(rsrc_t r, unsigned vo, unsigned so) { return __builtin_amdgcn_raw_buffer_load_b128(r, vo, so, AUX_SC1); }
__device__ __forceinline__ void st_sc(rsrc_t r, unsigned vo, unsigned so, v4u v) { __builtin_amdgcn_raw_buffer_store_b128(v, r, vo, so, AUX_SC1); }
__device__ __forceinline__ float u2f(unsigned u) { return __builtin_bit_cast(float, u); }
__device__ __forceinline__ unsigned f2u(float f) { return __builtin_bit_cast(unsigned, f); }
__device__ __forceinline__ unsigned cvtpk(float lo, float hi) {
  bf16x2 r = {(__bf16)lo, (__bf16)hi};
  return __builtin_bit_cast(unsigned, r);
}
__device__ __forceinline__ float rbf(float f) { return (float)(__bf16)f; }
__device__ __forceinline__ float ex2(float f) { return __builtin_amdgcn_exp2f(f); }
__device__ __forceinline__ float dot2(unsigned a, unsigned b, float c) {
  return __builtin_amdgcn_fdot2_f32_bf16(__builtin_bit_cast(bf16x2, a), __builtin_bit_cast(bf16x2, b), c, false);
}
__device__ __forceinline__ f32x4 mfma(v4u a, v4u b, f32x4 c) {
  return __builtin_amdgcn_mfma_f32_16x16x32_bf16(__builtin_bit_cast(bf16x8, a), __builtin_bit_cast(bf16x8, b), c, 0, 0, 0);
}
template <int CTRL> __device__ __forceinline__ float dpp(float x) { return u2f(__builtin_amdgcn_mov_dpp(f2u(x), CTRL, 0xf, 0xf, true)); }
template <int CTRL> __device__ __forceinline__ unsigned dppu(unsigned x) { return __builtin_amdgcn_mov_dpp(x, CTRL, 0xf, 0xf, true); }
constexpr int XOR1 = 0xB1, XOR2 = 0x4E, XOR7 = 0x141, XOR8 = 0x128, ROR4 = 0x124, BCAST = 0x150;
__device__ __forceinline__ float sum16x(float x) { auto s = __builtin_amdgcn_permlane16_swap(f2u(x), f2u(x), false, false); return u2f(s[0]) + u2f(s[1]); }
__device__ __forceinline__ float sum32x(float x) { auto s = __builtin_amdgcn_permlane32_swap(f2u(x), f2u(x), false, false); return u2f(s[0]) + u2f(s[1]); }
__device__ __forceinline__ float max16x(float x) { auto s = __builtin_amdgcn_permlane16_swap(f2u(x), f2u(x), false, false); return fmaxf(u2f(s[0]), u2f(s[1])); }
__device__ __forceinline__ float max32x(float x) { auto s = __builtin_amdgcn_permlane32_swap(f2u(x), f2u(x), false, false); return fmaxf(u2f(s[0]), u2f(s[1])); }
__device__ __forceinline__ float rowsum(float x) { x += dpp<XOR1>(x); x += dpp<XOR2>(x); x += dpp<XOR7>(x); x += dpp<XOR8>(x); return x; }
__device__ __forceinline__ float rowmax(float x) { x = fmaxf(x, dpp<XOR1>(x)); x = fmaxf(x, dpp<XOR2>(x)); x = fmaxf(x, dpp<XOR7>(x)); x = fmaxf(x, dpp<XOR8>(x)); return x; }
#define SB() __builtin_amdgcn_sched_barrier(0)
#define MEMBAR() asm volatile("" ::: "memory")
#define LDS_FENCE() asm volatile("s_waitcnt lgkmcnt(0)" ::: "memory")
#define BARRIER() do { SB(); asm volatile("s_barrier" ::: "memory"); SB(); } while (0)
#define STR2(x) #x
#define STR(x) STR2(x)
__device__ __forceinline__ unsigned lds_ld(LAS unsigned* p) { return __hip_atomic_load(p, __ATOMIC_RELAXED, __HIP_MEMORY_SCOPE_WORKGROUP); }
__device__ __forceinline__ void lds_wait(LAS unsigned* p) {
  while (__builtin_amdgcn_readfirstlane(lds_ld(p)) == 0) __builtin_amdgcn_s_sleep(1);
  MEMBAR();
}
__device__ __forceinline__ void lds_wait4(LAS unsigned* p, int lane) {
  while (__builtin_amdgcn_ballot_w64(lds_ld(p + (lane & 3)) == 0) != 0ull) __builtin_amdgcn_s_sleep(4);
  MEMBAR();
}
__device__ __forceinline__ void lds_signal(LAS unsigned* p, int lane) {
  LDS_FENCE();
  if (lane == 0) __hip_atomic_store(p, 1u, __ATOMIC_RELAXED, __HIP_MEMORY_SCOPE_WORKGROUP);
}
__device__ __forceinline__ unsigned lds_arrive(LAS unsigned* p, int lane) {
  LDS_FENCE();
  unsigned old = 0;
  if (lane == 0) old = __hip_atomic_fetch_add(p, 1u, __ATOMIC_RELAXED, __HIP_MEMORY_SCOPE_WORKGROUP);
  old = __builtin_amdgcn_readfirstlane(old);
  MEMBAR();
  return old;
}
__device__ __forceinline__ bool none_bad(unsigned bad) { return __builtin_amdgcn_ballot_w64(bad != 0u) == 0ull; }
__device__ __forceinline__ unsigned tagbad(const v4u& v, unsigned T) { return (v.y ^ T) | (v.w ^ T); }
__device__ __forceinline__ v4u gran(float a, float b, unsigned T) { return v4u{f2u(a), T, f2u(b), T}; }
__device__ __forceinline__ v4u afrag(unsigned dup, const v4u& m) { return v4u{dup & m.x, dup & m.y, dup & m.z, dup & m.w}; }
__device__ __forceinline__ void poll2(rsrc_t rW, unsigned vo, unsigned so, unsigned T, v4u& r0, v4u& r1) {
#pragma unroll 1
  for (int spin = 0; spin < SPIN_CAP; ++spin) {
    MEMBAR();
    r0 = ld_sc(rW, vo, so); r1 = ld_sc(rW, vo, so + 1024);
    if (none_bad(tagbad(r0, T) | tagbad(r1, T))) break;
  }
}

#define STAMP(slot) do {} while (0)

template <int N, int M> __device__ __forceinline__ f32x4 gemv(const LAS unsigned* xs, const v4u& am, const v4u (&w)[M]) {
  unsigned xv[N];
#pragma unroll
  for (int i = 0; i < N; ++i) xv[i] = xs[4 * i];
  SB();
  f32x4 a0 = {0.f, 0.f, 0.f, 0.f}, a1 = a0;
#pragma unroll
  for (int i = 0; i < N; ++i) { if (i & 1) a1 = mfma(afrag(xv[i], am), w[i], a1); else a0 = mfma(afrag(xv[i], am), w[i], a0); }
  return a0 + a1;
}

__global__ void __launch_bounds__(384, 2)
fused_decode(const char* xg, const char* kc, const char* vc, const char* wq, const char* wo, const char* wg, const char* wu, const char* wd,
             char* out, char* ws) {
  extern __shared__ __attribute__((aligned(16))) unsigned smem[];
  LAS unsigned* L = (LAS unsigned*)smem;
  LAS float* Lf = (LAS float*)smem;
  const unsigned long long tk0 = __builtin_amdgcn_s_memrealtime();
  unsigned long long tph = tk0;
  unsigned long long pc0 = __builtin_amdgcn_s_getpc();
  asm volatile("" : "+s"(pc0));
  asm volatile("" : "+s"(xg), "+s"(kc), "+s"(vc), "+s"(wq), "+s"(wo), "+s"(wg), "+s"(wu), "+s"(wd), "+s"(out), "+s"(ws));
  const int lane = threadIdx.x & 63, wave = __builtin_amdgcn_readfirstlane(threadIdx.x >> 6);
  const int g = lane >> 4, c = lane & 15;
  const int bid = blockIdx.x, xc = bid & 7, r = bid >> 3;
  const int h = xc + 8 * (r & 3), j = r >> 2, s = j & 3, b = j >> 2;
  const int hp = 4 * (h & 7) + (h >> 3);
  const unsigned hm = (c & 1) ? 0xffff0000u : 0x0000ffffu;
  const int mt = (c & 7) >> 1;
  const v4u am = {mt == 0 ? hm : 0u, mt == 1 ? hm : 0u, mt == 2 ? hm : 0u, mt == 3 ? hm : 0u};
  const unsigned tvo = c < 9 ? (unsigned)(c * 16) : OOB;
  const rsrc_t rW = mk(ws, WS_USED);

  if (wave >= 2) {
    const int sw = wave - 2;
    const size_t cb = (size_t)((b * 32 + h) * 4 + s) * 524288;
    const char* kb = kc + cb;
    const char* vb = vc + cb;
    const rsrc_t rK = mk(kb, 524288), rV = mk(vb, 524288);
    const unsigned vo = sw * 1024 + lane * 16;
    constexpr int NVB = 8 - NPRE;
    BARRIER();
    v4u a[WIN], pre[NPRE * 16 + 1];
#pragma unroll
    for (int i = 0; i < WIN; ++i) a[i] = ld_k(rK, vo, i * 4096);
    SB();
#pragma unroll
    for (int i = 0; i < NPRE * 16; ++i) {
      asm volatile("s_waitcnt vmcnt(" STR(WIN) "-1)" ::: "memory");
      if (PH == 1 && i == 0) tph = __builtin_amdgcn_s_memrealtime();
      pre[i] = ld_v(rV, vo, NVB * 65536 + i * 4096);
      SB();
    }
    if (PH == 16) tph = __builtin_amdgcn_s_memrealtime();
    lds_wait(L + L_FL + F_Q);
    const v4u q = *(LAS v4u*)(L + L_QB + c * 4);
    if (PH == 17) tph = __builtin_amdgcn_s_memrealtime();
    float sc[8];
#pragma unroll
    for (int i = 0; i < 8; ++i) sc[i] = 0.f;
#pragma unroll 1
    for (int blk = 0; blk < 8; ++blk) {
      const rsrc_t rN = mk(blk < 7 ? kb : vb, 524288);
      const unsigned sC = blk * 65536, sN = blk < 7 ? sC + 65536 : 0;
      float x[16];
#pragma unroll
      for (int i = 0; i < 16; ++i) {
        const v4u k = a[i % WIN];
        float d = dot2(k.x, q.x, 0.f); d = dot2(k.y, q.y, d); d = dot2(k.z, q.z, d); d = dot2(k.w, q.w, d);
        x[i] = d;
        asm volatile("" : "+v"(x[i]));
        SB();
        if (i + WIN < 16) a[i % WIN] = ld_k(rK, vo, sC + (i + WIN) * 4096);
        else a[i % WIN] = ld_k(rN, vo, sN + (i + WIN - 16) * 4096);
        SB();
      }
#define TR_STEP(HALF, CTRL) _Pragma("unroll") for (int u = 0; u < (HALF); ++u) { const float lo_ = x[u], hi_ = x[u + (HALF)]; \
        const float t1_ = lo_ + dpp<CTRL>(lo_), t2_ = hi_ + dpp<CTRL>(hi_); x[u] = (c & (HALF)) ? t2_ : t1_; }
      TR_STEP(8, XOR8) TR_STEP(4, XOR7) TR_STEP(2, XOR2) TR_STEP(1, XOR1)
#undef TR_STEP
#pragma unroll
      for (int i = 0; i < 7; ++i) sc[i] = sc[i + 1];
      sc[7] = rbf(rbf(x[0]) * 0.08838834764831845f);
    }
    float m = sc[0];
#pragma unroll
    for (int i = 1; i < 8; ++i) m = fmaxf(m, sc[i]);
    m = max32x(max16x(rowmax(m)));
    float l = 0.f;
    unsigned pp[8];
#pragma unroll
    for (int i = 0; i < 8; ++i) { const float p = ex2((sc[i] - m) * LOG2E); l += p; pp[i] = cvtpk(p, p); }
    l = sum32x(sum16x(rowsum(l)));
    if (lane == 0) { Lf[L_ML + 2 * sw] = m; Lf[L_ML + 2 * sw + 1] = l; }
    lds_signal(L + L_FL + F_KD + sw, lane);
    if (PH == 18) tph = __builtin_amdgcn_s_memrealtime();
    const v4u pm = {c < 8 ? am.x : 0u, c < 8 ? am.y : 0u, c < 8 ? am.z : 0u, c < 8 ? am.w : 0u};
    f32x4 acc = {0.f, 0.f, 0.f, 0.f};
#define PVM(I, P, V) { acc = mfma(afrag(dppu<BCAST + (I)>(P), pm), (V), acc); asm volatile("" : "+v"(acc)); SB(); }
#define PV(I) { PVM(I, pp[0], a[(I) % WIN]) a[(I) % WIN] = ld_v(rV, vo, sC + ((I) + WIN) * 4096); SB(); }
#define PRE16(B) PVM(0, pp[NVB + B], pre[B * 16 + 0]) PVM(1, pp[NVB + B], pre[B * 16 + 1]) PVM(2, pp[NVB + B], pre[B * 16 + 2]) PVM(3, pp[NVB + B], pre[B * 16 + 3]) \
      PVM(4, pp[NVB + B], pre[B * 16 + 4]) PVM(5, pp[NVB + B], pre[B * 16 + 5]) PVM(6, pp[NVB + B], pre[B * 16 + 6]) PVM(7, pp[NVB + B], pre[B * 16 + 7]) \
      PVM(8, pp[NVB + B], pre[B * 16 + 8]) PVM(9, pp[NVB + B], pre[B * 16 + 9]) PVM(10, pp[NVB + B], pre[B * 16 + 10]) PVM(11, pp[NVB + B], pre[B * 16 + 11]) \
      PVM(12, pp[NVB + B], pre[B * 16 + 12]) PVM(13, pp[NVB + B], pre[B * 16 + 13]) PVM(14, pp[NVB + B], pre[B * 16 + 14]) PVM(15, pp[NVB + B], pre[B * 16 + 15])
    PRE16(0)
    PRE16(1)
#pragma unroll 1
    for (int blk = 0; blk < NVB - 1; ++blk) {
      const unsigned sC = blk * 65536;
      PV(0) PV(1) PV(2) PV(3) PV(4) PV(5) PV(6) PV(7) PV(8) PV(9) PV(10) PV(11) PV(12) PV(13) PV(14) PV(15)
#pragma unroll
      for (int i = 0; i < NVB - 1; ++i) pp[i] = pp[i + 1];
    }
    v4u wz[9], wy[8];
    {
      const unsigned sC = (NVB - 1) * 65536;
      const rsrc_t rD = mk(wd + (size_t)(144 * hp + 36 * sw) * 1152 + j * 144, 36 * 1152 - j * 144);
      const rsrc_t rO = mk(wo + (size_t)(128 * h + 32 * sw) * 1152 + j * 144, 32 * 1152 - j * 144);
      const unsigned to = tvo + g * 1152;
#define TL(n) { if ((n) < 9) wz[(n) < 9 ? (n) : 0] = ld_w(rD, to, (n) * 4608); else if ((n) < 17) wy[(n) < 9 || (n) >= 17 ? 0 : (n) - 9] = ld_w(rO, to, ((n) - 9) * 4608); }
#define PVL(I) { if ((I) + WIN < 16) PV(I) else { PVM(I, pp[0], a[(I) % WIN]) TL(((I) + WIN - 16) * 2) TL(((I) + WIN - 16) * 2 + 1) if ((I) == 15) TL(16) SB(); } }
      PVL(0) PVL(1) PVL(2) PVL(3) PVL(4) PVL(5) PVL(6) PVL(7) PVL(8) PVL(9) PVL(10) PVL(11) PVL(12) PVL(13) PVL(14) PVL(15)
    }
    if (lane < 32) *(LAS f32x4*)(Lf + L_OW + sw * 128 + c * 8 + g * 4) = acc;
    const unsigned T = __builtin_amdgcn_readfirstlane(L[L_FL + F_T]);
    if (PH == 19) tph = __builtin_amdgcn_s_memrealtime();
    if (lds_arrive(L + L_FL + F_OC, lane) == 3) {
      lds_wait(L + L_FL + F_FW);
      const f32x4 fw = *(LAS f32x4*)(Lf + L_FW);
      float o0 = 0.f, o1 = 0.f;
#pragma unroll
      for (int i = 0; i < 4; ++i) { const f32x2 t = *(LAS f32x2*)(Lf + L_OW + i * 128 + g * 32 + (c & 7) * 4 + (c >> 3) * 2); o0 = fmaf(fw[i], t.x, o0); o1 = fmaf(fw[i], t.y, o1); }
      st_sc(rW, g * 2048 + c * 16, OG_OFF + h * 8192 + j * 256, gran(o0, o1, T));
      STAMP(6);
    }
    lds_wait(L + L_FL + F_HD);
    f32x4 y = gemv<9>(L + L_HD + (c >> 3) * HD_S + 36 * sw + g, am, wz);
    asm volatile("" : "+v"(y));
    v4u p0, p1;
    poll2(rW, lane * 16, OG_OFF + h * 8192 + sw * 2048, T, p0, p1);
    if (sw == 0) STAMP(7);
    {
      const float e00 = sum32x(sum16x(u2f(p0.x))), e01 = sum32x(sum16x(u2f(p0.z))), e10 = sum32x(sum16x(u2f(p1.x))), e11 = sum32x(sum16x(u2f(p1.z)));
      unsigned pk = cvtpk((g & 1) ? e01 : e00, (g & 1) ? e11 : e10);
      pk = __builtin_amdgcn_update_dpp(pk, pk, XOR8, 0xc, 0xf, false);
      const unsigned src = (c >> 3) ? 0x0302u : 0x0100u, sel = (c & 1) ? (src << 16) | 0x0c0cu : 0x0c0c0000u | src;
      const v4u sl = {mt == 0 ? sel : 0x0c0c0c0cu, mt == 1 ? sel : 0x0c0c0c0cu, mt == 2 ? sel : 0x0c0c0c0cu, mt == 3 ? sel : 0x0c0c0c0cu};
      f32x4 y1 = {0.f, 0.f, 0.f, 0.f};
#define YM(I, ACC) { const unsigned bc = dppu<BCAST + (I)>(pk); ACC = mfma(v4u{__builtin_amdgcn_perm(0u, bc, sl.x), __builtin_amdgcn_perm(0u, bc, sl.y), \
        __builtin_amdgcn_perm(0u, bc, sl.z), __builtin_amdgcn_perm(0u, bc, sl.w)}, wy[I], ACC); }
      YM(0, y) YM(1, y1) YM(2, y) YM(3, y1) YM(4, y) YM(5, y1) YM(6, y) YM(7, y1)
      y += y1;
    }
    *(LAS f32x4*)(Lf + L_YW + sw * 256 + lane * 4) = y;
    if (lds_arrive(L + L_FL + F_YC, lane) == 3) {
      f32x4 t = *(LAS f32x4*)(Lf + L_YW + lane * 4);
#pragma unroll
      for (int i = 1; i < 4; ++i) t += *(LAS f32x4*)(Lf + L_YW + i * 256 + lane * 4);
      const unsigned o = c < 9 ? (unsigned)(YG_OFF + ((j * 18 + (g >> 1) * 9 + c) * 32 + h) * 64 + (g & 1) * 32) : OOB;
      st_sc(rW, o, 0, gran(t[0], t[1], T));
      st_sc(rW, o + 16, 0, gran(t[2], t[3], T));
      STAMP(20);
      if (h < 18) {
        const int oo = (h / 9) * 1152 + (72 * j + 8 * (h % 9)) * 2 + (lane & 3) * 4;
        const unsigned xres = *(const GAS unsigned*)(xg + oo);
        poll2(rW, lane * 16, YG_OFF + (j * 18 + h) * 2048, T, p0, p1);
        float y0 = u2f(p0.x) + u2f(p1.x), y1 = u2f(p0.z) + u2f(p1.z);
        y0 += dpp<ROR4>(y0); y1 += dpp<ROR4>(y1);
        y0 += dpp<XOR8>(y0); y1 += dpp<XOR8>(y1);
        y0 = sum32x(sum16x(y0)); y1 = sum32x(sum16x(y1));
        if (lane < 4) *(GAS unsigned*)(out + oo) = cvtpk(u2f(xres << 16) + y0, u2f(xres & 0xffff0000u) + y1);
        if (bid == 0 && lane == 0) __builtin_amdgcn_raw_buffer_store_b32(T, rW, E_OFF, 0, AUX_SC1);
        STAMP(15);
      }
    }
    if (PH == 15) tph = __builtin_amdgcn_s_memrealtime();
    while (bid == 0 && __builtin_amdgcn_s_memrealtime() - tph < 8000ull) __builtin_amdgcn_s_sleep(2);
    return;
  }

  const bool isA = wave == 0;
  STAMP(wave * 8 + 0);
  v4u w[36];
  unsigned T;
  if (isA) {
    const int hq = (r & 7) + 8 * (xc & 3), jq = (r >> 3) + 4 * (xc >> 2);
    const rsrc_t rQ = mk(wq + (size_t)(72 * jq) * 8192 + hq * 256, 72 * 8192 - hq * 256);
    const unsigned E = __builtin_amdgcn_raw_buffer_load_b32(rW, E_OFF, 0, AUX_SC1);
#pragma unroll
    for (int i = 0; i < 18; ++i) w[i] = ld_w(rQ, g * 8192 + c * 16, i * 32768);
    BARRIER();
    T = __builtin_amdgcn_readfirstlane(E) + 0x9E3779B9u;
    if (lane == 0) L[L_FL + F_T] = T;
    lds_signal(L + L_FL + F_TOK, lane);
    lds_wait(L + L_FL + F_XN);
    STAMP(1);
    const f32x4 a0 = gemv<18>(L + L_XN + (c >> 3) * XN_S + 72 * jq + g, am, w);
    const unsigned o = QG_OFF + ((hq * 2 + (g >> 1)) * 8 + jq) * 1024 + (c * 8 + (g & 1) * 4) * 8;
    st_sc(rW, o, 0, gran(a0[0], a0[1], T));
    st_sc(rW, o + 16, 0, gran(a0[2], a0[3], T));
    STAMP(2);
    float q0, q1;
#pragma unroll 1
    for (int spin = 0; spin < SPIN_CAP; ++spin) {
      __builtin_amdgcn_s_sleep(8);
      MEMBAR();
#pragma unroll
      for (int i = 0; i < 8; ++i) w[i] = ld_sc(rW, lane * 16, QG_OFF + ((h * 2 + b) * 8 + i) * 1024);
      unsigned bad = 0; q0 = 0.f; q1 = 0.f;
#pragma unroll
      for (int i = 0; i < 8; ++i) { bad |= tagbad(w[i], T); q0 += u2f(w[i].x); q1 += u2f(w[i].z); }
      if (none_bad(bad)) break;
    }
    L[L_QB + lane] = cvtpk(q0, q1);
    lds_signal(L + L_FL + F_Q, lane);
    STAMP(3);
  } else {
    v3u xr[3];
    const rsrc_t rX = mk(xg, 2304);
#pragma unroll
    for (int k = 0; k < 3; ++k) xr[k] = __builtin_amdgcn_raw_buffer_load_b96(rX, (lane >> 5) * 1152 + (lane & 31) * 12, k * 384, 0);
    if (lane < 12) L[L_FL + lane] = 0u;
    LDS_FENCE();
    BARRIER();
    const unsigned cv = __builtin_amdgcn_raw_buffer_load_b32(mk((const void*)(pc0 & ~255ull), CODE_BYTES), (r * 4 + (lane & 3)) * 128, 0, 0);
    const char* tp = (lane & 4) ? wo + (size_t)(128 * h + 40 * (lane & 3)) * 1152 + j * 144 : wd + (size_t)(144 * hp + 45 * (lane & 3)) * 1152 + j * 144;
    if ((lane & 15) >= 8) tp = out;
    const unsigned tv = __builtin_nontemporal_load((const GAS unsigned*)tp);
    SB();
    float xv[18];
#pragma unroll
    for (int k = 0; k < 3; ++k)
#pragma unroll
      for (int t = 0; t < 3; ++t) { xv[k * 6 + 2 * t] = u2f(xr[k][t] << 16); xv[k * 6 + 2 * t + 1] = u2f(xr[k][t] & 0xffff0000u); }
    float sum = 0.f;
#pragma unroll
    for (int i = 0; i < 18; ++i) sum += xv[i];
    const float mean = sum16x(rowsum(sum)) * (1.0f / 576.0f);
    float sq = 0.f;
#pragma unroll
    for (int i = 0; i < 18; ++i) { const float d = xv[i] - mean; sq = fmaf(d, d, sq); }
    const float var = sum16x(rowsum(sq)) * (1.0f / 576.0f);
    const float mb = rbf(mean), sd = rbf(__builtin_sqrtf(rbf(rbf(var) + 1e-5f))), rs = 1.0f / sd;
    LAS v2u* dst = (LAS v2u*)(L + L_XN + (lane >> 5) * XN_S + (lane & 31) * 6);
#pragma unroll
    for (int k = 0; k < 3; ++k)
#pragma unroll
      for (int t = 0; t < 3; ++t) {
        float n[2];
#pragma unroll
        for (int e = 0; e < 2; ++e) { const float d = rbf(xv[k * 6 + 2 * t + e] - mb); const float q0 = d * rs; n[e] = fmaf(fmaf(-q0, sd, d), rs, q0); }
        dst[k * 96 + t] = v2u{cvtpk(n[0], n[0]), cvtpk(n[1], n[1])};
      }
    lds_signal(L + L_FL + F_XN, lane);
    STAMP(9);
    lds_wait(L + L_FL + F_TOK);
    asm volatile("" :: "v"(cv));
    asm volatile("" :: "v"(tv));
    T = __builtin_amdgcn_readfirstlane(L[L_FL + F_T]);
  }
  {
    const int qd = j >> 1, half = j & 1;
    const unsigned cofs = hp * 288 + half * 144;
    const rsrc_t rG = mk((isA ? wu : wg) + (size_t)(144 * qd) * 9216 + cofs, 144 * 9216 - cofs);
#pragma unroll
    for (int i = 0; i < 36; ++i) w[i] = ld_w(rG, tvo + g * 9216, i * 36864);
    SB();
    const f32x4 a0 = gemv<36>(L + L_XN + (c >> 3) * XN_S + 144 * qd + g, am, w);
    const unsigned o = c < 9 ? (unsigned)(GU_OFF + ((((hp * 2 + half) * 4 + qd) * 2 + (isA ? 1 : 0)) * 2 + (g >> 1)) * 576 + (c * 8 + (g & 1) * 4) * 8) : OOB;
    st_sc(rW, o, 0, gran(a0[0], a0[1], T));
    st_sc(rW, o + 16, 0, gran(a0[2], a0[3], T));
  }
  STAMP(wave * 8 + 4);
  if (isA) {
    lds_wait4(L + L_FL + F_KD, lane);
    float mw[4], lw[4];
#pragma unroll
    for (int i = 0; i < 4; ++i) { mw[i] = Lf[L_ML + 2 * i]; lw[i] = Lf[L_ML + 2 * i + 1]; }
    const float mg = fmaxf(fmaxf(mw[0], mw[1]), fmaxf(mw[2], mw[3]));
    float lg = 0.f;
#pragma unroll
    for (int i = 0; i < 4; ++i) lg += lw[i] * ex2((mw[i] - mg) * LOG2E);
    const unsigned mlb = ML_OFF + (b * 32 + h) * 128;
    st_sc(rW, lane == 0 ? mlb + s * 16 : OOB, 0, gran(mg, lg, T));
    v4u v;
#pragma unroll 1
    for (int spin = 0; spin < SPIN_CAP; ++spin) {
      __builtin_amdgcn_s_sleep(16);
      MEMBAR();
      v = ld_sc(rW, (lane & 3) * 16, mlb);
      if (none_bad(tagbad(v, T))) break;
    }
    const float ms = u2f(v.x);
    float M = fmaxf(ms, dpp<XOR1>(ms)); M = fmaxf(M, dpp<XOR2>(M));
    float Ls = u2f(v.z) * ex2((ms - M) * LOG2E);
    Ls += dpp<XOR1>(Ls); Ls += dpp<XOR2>(Ls);
    const float inv = 1.0f / Ls;
    if (lane == 0) *(LAS f32x4*)(Lf + L_FW) = f32x4{ex2((mw[0] - M) * LOG2E) * inv, ex2((mw[1] - M) * LOG2E) * inv, ex2((mw[2] - M) * LOG2E) * inv, ex2((mw[3] - M) * LOG2E) * inv};
    lds_signal(L + L_FL + F_FW, lane);
  } else {
    const unsigned gb = GU_OFF + (hp * 2 + (j >> 2)) * (4 * 4 * 576);
    const unsigned o = c < 9 ? (unsigned)(g * 576 + (j & 3) * 144 + c * 16) : OOB;
#pragma unroll 1
    for (int spin = 0; spin < SPIN_CAP; ++spin) {
      __builtin_amdgcn_s_sleep(32);
      MEMBAR();
      unsigned bad = 0;
#pragma unroll
      for (int i = 0; i < 4; ++i) w[i] = ld_sc(rW, o, gb + i * (4 * 576));
#pragma unroll
      for (int i = 0; i < 4; ++i) bad |= tagbad(w[i], T);
      if (none_bad(c < 9 ? bad : 0u)) break;
    }
    float hv[2];
#pragma unroll
    for (int e = 0; e < 2; ++e) {
      const float sm = rbf((u2f(w[0][2 * e]) + u2f(w[1][2 * e])) + (u2f(w[2][2 * e]) + u2f(w[3][2 * e])));
      auto sw2 = __builtin_amdgcn_permlane32_swap(f2u(sm), f2u(sm), false, false);
      const float gt = u2f(sw2[0]), up = u2f(sw2[1]);
      const float t2 = 1.5957691216057308f * (gt + 0.044715f * gt * gt * gt);
      const float ge = rbf(gt / (1.0f + ex2(-t2 * LOG2E)));
      hv[e] = ge * up;
    }
    const unsigned ho = (c < 9 && g < 2) ? (unsigned)(HG_OFF + hp * 2304 + g * 1152 + j * 144 + c * 16) : OOB;
    st_sc(rW, ho, 0, v4u{cvtpk(hv[0], hv[0]), T, cvtpk(hv[1], hv[1]), T});
#pragma unroll 1
    for (int spin = 0; spin < SPIN_CAP; ++spin) {
      __builtin_amdgcn_s_sleep(32);
      MEMBAR();
#pragma unroll
      for (int i = 0; i < 3; ++i) w[i] = ld_sc(rW, (i < 2 || lane < 16) ? (unsigned)(lane * 16) : OOB, HG_OFF + hp * 2304 + i * 1024);
      if (none_bad(tagbad(w[0], T) | tagbad(w[1], T) | (lane < 16 ? tagbad(w[2], T) : 0u))) break;
    }
#pragma unroll
    for (int i = 0; i < 3; ++i) {
      const int e = (i * 64 + lane) * 2;
      const int bb = e >= 144 ? 1 : 0;
      if (i < 2 || lane < 16) *(LAS v2u*)(L + L_HD + bb * HD_S + e - bb * 144) = v2u{w[i].x, w[i].z};
    }
    lds_signal(L + L_FL + F_HD, lane);
  }
  STAMP(wave * 8 + 5);
}

extern "C" void kernel_launch(void* const* d_in, const int* in_sizes, int n_in, void* d_out, int out_size, void* d_ws, size_t ws_size,
                              hipStream_t stream) {
  (void)hipFuncSetAttribute((const void*)fused_decode, hipFuncAttributeMaxDynamicSharedMemorySize, LDS_BYTES);
  fused_decode<<<256, 384, LDS_BYTES, stream>>>((const char*)d_in[0], (const char*)d_in[1], (const char*)d_in[2], (const char*)d_in[3],
                                                (const char*)d_in[4], (const char*)d_in[6], (const char*)d_in[7], (const char*)d_in[8],
                                                (char*)d_out, (char*)d_ws);
}
```

```cpp
#define QDELAY 32
#define TAU 262
#define WIN 16
#define NPRE 1
#include <hip/hip_runtime.h>
#include <hip/hip_bf16.h>

#define CODE_PF 1
#define TOUCH 1
#define MAP 2
#define KAUX 2
#define VAUX 2
#define WAUX 2
#define TSLEEP 0
#define ABAR 0
#define DIAG 0

#define GAS __attribute__((address_space(1)))
#define LAS __attribute__((address_space(3)))
typedef unsigned v4u __attribute__((ext_vector_type(4)));
typedef unsigned v3u __attribute__((ext_vector_type(3)));
typedef unsigned v2u __attribute__((ext_vector_type(2)));
typedef float f32x4 __attribute__((ext_vector_type(4)));
typedef float f32x2 __attribute__((ext_vector_type(2)));
typedef __bf16 bf16x8 __attribute__((ext_vector_type(8)));
typedef __bf16 bf16x2 __attribute__((ext_vector_type(2)));
typedef __amdgpu_buffer_rsrc_t rsrc_t;

constexpr int E_OFF = 0;
constexpr int QG_OFF = 4096;
constexpr int ML_OFF = QG_OFF + 262144;
constexpr int GU_OFF = ML_OFF + 8192;
constexpr int OG_OFF = GU_OFF + 294912;
constexpr int YG_OFF = OG_OFF + 262144;
constexpr int WS_USED = YG_OFF + 294912;
constexpr int TS_OFF = 4 << 20;

constexpr int L_XN = 0, XN_S = 580;
constexpr int L_HD = 1160, HD_S = 148;
constexpr int L_QB = L_HD + 2 * HD_S;
constexpr int L_OW = L_QB + 64;
constexpr int L_YW = L_OW + 512;
constexpr int L_ML = L_YW + 1024;
constexpr int L_FW = L_ML + 8;
constexpr int L_FL = L_FW + 4;
constexpr int F_KD = 0, F_Q = 4, F_T = 5, F_TOK = 6, F_XN = 7, F_FW = 8, F_HD = 9, F_OC = 10, F_YC = 11;
static_assert(L_FL % 4 == 0 && L_OW % 4 == 0 && L_YW % 4 == 0 && L_QB % 4 == 0 && L_FW % 4 == 0, "16-byte aligned LDS pieces");
constexpr int LDS_BYTES = 84 * 1024;

constexpr int AUX_NT = 2, AUX_SC1 = 16;
constexpr unsigned OOB = 0x80000000u;
constexpr unsigned CODE_BYTES = 16 * 1024;
constexpr int SPIN_CAP = 1 << 18;
constexpr float LOG2E = 1.4426950408889634f;

__device__ __forceinline__ rsrc_t mk(const void* p, unsigned bytes) { return __builtin_amdgcn_make_buffer_rsrc((void*)p, 0, bytes, 0x00020000); }
__device__ __forceinline__ v4u ld_nt(rsrc_t r, unsigned vo, unsigned so) { return __builtin_amdgcn_raw_buffer_load_b128(r, vo, so, AUX_NT); }
__device__ __forceinline__ v4u ld_k(rsrc_t r, unsigned vo, unsigned so) { return __builtin_amdgcn_raw_buffer_load_b128(r, vo, so, KAUX); }
__device__ __forceinline__ v4u ld_v(rsrc_t r, unsigned vo, unsigned so) { return __builtin_amdgcn_raw_buffer_load_b128(r, vo, so, VAUX); }
__device__ __forceinline__ v4u ld_w(rsrc_t r, unsigned vo, unsigned so) { return __builtin_amdgcn_raw_buffer_load_b128(r, vo, so, WAUX); }
__device__ __forceinline__ v4u ld_sc(rsrc_t r, unsigned vo, unsigned so) { return __builtin_amdgcn_raw_buffer_load_b128(r, vo, so, AUX_SC1); }
__device__ __forceinline__ void st_sc(rsrc_t r, unsigned vo, unsigned so, v4u v) { __builtin_amdgcn_raw_buffer_store_b128(v, r, vo, so, AUX_SC1); }
__device__ __forceinline__ float u2f(unsigned u) { return __builtin_bit_cast(float, u); }
__device__ __forceinline__ unsigned f2u(float f) { return __builtin_bit_cast(unsigned, f); }
__device__ __forceinline__ unsigned cvtpk(float lo, float hi) {
  bf16x2 r = {(__bf16)lo, (__bf16)hi};
  return __builtin_bit_cast(unsigned, r);
}
__device__ __forceinline__ float rbf(float f) { return (float)(__bf16)f; }
__device__ __forceinline__ float ex2(float f) { return __builtin_amdgcn_exp2f(f); }
__device__ __forceinline__ float dot2(unsigned a, unsigned b, float c) {
  return __builtin_amdgcn_fdot2_f32_bf16(__builtin_bit_cast(bf16x2, a), __builtin_bit_cast(bf16x2, b), c, false);
}
__device__ __forceinline__ f32x4 mfma(v4u a, v4u b, f32x4 c) {
  return __builtin_amdgcn_mfma_f32_16x16x32_bf16(__builtin_bit_cast(bf16x8, a), __builtin_bit_cast(bf16x8, b), c, 0, 0, 0);
}
template <int CTRL> __device__ __forceinline__ float dpp(float x) { return u2f(__builtin_amdgcn_mov_dpp(f2u(x), CTRL, 0xf, 0xf, true)); }
template <int CTRL> __device__ __forceinline__ unsigned dppu(unsigned x) { return __builtin_amdgcn_mov_dpp(x, CTRL, 0xf, 0xf, true); }
constexpr int XOR1 = 0xB1, XOR2 = 0x4E, XOR7 = 0x141, XOR8 = 0x128, ROR4 = 0x124, BCAST = 0x150;
__device__ __forceinline__ float sum16x(float x) { auto s = __builtin_amdgcn_permlane16_swap(f2u(x), f2u(x), false, false); return u2f(s[0]) + u2f(s[1]); }
__device__ __forceinline__ float sum32x(float x) { auto s = __builtin_amdgcn_permlane32_swap(f2u(x), f2u(x), false, false); return u2f(s[0]) + u2f(s[1]); }
__device__ __forceinline__ float max16x(float x) { auto s = __builtin_amdgcn_permlane16_swap(f2u(x), f2u(x), false, false); return fmaxf(u2f(s[0]), u2f(s[1])); }
__device__ __forceinline__ float max32x(float x) { auto s = __builtin_amdgcn_permlane32_swap(f2u(x), f2u(x), false, false); return fmaxf(u2f(s[0]), u2f(s[1])); }
__device__ __forceinline__ float rowsum(float x) { x += dpp<XOR1>(x); x += dpp<XOR2>(x); x += dpp<XOR7>(x); x += dpp<XOR8>(x); return x; }
__device__ __forceinline__ float rowmax(float x) { x = fmaxf(x, dpp<XOR1>(x)); x = fmaxf(x, dpp<XOR2>(x)); x = fmaxf(x, dpp<XOR7>(x)); x = fmaxf(x, dpp<XOR8>(x)); return x; }
#define SB() __builtin_amdgcn_sched_barrier(0)
#define MEMBAR() asm volatile("" ::: "memory")
#define LDS_FENCE() asm volatile("s_waitcnt lgkmcnt(0)" ::: "memory")
#define BARRIER() do { SB(); asm volatile("s_barrier" ::: "memory"); SB(); } while (0)
#define STR2(x) #x
#define STR(x) STR2(x)
__device__ __forceinline__ unsigned lds_ld(LAS unsigned* p) { return __hip_atomic_load(p, __ATOMIC_RELAXED, __HIP_MEMORY_SCOPE_WORKGROUP); }
__device__ __forceinline__ void lds_wait(LAS unsigned* p) {
  while (__builtin_amdgcn_readfirstlane(lds_ld(p)) == 0) __builtin_amdgcn_s_sleep(1);
  MEMBAR();
}
__device__ __forceinline__ void lds_wait4(LAS unsigned* p, int lane) {
  while (__builtin_amdgcn_ballot_w64(lds_ld(p + (lane & 3)) == 0) != 0ull) __builtin_amdgcn_s_sleep(4);
  MEMBAR();
}
__device__ __forceinline__ void lds_signal(LAS unsigned* p, int lane) {
  LDS_FENCE();
  if (lane == 0) __hip_atomic_store(p, 1u, __ATOMIC_RELAXED, __HIP_MEMORY_SCOPE_WORKGROUP);
}
__device__ __forceinline__ unsigned lds_arrive(LAS unsigned* p, int lane) {
  LDS_FENCE();
  unsigned old = 0;
  if (lane == 0) old = __hip_atomic_fetch_add(p, 1u, __ATOMIC_RELAXED, __HIP_MEMORY_SCOPE_WORKGROUP);
  old = __builtin_amdgcn_readfirstlane(old);
  MEMBAR();
  return old;
}
__device__ __forceinline__ bool none_bad(unsigned bad) { return __builtin_amdgcn_ballot_w64(bad != 0u) == 0ull; }
__device__ __forceinline__ unsigned tagbad(const v4u& v, unsigned T) { return (v.y ^ T) | (v.w ^ T); }
__device__ __forceinline__ v4u gran(float a, float b, unsigned T) { return v4u{f2u(a), T, f2u(b), T}; }
__device__ __forceinline__ v4u afrag(unsigned dup, const v4u& m) { return v4u{dup & m.x, dup & m.y, dup & m.z, dup & m.w}; }
__device__ __forceinline__ void poll2(rsrc_t rW, unsigned vo, unsigned so, unsigned T, v4u& r0, v4u& r1) {
#pragma unroll 1
  for (int spin = 0; spin < SPIN_CAP; ++spin) {
    MEMBAR();
    r0 = ld_sc(rW, vo, so); r1 = ld_sc(rW, vo, so + 1024);
    if (none_bad(tagbad(r0, T) | tagbad(r1, T))) break;
    if (TSLEEP) __builtin_amdgcn_s_sleep(TSLEEP);
  }
}

#define STAMP(slot) do {} while (0)

template <int N, int M> __device__ __forceinline__ f32x4 gemv(f32x4 a0, const LAS unsigned* xs, const v4u& am, const v4u (&w)[M]) {
  unsigned xv[N];
#pragma unroll
  for (int i = 0; i < N; ++i) xv[i] = xs[4 * i];
  SB();
  f32x4 a1 = {0.f, 0.f, 0.f, 0.f};
#pragma unroll
  for (int i = 0; i < N; ++i) { if (i & 1) a1 = mfma(afrag(xv[i], am), w[i], a1); else a0 = mfma(afrag(xv[i], am), w[i], a0); }
  return a0 + a1;
}
#define PACE(n) do { const unsigned need_ = ((unsigned)(n) * TAU) >> 4; while ((unsigned)__builtin_amdgcn_s_memrealtime() - tk0 < need_) __builtin_amdgcn_s_sleep(1); } while (0)

__global__ void __launch_bounds__(384, 2)
fused_decode(const char* xg, const char* kc, const char* vc, const char* wq, const char* wo, const char* wg, const char* wu, const char* wd,
             char* out, char* ws) {
  extern __shared__ __attribute__((aligned(16))) unsigned smem[];
  LAS unsigned* L = (LAS unsigned*)smem;
  LAS float* Lf = (LAS float*)smem;
  unsigned long long pc0 = __builtin_amdgcn_s_getpc();
  asm volatile("" : "+s"(pc0));
  asm volatile("" : "+s"(xg), "+s"(kc), "+s"(vc), "+s"(wq), "+s"(wo), "+s"(wg), "+s"(wu), "+s"(wd), "+s"(out), "+s"(ws));
  const int lane = threadIdx.x & 63, wave = __builtin_amdgcn_readfirstlane(threadIdx.x >> 6);
  const int g = lane >> 4, c = lane & 15;
  const int bid = blockIdx.x, xc = bid & 7, r = bid >> 3;
  const int h = xc + 8 * (r & 3), j = r >> 2, s = j & 3, b = j >> 2;
  const int hp = 4 * (h & 7) + (h >> 3);
  const unsigned hm = (c & 1) ? 0xffff0000u : 0x0000ffffu;
  const int mt = (c & 7) >> 1;
  const v4u am = {mt == 0 ? hm : 0u, mt == 1 ? hm : 0u, mt == 2 ? hm : 0u, mt == 3 ? hm : 0u};
  const unsigned tvo = c < 9 ? (unsigned)(c * 16) : OOB;
  const rsrc_t rW = mk(ws, WS_USED);

  if (__builtin_expect(wave < 4, 1)) {
    const int sw = wave;
    const unsigned tk0 = (unsigned)__builtin_amdgcn_s_memrealtime();
    const size_t cb = (size_t)((b * 32 + h) * 4 + s) * 524288;
    const char* kb = kc + cb;
    const char* vb = vc + cb;
    const rsrc_t rK = mk(kb, 524288), rV = mk(vb, 524288);
    const unsigned vo = sw * 1024 + lane * 16;
    constexpr int NVB = 8 - NPRE;
    v4u a[WIN], pre[NPRE * 16 + 1];
#pragma unroll
    for (int i = 0; i < WIN; ++i) a[i] = ld_k(rK, vo, i * 4096);
    BARRIER();
#pragma unroll
    for (int i = 0; i < NPRE * 16; ++i) {
      asm volatile("s_waitcnt vmcnt(7)" ::: "memory");
      pre[i] = ld_v(rV, vo, NVB * 65536 + i * 4096);
      SB();
    }
    if (sw == 0) STAMP(16);
    lds_wait(L + L_FL + F_Q);
    const v4u q = *(LAS v4u*)(L + L_QB + c * 4);
    if (sw == 0) STAMP(17);
    float sc[8];
#pragma unroll
    for (int i = 0; i < 8; ++i) sc[i] = 0.f;
#pragma unroll 1
    for (int blk = 0; blk < 8; ++blk) {
      const rsrc_t rN = mk(blk < 7 ? kb : vb, 524288);
      const unsigned sC = blk * 65536, sN = blk < 7 ? sC + 65536 : 0;
      float x[16];
#pragma unroll
      for (int i = 0; i < 16; ++i) {
        const v4u k = a[i % WIN];
        float d = dot2(k.x, q.x, 0.f); d = dot2(k.y, q.y, d); d = dot2(k.z, q.z, d); d = dot2(k.w, q.w, d);
        x[i] = d;
        asm volatile("" : "+v"(x[i]));
        SB();
        if ((i & 3) == 0) PACE(NPRE * 16 + blk * 16 + i);
        if (i + WIN < 16) a[i % WIN] = ld_k(rK, vo, sC + (i + WIN) * 4096);
        else a[i % WIN] = ld_k(rN, vo, sN + (i + WIN - 16) * 4096);
        SB();
      }
#define TR_STEP(HALF, CTRL) _Pragma("unroll") for (int u = 0; u < (HALF); ++u) { const float lo_ = x[u], hi_ = x[u + (HALF)]; \
        const float t1_ = lo_ + dpp<CTRL>(lo_), t2_ = hi_ + dpp<CTRL>(hi_); x[u] = (c & (HALF)) ? t2_ : t1_; }
      TR_STEP(8, XOR8) TR_STEP(4, XOR7) TR_STEP(2, XOR2) TR_STEP(1, XOR1)
#undef TR_STEP
#pragma unroll
      for (int i = 0; i < 7; ++i) sc[i] = sc[i + 1];
      sc[7] = rbf(rbf(x[0]) * 0.08838834764831845f);
    }
    float m = sc[0];
#pragma unroll
    for (int i = 1; i < 8; ++i) m = fmaxf(m, sc[i]);
    m = max32x(max16x(rowmax(m)));
    float l = 0.f;
    unsigned pp[8];
#pragma unroll
    for (int i = 0; i < 8; ++i) { const float p = ex2((sc[i] - m) * LOG2E); l += p; pp[i] = cvtpk(p, p); }
    l = sum32x(sum16x(rowsum(l)));
    if (lane == 0) { Lf[L_ML + 2 * sw] = m; Lf[L_ML + 2 * sw + 1] = l; }
    lds_signal(L + L_FL + F_KD + sw, lane);
    if (sw == 0) STAMP(18);
    const v4u pm = {c < 8 ? am.x : 0u, c < 8 ? am.y : 0u, c < 8 ? am.z : 0u, c < 8 ? am.w : 0u};
    f32x4 acc = {0.f, 0.f, 0.f, 0.f};
#define PVM(I, P, V) { acc = mfma(afrag(dppu<BCAST + (I)>(P), pm), (V), acc); asm volatile("" : "+v"(acc)); SB(); }
#define PV(I) { PVM(I, pp[0], a[(I) % WIN]) if (((I) & 3) == 0) PACE(NPRE * 16 + 128 + (sC >> 12) + (I)); a[(I) % WIN] = ld_v(rV, vo, sC + ((I) + WIN) * 4096); SB(); }
#define PRE16(B) PVM(0, pp[NVB + B], pre[B * 16 + 0]) PVM(1, pp[NVB + B], pre[B * 16 + 1]) PVM(2, pp[NVB + B], pre[B * 16 + 2]) PVM(3, pp[NVB + B], pre[B * 16 + 3]) \
      PVM(4, pp[NVB + B], pre[B * 16 + 4]) PVM(5, pp[NVB + B], pre[B * 16 + 5]) PVM(6, pp[NVB + B], pre[B * 16 + 6]) PVM(7, pp[NVB + B], pre[B * 16 + 7]) \
      PVM(8, pp[NVB + B], pre[B * 16 + 8]) PVM(9, pp[NVB + B], pre[B * 16 + 9]) PVM(10, pp[NVB + B], pre[B * 16 + 10]) PVM(11, pp[NVB + B], pre[B * 16 + 11]) \
      PVM(12, pp[NVB + B], pre[B * 16 + 12]) PVM(13, pp[NVB + B], pre[B * 16 + 13]) PVM(14, pp[NVB + B], pre[B * 16 + 14]) PVM(15, pp[NVB + B], pre[B * 16 + 15])
    PRE16(0)
#pragma unroll 1
    for (int blk = 0; blk < NVB - 1; ++blk) {
      const unsigned sC = blk * 65536;
      PV(0) PV(1) PV(2) PV(3) PV(4) PV(5) PV(6) PV(7) PV(8) PV(9) PV(10) PV(11) PV(12) PV(13) PV(14) PV(15)
#pragma unroll
      for (int i = 0; i < NVB - 1; ++i) pp[i] = pp[i + 1];
    }
    v4u wz[9], wy[8];
    {
      const unsigned sC = (NVB - 1) * 65536;
      const rsrc_t rD = mk(wd + (size_t)(144 * hp + 36 * sw) * 1152 + j * 144, 36 * 1152 - j * 144);
      const rsrc_t rO = mk(wo + (size_t)(128 * h + 32 * sw) * 1152 + j * 144, 32 * 1152 - j * 144);
      const unsigned to = tvo + g * 1152;
#define TL(n) { if ((n) < 9) wz[(n) < 9 ? (n) : 0] = ld_w(rD, to, (n) * 4608); else if ((n) < 17) wy[(n) < 9 || (n) >= 17 ? 0 : (n) - 9] = ld_w(rO, to, ((n) - 9) * 4608); }
#define PVL(I) { if ((I) + WIN < 16) PV(I) else { PVM(I, pp[0], a[(I) % WIN]) TL(((I) + WIN - 16) * 2) TL(((I) + WIN - 16) * 2 + 1) if ((I) == 15) TL(16) SB(); } }
      PVL(0) PVL(1) PVL(2) PVL(3) PVL(4) PVL(5) PVL(6) PVL(7) PVL(8) PVL(9) PVL(10) PVL(11) PVL(12) PVL(13) PVL(14) PVL(15)
    }
    if (lane < 32) *(LAS f32x4*)(Lf + L_OW + sw * 128 + c * 8 + g * 4) = acc;
    const unsigned T = __builtin_amdgcn_readfirstlane(L[L_FL + F_T]);
    if (sw == 0) STAMP(19);
    if (lds_arrive(L + L_FL + F_OC, lane) == 3) {
      lds_wait(L + L_FL + F_FW);
      const f32x4 fw = *(LAS f32x4*)(Lf + L_FW);
      float o0 = 0.f, o1 = 0.f;
#pragma unroll
      for (int i = 0; i < 4; ++i) { const f32x2 t = *(LAS f32x2*)(Lf + L_OW + i * 128 + g * 32 + (c & 7) * 4 + (c >> 3) * 2); o0 = fmaf(fw[i], t.x, o0); o1 = fmaf(fw[i], t.y, o1); }
      st_sc(rW, g * 2048 + c * 16, OG_OFF + h * 8192 + j * 256, gran(o0, o1, T));
      STAMP(6);
    }
    lds_wait(L + L_FL + F_HD);
    f32x4 y = gemv<9>(f32x4{0.f, 0.f, 0.f, 0.f}, L + L_HD + (c >> 3) * HD_S + 36 * sw + g, am, wz);
    asm volatile("" : "+v"(y));
    v4u p0, p1;
    poll2(rW, lane * 16, OG_OFF + h * 8192 + sw * 2048, T, p0, p1);
    if (sw == 0) STAMP(7);
    {
      const float e00 = sum32x(sum16x(u2f(p0.x))), e01 = sum32x(sum16x(u2f(p0.z))), e10 = sum32x(sum16x(u2f(p1.x))), e11 = sum32x(sum16x(u2f(p1.z)));
      unsigned pk = cvtpk((g & 1) ? e01 : e00, (g & 1) ? e11 : e10);
      pk = __builtin_amdgcn_update_dpp(pk, pk, XOR8, 0xc, 0xf, false);
      const unsigned src = (c >> 3) ? 0x0302u : 0x0100u, sel = (c & 1) ? (src << 16) | 0x0c0cu : 0x0c0c0000u | src;
      const v4u sl = {mt == 0 ? sel : 0x0c0c0c0cu, mt == 1 ? sel : 0x0c0c0c0cu, mt == 2 ? sel : 0x0c0c0c0cu, mt == 3 ? sel : 0x0c0c0c0cu};
      f32x4 y1 = {0.f, 0.f, 0.f, 0.f};
#define YM(I, ACC) { const unsigned bc = dppu<BCAST + (I)>(pk); ACC = mfma(v4u{__builtin_amdgcn_perm(0u, bc, sl.x), __builtin_amdgcn_perm(0u, bc, sl.y), \
        __builtin_amdgcn_perm(0u, bc, sl.z), __builtin_amdgcn_perm(0u, bc, sl.w)}, wy[I], ACC); }
      YM(0, y) YM(1, y1) YM(2, y) YM(3, y1) YM(4, y) YM(5, y1) YM(6, y) YM(7, y1)
      y += y1;
    }
    *(LAS f32x4*)(Lf + L_YW + sw * 256 + lane * 4) = y;
    if (lds_arrive(L + L_FL + F_YC, lane) == 3) {
      f32x4 t = *(LAS f32x4*)(Lf + L_YW + lane * 4);
#pragma unroll
      for (int i = 1; i < 4; ++i) t += *(LAS f32x4*)(Lf + L_YW + i * 256 + lane * 4);
      const unsigned o = c < 9 ? (unsigned)(YG_OFF + ((j * 18 + (g >> 1) * 9 + c) * 32 + h) * 64 + (g & 1) * 32) : OOB;
      st_sc(rW, o, 0, gran(t[0], t[1], T));
      st_sc(rW, o + 16, 0, gran(t[2], t[3], T));
      STAMP(20);
      if (h < 18) {
        const int oo = (h / 9) * 1152 + (72 * j + 8 * (h % 9)) * 2 + (lane & 3) * 4;
        const unsigned xres = *(const GAS unsigned*)(xg + oo);
        poll2(rW, lane * 16, YG_OFF + (j * 18 + h) * 2048, T, p0, p1);
        float y0 = u2f(p0.x) + u2f(p1.x), y1 = u2f(p0.z) + u2f(p1.z);
        y0 += dpp<ROR4>(y0); y1 += dpp<ROR4>(y1);
        y0 += dpp<XOR8>(y0); y1 += dpp<XOR8>(y1);
        y0 = sum32x(sum16x(y0)); y1 = sum32x(sum16x(y1));
        if (lane < 4) *(GAS unsigned*)(out + oo) = cvtpk(u2f(xres << 16) + y0, u2f(xres & 0xffff0000u) + y1);
        if (bid == 0 && lane == 0) __builtin_amdgcn_raw_buffer_store_b32(T, rW, E_OFF, 0, AUX_SC1);
        STAMP(15);
      }
    }
    return;
  }

  const bool isA = wave == 4;
  STAMP(isA ? 0 : 8);
  v4u w[36];
  v3u xr[3];
  unsigned E = 0;
  const int u = (r & 15) + 16 * (xc & 3), qq = (r >> 4) + 2 * (xc >> 2);
  const int gm = j & 1, half = (j >> 1) & 1, rh = j >> 2;
  const unsigned stride = isA ? 8192 : 9216, cofs = isA ? u * 128 : hp * 288 + half * 144;
  int row0 = isA ? 144 * qq : 288 * rh;
  const rsrc_t rG = mk((isA ? wq : gm ? wu : wg) + cofs, 576 * stride - cofs);
  const unsigned gvo = (isA && c == 8 ? OOB : tvo) + g * stride;
  if (isA) {
    __builtin_amdgcn_s_setprio(3);
    E = __builtin_amdgcn_raw_buffer_load_b32(rW, E_OFF, 0, AUX_SC1);
    if (!ABAR) BARRIER();
  } else {
    __builtin_amdgcn_s_setprio(2);
    const rsrc_t rX = mk(xg, 2304);
#pragma unroll
    for (int k = 0; k < 3; ++k) xr[k] = __builtin_amdgcn_raw_buffer_load_b96(rX, (lane >> 5) * 1152 + (lane & 31) * 12, k * 384, 0);
    if (lane < 12) L[L_FL + lane] = 0u;
    LDS_FENCE();
    BARRIER();
        float xv[18];
#pragma unroll
        for (int k = 0; k < 3; ++k)
#pragma unroll
          for (int e = 0; e < 3; ++e) { xv[k * 6 + 2 * e] = u2f(xr[k][e] << 16); xv[k * 6 + 2 * e + 1] = u2f(xr[k][e] & 0xffff0000u); }
        float sum = 0.f;
#pragma unroll
        for (int i = 0; i < 18; ++i) sum += xv[i];
        const float mean = sum16x(rowsum(sum)) * (1.0f / 576.0f);
        float sq = 0.f;
#pragma unroll
        for (int i = 0; i < 18; ++i) { const float d = xv[i] - mean; sq = fmaf(d, d, sq); }
        const float var = sum16x(rowsum(sq)) * (1.0f / 576.0f);
        const float mb = rbf(mean), sd = rbf(__builtin_sqrtf(rbf(rbf(var) + 1e-5f))), rs = 1.0f / sd;
        LAS v2u* dst = (LAS v2u*)(L + L_XN + (lane >> 5) * XN_S + (lane & 31) * 6);
#pragma unroll
        for (int k = 0; k < 3; ++k)
#pragma unroll
          for (int e = 0; e < 3; ++e) {
            float n[2];
#pragma unroll
            for (int f = 0; f < 2; ++f) { const float d = rbf(xv[k * 6 + 2 * e + f] - mb); const float q0 = d * rs; n[f] = fmaf(fmaf(-q0, sd, d), rs, q0); }
            dst[k * 96 + e] = v2u{cvtpk(n[0], n[0]), cvtpk(n[1], n[1])};
          }
        lds_signal(L + L_FL + F_XN, lane);
        STAMP(9);
    __builtin_amdgcn_s_setprio(0);
  }
  unsigned T = 0;
  f32x4 ga = {0.f, 0.f, 0.f, 0.f};
#pragma unroll 1
  for (int t = 0; t < (isA ? 1 : 2); ++t, row0 += 144) {
    unsigned so = row0 * stride;
#pragma unroll
    for (int i = 0; i < 36; ++i) { w[i] = ld_w(rG, gvo, so); so += 4 * stride; }
    SB();
    if (isA) {
      if (ABAR) BARRIER();
      T = __builtin_amdgcn_readfirstlane(E) + 0x9E3779B9u;
      if (lane == 0) L[L_FL + F_T] = T;
      lds_signal(L + L_FL + F_TOK, lane);
      lds_wait(L + L_FL + F_XN);
      STAMP(1);
    }
    ga = gemv<36>(ga, L + L_XN + (c >> 3) * XN_S + row0 + g, am, w);
  }
  if (isA) {
    const float s2 = dpp<XOR8>(ga[2]), s3 = dpp<XOR8>(ga[3]);
    const unsigned o = QG_OFF + (((u >> 1) * 2 + (g >> 1)) * 4 + qq) * 1024 + ((u & 1) * 64 + (c & 7) * 8 + (g & 1) * 4 + (c >> 3) * 2) * 8;
    st_sc(rW, o, 0, gran(c < 8 ? ga[0] : s2, c < 8 ? ga[1] : s3, T));
    STAMP(2);
    __builtin_amdgcn_s_sleep(QDELAY);
    float q0, q1;
#pragma unroll 1
    for (int spin = 0; spin < SPIN_CAP; ++spin) {
      MEMBAR();
#pragma unroll
      for (int i = 0; i < 4; ++i) w[i] = ld_sc(rW, lane * 16, QG_OFF + ((h * 2 + b) * 4 + i) * 1024);
      q0 = (u2f(w[0].x) + u2f(w[1].x)) + (u2f(w[2].x) + u2f(w[3].x));
      q1 = (u2f(w[0].z) + u2f(w[1].z)) + (u2f(w[2].z) + u2f(w[3].z));
      if (none_bad(tagbad(w[0], T) | tagbad(w[1], T) | tagbad(w[2], T) | tagbad(w[3], T))) break;
      STAMP(21);
      __builtin_amdgcn_s_sleep(16);
    }
    L[L_QB + lane] = cvtpk(q0, q1);
    lds_signal(L + L_FL + F_Q, lane);
    STAMP(3);
    __builtin_amdgcn_s_setprio(0);
    const unsigned cv = __builtin_amdgcn_raw_buffer_load_b32(mk((const void*)(pc0 & ~255ull), CODE_BYTES), (r * 8 + (lane & 7)) * 128, 0, 0);
    asm volatile("" :: "v"(cv));
    lds_wait4(L + L_FL + F_KD, lane);
    float mw[4], lw[4];
#pragma unroll
    for (int i = 0; i < 4; ++i) { mw[i] = Lf[L_ML + 2 * i]; lw[i] = Lf[L_ML + 2 * i + 1]; }
    const float mg = fmaxf(fmaxf(mw[0], mw[1]), fmaxf(mw[2], mw[3]));
    float lg = 0.f;
#pragma unroll
    for (int i = 0; i < 4; ++i) lg += lw[i] * ex2((mw[i] - mg) * LOG2E);
    const unsigned mlb = ML_OFF + (b * 32 + h) * 128;
    st_sc(rW, lane == 0 ? mlb + s * 16 : OOB, 0, gran(mg, lg, T));
    v4u v;
#pragma unroll 1
    for (int spin = 0; spin < SPIN_CAP; ++spin) {
      __builtin_amdgcn_s_sleep(100);
      MEMBAR();
      v = ld_sc(rW, (lane & 3) * 16, mlb);
      if (none_bad(tagbad(v, T))) break;
    }
    const float ms = u2f(v.x);
    float M = fmaxf(ms, dpp<XOR1>(ms)); M = fmaxf(M, dpp<XOR2>(M));
    float Ls = u2f(v.z) * ex2((ms - M) * LOG2E);
    Ls += dpp<XOR1>(Ls); Ls += dpp<XOR2>(Ls);
    const float inv = 1.0f / Ls;
    if (lane == 0) *(LAS f32x4*)(Lf + L_FW) = f32x4{ex2((mw[0] - M) * LOG2E) * inv, ex2((mw[1] - M) * LOG2E) * inv, ex2((mw[2] - M) * LOG2E) * inv, ex2((mw[3] - M) * LOG2E) * inv};
    lds_signal(L + L_FL + F_FW, lane);
    STAMP(5);
  } else {
    lds_wait(L + L_FL + F_TOK);
    T = __builtin_amdgcn_readfirstlane(L[L_FL + F_T]);
    const unsigned o = c < 9 ? (unsigned)(GU_OFF + hp * 9216 + ((rh * 2 + gm) * 2 + (g >> 1)) * 1152 + (half * 72 + c * 8 + (g & 1) * 4) * 8) : OOB;
    st_sc(rW, o, 0, gran(ga[0], ga[1], T));
    st_sc(rW, o + 16, 0, gran(ga[2], ga[3], T));
    STAMP(12);
    lds_wait4(L + L_FL + F_KD, lane);
#pragma unroll 1
    for (int spin = 0; spin < SPIN_CAP; ++spin) {
      MEMBAR();
      unsigned bad = 0;
#pragma unroll
      for (int q4 = 0; q4 < 4; ++q4)
#pragma unroll
        for (int i = 0; i < 3; ++i) w[q4 * 3 + i] = ld_sc(rW, (i < 2 || lane < 16) ? (unsigned)(lane * 16) : OOB, GU_OFF + hp * 9216 + q4 * 2304 + i * 1024);
#pragma unroll
      for (int q4 = 0; q4 < 4; ++q4) bad |= tagbad(w[q4 * 3], T) | tagbad(w[q4 * 3 + 1], T) | (lane < 16 ? tagbad(w[q4 * 3 + 2], T) : 0u);
      if (none_bad(bad)) break;
      __builtin_amdgcn_s_sleep(32);
    }
#pragma unroll
    for (int i = 0; i < 3; ++i) {
      unsigned hv[2];
#pragma unroll
      for (int e = 0; e < 2; ++e) {
        const float gt = rbf(u2f(w[i][2 * e]) + u2f(w[6 + i][2 * e])), up = rbf(u2f(w[3 + i][2 * e]) + u2f(w[9 + i][2 * e]));
        const float t2 = 1.5957691216057308f * (gt + 0.044715f * gt * gt * gt);
        const float hh = rbf(gt / (1.0f + ex2(-t2 * LOG2E))) * up;
        hv[e] = cvtpk(hh, hh);
      }
      const int e0 = (i * 64 + lane) * 2;
      const int bb = e0 >= 144 ? 1 : 0;
      if (i < 2 || lane < 16) *(LAS v2u*)(L + L_HD + bb * HD_S + e0 - bb * 144) = v2u{hv[0], hv[1]};
    }
    lds_signal(L + L_FL + F_HD, lane);
    STAMP(13);
  }
}

extern "C" void kernel_launch(void* const* d_in, const int* in_sizes, int n_in, void* d_out, int out_size, void* d_ws, size_t ws_size,
                              hipStream_t stream) {
  (void)hipFuncSetAttribute((const void*)fused_decode, hipFuncAttributeMaxDynamicSharedMemorySize, LDS_BYTES);
  fused_decode<<<256, 384, LDS_BYTES, stream>>>((const char*)d_in[0], (const char*)d_in[1], (const char*)d_in[2], (const char*)d_in[3],
                                                (const char*)d_in[4], (const char*)d_in[6], (const char*)d_in[7], (const char*)d_in[8],
                                                (char*)d_out, (char*)d_ws);
}
```

```cpp
#include <hip/hip_runtime.h>
#include <hip/hip_bf16.h>

#define WIN 16
#define NPRE 2
#define PINFL 6
#define TAUK 240
#define TAUV 216
#define LSTEP 80
#define WC0 3
#define WC1 4
#define WC2 5
#define WC3 7
#define WC4 9
#define WC5 12
#define QDELAY 32
#define PPOLL 0
#define TBAR 1
#define TPOS 0
#define MLSLEEP 80
#define DIAG 0

#define GAS __attribute__((address_space(1)))
#define LAS __attribute__((address_space(3)))
typedef unsigned v4u __attribute__((ext_vector_type(4)));
typedef unsigned v3u __attribute__((ext_vector_type(3)));
typedef unsigned v2u __attribute__((ext_vector_type(2)));
typedef float f32x4 __attribute__((ext_vector_type(4)));
typedef float f32x2 __attribute__((ext_vector_type(2)));
typedef __bf16 bf16x8 __attribute__((ext_vector_type(8)));
typedef __bf16 bf16x2 __attribute__((ext_vector_type(2)));
typedef __amdgpu_buffer_rsrc_t rsrc_t;

constexpr int E_OFF = 0;
constexpr int QG_OFF = 4096;
constexpr int ML_OFF = QG_OFF + 262144;
constexpr int GU_OFF = ML_OFF + 4096;
constexpr int OG_OFF = GU_OFF + 163840;
constexpr int YG_OFF = OG_OFF + 262144;
constexpr int WS_USED = YG_OFF + 294912;
constexpr int TS_OFF = 4 << 20;

constexpr int L_XN = 0, XN_S = 580;
constexpr int L_HD = 1160, HD_S = 148;
constexpr int L_QB = L_HD + 2 * HD_S;
constexpr int L_OW = L_QB + 64;
constexpr int L_YW = L_OW + 512;
constexpr int L_ML = L_YW + 1056;
constexpr int L_FW = L_ML + 8;
constexpr int L_FL = L_FW + 4;
constexpr int F_KD = 0, F_Q = 4, F_T = 5, F_TOK = 6, F_XN = 7, F_FW = 8;
constexpr int L_END = L_FL + 12;
static_assert(L_FL % 4 == 0 && L_OW % 4 == 0 && L_YW % 4 == 0 && L_QB % 4 == 0 && L_FW % 4 == 0, "16-byte aligned LDS pieces");

constexpr int AUX_NT = 2, AUX_SC1 = 16;
constexpr unsigned OOB = 0x80000000u;
constexpr unsigned CODE_BYTES = 17920;
constexpr int SPIN_CAP = 1 << 18;
constexpr float LOG2E = 1.4426950408889634f;

__device__ __forceinline__ rsrc_t mk(const void* p, unsigned bytes) { return __builtin_amdgcn_make_buffer_rsrc((void*)p, 0, bytes, 0x00020000); }
__device__ __forceinline__ v4u ld_nt(rsrc_t r, unsigned vo, unsigned so) { return __builtin_amdgcn_raw_buffer_load_b128(r, vo, so, AUX_NT); }
__device__ __forceinline__ v4u ld_sc(rsrc_t r, unsigned vo, unsigned so) { return __builtin_amdgcn_raw_buffer_load_b128(r, vo, so, AUX_SC1); }
__device__ __forceinline__ void st_sc(rsrc_t r, unsigned vo, unsigned so, v4u v) { __builtin_amdgcn_raw_buffer_store_b128(v, r, vo, so, AUX_SC1); }
__device__ __forceinline__ float u2f(unsigned u) { return __builtin_bit_cast(float, u); }
__device__ __forceinline__ unsigned f2u(float f) { return __builtin_bit_cast(unsigned, f); }
__device__ __forceinline__ unsigned cvtpk(float lo, float hi) {
  bf16x2 r = {(__bf16)lo, (__bf16)hi};
  return __builtin_bit_cast(unsigned, r);
}
__device__ __forceinline__ float rbf(float f) { return (float)(__bf16)f; }
__device__ __forceinline__ float ex2(float f) { return __builtin_amdgcn_exp2f(f); }
__device__ __forceinline__ float dot2(unsigned a, unsigned b, float c) {
  return __builtin_amdgcn_fdot2_f32_bf16(__builtin_bit_cast(bf16x2, a), __builtin_bit_cast(bf16x2, b), c, false);
}
__device__ __forceinline__ f32x4 mfma(v4u a, v4u b, f32x4 c) {
  return __builtin_amdgcn_mfma_f32_16x16x32_bf16(__builtin_bit_cast(bf16x8, a), __builtin_bit_cast(bf16x8, b), c, 0, 0, 0);
}
template <int CTRL> __device__ __forceinline__ float dpp(float x) { return u2f(__builtin_amdgcn_mov_dpp(f2u(x), CTRL, 0xf, 0xf, true)); }
template <int CTRL> __device__ __forceinline__ unsigned dppu(unsigned x) { return __builtin_amdgcn_mov_dpp(x, CTRL, 0xf, 0xf, true); }
constexpr int XOR1 = 0xB1, XOR2 = 0x4E, XOR7 = 0x141, XOR8 = 0x128, ROR4 = 0x124, BCAST = 0x150;
__device__ __forceinline__ float add16(float a, float b) {
  auto s = __builtin_amdgcn_permlane16_swap(f2u(a), f2u(b), false, false); return u2f(s[0]) + u2f(s[1]); }
__device__ __forceinline__ float add32(float a, float b) {
  auto s = __builtin_amdgcn_permlane32_swap(f2u(a), f2u(b), false, false); return u2f(s[0]) + u2f(s[1]); }
__device__ __forceinline__ float sum16x(float x) { return add16(x, x); }
__device__ __forceinline__ float sum32x(float x) { return add32(x, x); }
__device__ __forceinline__ float max16x(float x) { auto s = __builtin_amdgcn_permlane16_swap(f2u(x), f2u(x), false, false); return fmaxf(u2f(s[0]), u2f(s[1])); }
__device__ __forceinline__ float max32x(float x) { auto s = __builtin_amdgcn_permlane32_swap(f2u(x), f2u(x), false, false); return fmaxf(u2f(s[0]), u2f(s[1])); }
__device__ __forceinline__ float rowsum(float x) { x += dpp<XOR1>(x); x += dpp<XOR2>(x); x += dpp<XOR7>(x); x += dpp<XOR8>(x); return x; }
__device__ __forceinline__ float rowmax(float x) { x = fmaxf(x, dpp<XOR1>(x)); x = fmaxf(x, dpp<XOR2>(x)); x = fmaxf(x, dpp<XOR7>(x)); x = fmaxf(x, dpp<XOR8>(x)); return x; }
#define SB() __builtin_amdgcn_sched_barrier(0)
#define MEMBAR() asm volatile("" ::: "memory")
#define LDS_FENCE() asm volatile("s_waitcnt lgkmcnt(0)" ::: "memory")
#define BARRIER() do { SB(); asm volatile("s_barrier" ::: "memory"); SB(); } while (0)
#define LDS_BARRIER() do { SB(); asm volatile("s_waitcnt lgkmcnt(0)\n\ts_barrier" ::: "memory"); SB(); } while (0)
#define VMWAIT(n) asm volatile("s_waitcnt vmcnt(%0)" :: "i"(n) : "memory")
__device__ __forceinline__ unsigned lds_ld(LAS unsigned* p) { return __hip_atomic_load(p, __ATOMIC_RELAXED, __HIP_MEMORY_SCOPE_WORKGROUP); }
__device__ __forceinline__ void lds_wait(LAS unsigned* p) {
  while (__builtin_amdgcn_readfirstlane(lds_ld(p)) == 0) __builtin_amdgcn_s_sleep(1);
  MEMBAR();
}
__device__ __forceinline__ void lds_wait4(LAS unsigned* p, int lane) {
  while (__builtin_amdgcn_ballot_w64(lds_ld(p + (lane & 3)) == 0) != 0ull) __builtin_amdgcn_s_sleep(4);
  MEMBAR();
}
__device__ __forceinline__ void lds_signal(LAS unsigned* p, int lane) {
  LDS_FENCE();
  if (lane == 0) __hip_atomic_store(p, 1u, __ATOMIC_RELAXED, __HIP_MEMORY_SCOPE_WORKGROUP);
}
__device__ __forceinline__ bool none_bad(unsigned bad) { return __builtin_amdgcn_ballot_w64(bad != 0u) == 0ull; }
__device__ __forceinline__ unsigned tagbad(const v4u& v, unsigned T) { return (v.y ^ T) | (v.w ^ T); }
__device__ __forceinline__ v4u gran(float a, float b, unsigned T) { return v4u{f2u(a), T, f2u(b), T}; }
__device__ __forceinline__ v4u afrag(unsigned dup, const v4u& m) { return v4u{dup & m.x, dup & m.y, dup & m.z, dup & m.w}; }
template <int D> __device__ __forceinline__ void poll2(rsrc_t rW, unsigned vo, unsigned so, unsigned T, v4u& r0, v4u& r1) {
#pragma unroll 1
  for (int spin = 0; spin < SPIN_CAP; ++spin) {
    MEMBAR();
    r0 = ld_sc(rW, vo, so); r1 = ld_sc(rW, vo, so + D);
    if (none_bad(tagbad(r0, T) | tagbad(r1, T))) break;
  }
}

#define STAMP(slot) do {} while (0)

#define PACE(t16) do { const unsigned need_ = tk0 + ((unsigned)(t16) >> 4); int lag_; \
    while ((lag_ = (int)((unsigned)__builtin_amdgcn_s_memrealtime() - need_)) < 0) __builtin_amdgcn_s_sleep(1); \
    const int cls_ = (int)(((unsigned)lag_ * (65536u / LSTEP)) >> 16); \
    asm volatile("s_cmp_gt_i32 %0, 5\n\ts_cbranch_scc1 1f\n\ts_waitcnt vmcnt(%1)\n\ts_cmp_eq_i32 %0, 5\n\ts_cbranch_scc1 1f\n\ts_waitcnt vmcnt(%2)\n\t" \
                 "s_cmp_eq_i32 %0, 4\n\ts_cbranch_scc1 1f\n\ts_waitcnt vmcnt(%3)\n\ts_cmp_eq_i32 %0, 3\n\ts_cbranch_scc1 1f\n\ts_waitcnt vmcnt(%4)\n\t" \
                 "s_cmp_eq_i32 %0, 2\n\ts_cbranch_scc1 1f\n\ts_waitcnt vmcnt(%5)\n\ts_cmp_eq_i32 %0, 1\n\ts_cbranch_scc1 1f\n\ts_waitcnt vmcnt(%6)\n1:" \
                 :: "s"(cls_), "i"(WC5 - 1), "i"(WC4 - 1), "i"(WC3 - 1), "i"(WC2 - 1), "i"(WC1 - 1), "i"(WC0 - 1) : "scc", "memory"); } while (0)

template <int N, int M> __device__ __forceinline__ f32x4 gemv(f32x4 a0, const unsigned (&xv)[N], const v4u& am, const v4u (&w)[M]) {
  f32x4 a1 = {0.f, 0.f, 0.f, 0.f};
#pragma unroll
  for (int i = 0; i < N; ++i) { if (i & 1) a1 = mfma(afrag(xv[i], am), w[i], a1); else a0 = mfma(afrag(xv[i], am), w[i], a0); }
  return a0 + a1;
}

__global__ void __launch_bounds__(384, 2)
fused_decode(const char* xg, const char* kc, const char* vc, const char* wq, const char* wo, const char* wg, const char* wu, const char* wd,
             char* out, char* ws) {
  __shared__ __attribute__((aligned(16))) unsigned smem[L_END];
  LAS unsigned* L = (LAS unsigned*)smem;
  LAS float* Lf = (LAS float*)smem;
  const unsigned tk0 = (unsigned)__builtin_amdgcn_s_memrealtime();
  const int bid = blockIdx.x, xc = bid & 7, r = bid >> 3;
  int lane = threadIdx.x & 63;
  const int wave = __builtin_amdgcn_readfirstlane(threadIdx.x >> 6);
  const unsigned cv = __builtin_amdgcn_raw_buffer_load_b32(mk((const void*)(__builtin_amdgcn_s_getpc() & ~255ull), CODE_BYTES), 0, min((unsigned)(r * 6 + wave) * 128u, CODE_BYTES - 128u), 0);
  const int h = xc + 8 * (r & 3), j = r >> 2, s = j & 3, b = j >> 2;
  const size_t cb = (size_t)((b * 32 + h) * 4 + s) * 524288;
  const unsigned vo = wave * 1024 + lane * 16;
  SB();

  if (__builtin_expect(wave < 4, 1)) {
    const int sw = wave;
    const char* kb = kc + cb;
    const rsrc_t rK = mk(kb, 524288);
    constexpr int NVB = 8 - NPRE;
    v4u a[WIN], pre[NPRE * 16];
#pragma unroll
    for (int i = 0; i < PINFL; ++i) a[i] = ld_nt(rK, vo, i * 4096);
    BARRIER();
    asm volatile("" : "+v"(lane));
    const char* vb = vc + cb;
    const rsrc_t rV = mk(vb, 524288);
#pragma unroll
    for (int i = 0; i < NPRE * 16; ++i) { VMWAIT(PINFL - 1); pre[i] = ld_nt(rV, vo, NVB * 65536 + i * 4096); SB(); }
#pragma unroll
    for (int i = PINFL; i < WIN; ++i) { VMWAIT(PINFL - 1); a[i] = ld_nt(rK, vo, i * 4096); SB(); }
    if (sw == 0) STAMP(16);
    const int g = lane >> 4, c = lane & 15;
    lds_wait(L + L_FL + F_Q);
    asm volatile("" :: "v"(cv));
    const v4u q = *(LAS v4u*)(L + L_QB + c * 4);
    if (sw == 0) STAMP(17);
    float sc[8];
#pragma unroll
    for (int i = 0; i < 8; ++i) sc[i] = 0.f;
#pragma unroll 1
    for (int blk = 0; blk < 8; ++blk) {
      const rsrc_t rN = mk(blk < 7 ? kb : vb, 524288);
      const unsigned sN = blk < 7 ? blk * 65536 + 65536 : 0;
      float x[16];
#pragma unroll
      for (int i = 0; i < 16; ++i) {
        const v4u k = a[i];
        float d = dot2(k.x, q.x, 0.f); d = dot2(k.y, q.y, d); d = dot2(k.z, q.z, d); d = dot2(k.w, q.w, d);
        x[i] = d;
        asm volatile("" : "+v"(x[i]));
        SB();
        if (i & 1) {
          PACE((NPRE * 16 + 7 + blk * 16 + i) * TAUK);
          a[i - 1] = ld_nt(rN, vo, sN + (i - 1) * 4096);
          a[i] = ld_nt(rN, vo, sN + i * 4096);
          SB();
        }
      }
#define TR_STEP(HALF, CTRL) _Pragma("unroll") for (int u = 0; u < (HALF); ++u) { const float lo_ = x[u], hi_ = x[u + (HALF)]; \
        const float t1_ = lo_ + dpp<CTRL>(lo_), t2_ = hi_ + dpp<CTRL>(hi_); x[u] = (c & (HALF)) ? t2_ : t1_; }
      TR_STEP(8, XOR8) TR_STEP(4, XOR7) TR_STEP(2, XOR2) TR_STEP(1, XOR1)
#undef TR_STEP
#pragma unroll
      for (int i = 0; i < 7; ++i) sc[i] = sc[i + 1];
      sc[7] = rbf(rbf(x[0]) * 0.08838834764831845f);
    }
    float m = sc[0];
#pragma unroll
    for (int i = 1; i < 8; ++i) m = fmaxf(m, sc[i]);
    m = max32x(max16x(rowmax(m)));
    float l = 0.f;
    unsigned pp[8];
#pragma unroll
    for (int i = 0; i < 8; ++i) { const float p = ex2((sc[i] - m) * LOG2E); l += p; pp[i] = cvtpk(p, p); }
    l = sum32x(sum16x(rowsum(l)));
    if (lane == 0) { Lf[L_ML + 2 * sw] = m; Lf[L_ML + 2 * sw + 1] = l; }
    lds_signal(L + L_FL + F_KD + sw, lane);
    if (sw == 0) STAMP(18);
    const unsigned hm = (c & 1) ? 0xffff0000u : 0x0000ffffu;
    const int mt = (c & 7) >> 1;
    const v4u am = {mt == 0 ? hm : 0u, mt == 1 ? hm : 0u, mt == 2 ? hm : 0u, mt == 3 ? hm : 0u};
    const v4u pm = {c < 8 ? am.x : 0u, c < 8 ? am.y : 0u, c < 8 ? am.z : 0u, c < 8 ? am.w : 0u};
    f32x4 acc = {0.f, 0.f, 0.f, 0.f};
#define PVM(I, P, V) { acc = mfma(afrag(dppu<BCAST + (I)>(P), pm), (V), acc); asm volatile("" : "+v"(acc)); SB(); }
#define PV2(I) { PVM(I, pp[0], a[I]) PVM((I) + 1, pp[0], a[(I) + 1]) PACE((NPRE * 16 + 128) * TAUK + (blk * 16 + (I) + 8) * TAUV); \
      a[I] = ld_nt(rV, vo, sN + (I) * 4096); a[(I) + 1] = ld_nt(rV, vo, sN + ((I) + 1) * 4096); SB(); }
#define PV16(P, A, O) PVM(0, P, A[O + 0]) PVM(1, P, A[O + 1]) PVM(2, P, A[O + 2]) PVM(3, P, A[O + 3]) PVM(4, P, A[O + 4]) PVM(5, P, A[O + 5]) \
      PVM(6, P, A[O + 6]) PVM(7, P, A[O + 7]) PVM(8, P, A[O + 8]) PVM(9, P, A[O + 9]) PVM(10, P, A[O + 10]) PVM(11, P, A[O + 11]) \
      PVM(12, P, A[O + 12]) PVM(13, P, A[O + 13]) PVM(14, P, A[O + 14]) PVM(15, P, A[O + 15])
    const unsigned pq0 = pp[NVB], pq1 = pp[NVB + 1];
#pragma unroll 1
    for (int blk = 0; blk < NVB - 1; ++blk) {
      const unsigned sN = blk * 65536 + 65536;
      PV2(0) PV2(2) PV2(4) PV2(6) PV2(8) PV2(10) PV2(12) PV2(14)
#pragma unroll
      for (int i = 0; i < NVB - 1; ++i) pp[i] = pp[i + 1];
      if (blk == 0) { PV16(pq0, pre, 0) }
      if (blk == 1) { PV16(pq1, pre, 16) }
    }
    const int hp = 4 * (h & 7) + (h >> 3);
    v4u wz[9], wy[8];
    const rsrc_t rD = mk(wd + (size_t)(144 * hp + 36 * sw) * 1152 + j * 144, 36 * 1152 - j * 144);
    const rsrc_t rO = mk(wo + (size_t)(128 * h + 32 * sw) * 1152 + j * 144, 32 * 1152 - j * 144);
    const unsigned to = (c < 9 ? (unsigned)(c * 16) : OOB) + g * 1152;
#define TILES(P) if (TPOS == (P)) { _Pragma("unroll") for (int i = 0; i < 9; ++i) wz[i] = ld_nt(rD, to, i * 4608); \
      _Pragma("unroll") for (int i = 0; i < 8; ++i) wy[i] = ld_nt(rO, to, i * 4608); SB(); }
    if (TBAR && TPOS == 0) BARRIER();
    TILES(0)
    const rsrc_t rW = mk(ws, WS_USED);
    lds_wait(L + L_FL + F_FW);
    const unsigned T = __builtin_amdgcn_readfirstlane(L[L_FL + F_T]);
    float fw = Lf[L_FW + sw];
    unsigned hx[9];
#pragma unroll
    for (int i = 0; i < 9; ++i) hx[i] = L[L_HD + (c >> 3) * HD_S + 36 * sw + g + 4 * i];
    const unsigned src = (c >> 3) ? 0x0302u : 0x0100u, sel = (c & 1) ? (src << 16) | 0x0c0cu : 0x0c0c0000u | src;
    v4u sl = {mt == 0 ? sel : 0x0c0c0c0cu, mt == 1 ? sel : 0x0c0c0c0cu, mt == 2 ? sel : 0x0c0c0c0cu, mt == 3 ? sel : 0x0c0c0c0cu};
    unsigned a_ow = (L_OW + sw * 128 + c * 8 + g * 4) * 4, a_or = (L_OW + sw * 32 + (c & 7) * 4 + (c >> 3) * 2) * 4, a_og = g == 0 ? c * 16 : OOB;
    const int yk = (sw & 1) * 16 + (lane >> 2), ykb = yk >= 9 ? 1 : 0;
    unsigned a_yr = (L_YW + ((ykb * 2 + ((lane & 3) >> 1)) * 16 + yk - ykb * 9) * 4 + (lane & 1) * 2) * 4, a_yg = (sw == 0 || lane < 8) ? lane * 16 : OOB;
    const int oo = (h >= 9 ? 1152 - 144 : 0) + 144 * j + 16 * h + (lane & 3) * 4;
    unsigned xres = 0;
    if (sw == 2 && h < 18) xres = *(const GAS unsigned*)(xg + oo);
    asm volatile("" : "+v"(fw), "+v"(sl), "+v"(a_ow), "+v"(a_or), "+v"(a_og), "+v"(a_yr), "+v"(a_yg));
    asm volatile("" : "+v"(hx[0]), "+v"(hx[1]), "+v"(hx[2]), "+v"(hx[3]), "+v"(hx[4]), "+v"(hx[5]), "+v"(hx[6]), "+v"(hx[7]), "+v"(hx[8]));
    SB();
    PVM(0, pp[0], a[0]) PVM(1, pp[0], a[1]) PVM(2, pp[0], a[2]) PVM(3, pp[0], a[3]) PVM(4, pp[0], a[4]) PVM(5, pp[0], a[5]) PVM(6, pp[0], a[6]) PVM(7, pp[0], a[7])
    PVM(8, pp[0], a[8]) PVM(9, pp[0], a[9]) PVM(10, pp[0], a[10]) PVM(11, pp[0], a[11]) TILES(12) PVM(12, pp[0], a[12]) TILES(13) PVM(13, pp[0], a[13]) TILES(14)
    PVM(14, pp[0], a[14]) TILES(15) PVM(15, pp[0], a[15])
    if (lane < 32) *(LAS f32x4*)((LAS char*)L + a_ow) = acc * fw;
    if (sw == 0) STAMP(19);
    TILES(16)
    LDS_BARRIER();
    {
      f32x2 t = *(LAS f32x2*)((LAS char*)L + a_or);
#pragma unroll
      for (int i = 1; i < 4; ++i) t += *(LAS f32x2*)((LAS char*)L + a_or + i * 512);
      st_sc(rW, a_og, OG_OFF + h * 8192 + sw * 2048 + j * 256, gran(t.x, t.y, T));
      SB();
    }
    if (sw == 0) STAMP(6);
    TILES(17)
    f32x4 y = gemv<9>(f32x4{0.f, 0.f, 0.f, 0.f}, hx, am, wz);
    asm volatile("" : "+v"(y));
    if (sw == 0) STAMP(25);
    v4u p0, p1;
    poll2<1024>(rW, lane * 16, OG_OFF + h * 8192 + sw * 2048, T, p0, p1);
    if (sw == 0) STAMP(7);
    {
      const float e = add32(add16(u2f(p0.x), u2f(p0.z)), add16(u2f(p1.x), u2f(p1.z)));
      auto eb = __builtin_amdgcn_permlane32_swap(f2u(e), f2u(e), false, false);
      unsigned pk = cvtpk(u2f(eb[0]), u2f(eb[1]));
      pk = __builtin_amdgcn_update_dpp(pk, pk, XOR8, 0xc, 0xf, false);
      f32x4 y1 = {0.f, 0.f, 0.f, 0.f};
#define YM(I, ACC) { const unsigned bc = dppu<BCAST + (I)>(pk); ACC = mfma(v4u{__builtin_amdgcn_perm(0u, bc, sl.x), __builtin_amdgcn_perm(0u, bc, sl.y), \
        __builtin_amdgcn_perm(0u, bc, sl.z), __builtin_amdgcn_perm(0u, bc, sl.w)}, wy[I], ACC); }
      YM(0, y) YM(1, y1) YM(2, y) YM(3, y1) YM(4, y) YM(5, y1) YM(6, y) YM(7, y1)
      y += y1;
    }
    *(LAS f32x4*)(Lf + L_YW + sw * 256 + lane * 4) = y;
    LDS_BARRIER();
    if (sw < 2) {
      f32x2 t = *(LAS f32x2*)((LAS char*)L + a_yr);
#pragma unroll
      for (int i = 1; i < 4; ++i) t += *(LAS f32x2*)((LAS char*)L + a_yr + i * 1024);
      st_sc(rW, a_yg, YG_OFF + (j * 32 + h) * 1152 + sw * 1024, gran(t.x, t.y, T));
      if (sw == 0) STAMP(20);
    } else if (sw == 2 && h < 18) {
      poll2<16 * 1152>(rW, (lane >> 2) * 1152 + (lane & 3) * 16, YG_OFF + j * 32 * 1152 + h * 64, T, p0, p1);
      float y0 = u2f(p0.x) + u2f(p1.x), y1 = u2f(p0.z) + u2f(p1.z);
      y0 += dpp<ROR4>(y0); y1 += dpp<ROR4>(y1);
      y0 += dpp<XOR8>(y0); y1 += dpp<XOR8>(y1);
      y0 = sum32x(add16(y0, y1));
      auto yy = __builtin_amdgcn_permlane16_swap(f2u(y0), f2u(y0), false, false);
      if (lane < 4) *(GAS unsigned*)(out + oo) = cvtpk(u2f(xres << 16) + u2f(yy[0]), u2f(xres & 0xffff0000u) + u2f(yy[1]));
      if (bid == 0 && lane == 0) __builtin_amdgcn_raw_buffer_store_b32(T, rW, E_OFF, 0, AUX_SC1);
      STAMP(15);
    }
    return;
  }

  const bool isA = wave == 4;
  STAMP(isA ? 0 : 8);
  const int g = lane >> 4, c = lane & 15;
  const int hp = 4 * (h & 7) + (h >> 3);
  const rsrc_t rW = mk(ws, WS_USED);
  v4u w[36];
  unsigned E = 0;
  const int u = (r & 15) + 16 * (xc & 3), qq = (r >> 4) + 2 * (xc >> 2);
  const int gm = j & 1, half = (j >> 1) & 1, rh = j >> 2;
  const unsigned stride = isA ? 8192 : 9216, cofs = isA ? u * 128 : hp * 288 + half * 144;
  int row0 = isA ? 144 * qq : 288 * rh;
  const rsrc_t rG = mk((isA ? wq : gm ? wu : wg) + cofs, 576 * stride - cofs);
  const unsigned gvo = (c < (isA ? 8 : 9) ? (unsigned)(c * 16) : OOB) + g * stride;
  if (isA) {
    __builtin_amdgcn_s_setprio(3);
    E = __builtin_amdgcn_raw_buffer_load_b32(rW, E_OFF, 0, AUX_SC1);
    BARRIER();
  } else {
    __builtin_amdgcn_s_setprio(2);
    v3u xr[3];
    const rsrc_t rX = mk(xg, 2304);
#pragma unroll
    for (int k = 0; k < 3; ++k) xr[k] = __builtin_amdgcn_raw_buffer_load_b96(rX, (lane >> 5) * 1152 + (lane & 31) * 12, k * 384, 0);
    if (lane < 12) L[L_FL + lane] = 0u;
    LDS_BARRIER();
    float xv[18];
#pragma unroll
    for (int k = 0; k < 3; ++k)
#pragma unroll
      for (int e = 0; e < 3; ++e) { xv[k * 6 + 2 * e] = u2f(xr[k][e] << 16); xv[k * 6 + 2 * e + 1] = u2f(xr[k][e] & 0xffff0000u); }
    float sum = 0.f;
#pragma unroll
    for (int i = 0; i < 18; ++i) sum += xv[i];
    const float mean = sum16x(rowsum(sum)) * (1.0f / 576.0f);
    float sq = 0.f;
#pragma unroll
    for (int i = 0; i < 18; ++i) { const float d = xv[i] - mean; sq = fmaf(d, d, sq); }
    const float var = sum16x(rowsum(sq)) * (1.0f / 576.0f);
    const float mb = rbf(mean), sd = rbf(__builtin_sqrtf(rbf(rbf(var) + 1e-5f))), rs = 1.0f / sd;
    LAS v2u* dst = (LAS v2u*)(L + L_XN + (lane >> 5) * XN_S + (lane & 31) * 6);
#pragma unroll
    for (int k = 0; k < 3; ++k)
#pragma unroll
      for (int e = 0; e < 3; ++e) {
        float n[2];
#pragma unroll
        for (int f = 0; f < 2; ++f) { const float d = rbf(xv[k * 6 + 2 * e + f] - mb); const float q0 = d * rs; n[f] = fmaf(fmaf(-q0, sd, d), rs, q0); }
        dst[k * 96 + e] = v2u{cvtpk(n[0], n[0]), cvtpk(n[1], n[1])};
      }
    lds_signal(L + L_FL + F_XN, lane);
    STAMP(9);
    __builtin_amdgcn_s_setprio(0);
  }
  const unsigned hm = (c & 1) ? 0xffff0000u : 0x0000ffffu;
  const int mt = (c & 7) >> 1;
  const v4u am = {mt == 0 ? hm : 0u, mt == 1 ? hm : 0u, mt == 2 ? hm : 0u, mt == 3 ? hm : 0u};
  unsigned T = 0;
  f32x4 ga = {0.f, 0.f, 0.f, 0.f};
#pragma unroll 1
  for (int t = 0; t < (isA ? 1 : 2); ++t, row0 += 144) {
    unsigned so = row0 * stride;
#pragma unroll
    for (int i = 0; i < 36; ++i) { w[i] = ld_nt(rG, gvo, so); so += 4 * stride; }
    SB();
    if (isA) {
      T = __builtin_amdgcn_readfirstlane(E) + 0x9E3779B9u;
      if (lane == 0) L[L_FL + F_T] = T;
      lds_wait(L + L_FL + F_XN);
      STAMP(1);
    }
    asm volatile("" :: "v"(cv));
    unsigned xv[36];
#pragma unroll
    for (int i = 0; i < 36; ++i) xv[i] = L[L_XN + (c >> 3) * XN_S + row0 + g + 4 * i];
    SB();
    ga = gemv<36>(ga, xv, am, w);
  }
  if (isA) {
    const float s2 = dpp<XOR8>(ga[2]), s3 = dpp<XOR8>(ga[3]);
    const unsigned o = QG_OFF + (((u >> 1) * 2 + (g >> 1)) * 4 + qq) * 1024 + ((u & 1) * 64 + (c & 7) * 8 + (g & 1) * 4 + (c >> 3) * 2) * 8;
    st_sc(rW, o, 0, gran(c < 8 ? ga[0] : s2, c < 8 ? ga[1] : s3, T));
    STAMP(2);
    __builtin_amdgcn_s_sleep(QDELAY);
    float q0, q1;
#pragma unroll 1
    for (int spin = 0; spin < SPIN_CAP; ++spin) {
      MEMBAR();
#pragma unroll
      for (int i = 0; i < 4; ++i) w[i] = ld_sc(rW, lane * 16, QG_OFF + ((h * 2 + b) * 4 + i) * 1024);
      q0 = (u2f(w[0].x) + u2f(w[1].x)) + (u2f(w[2].x) + u2f(w[3].x));
      q1 = (u2f(w[0].z) + u2f(w[1].z)) + (u2f(w[2].z) + u2f(w[3].z));
      if (none_bad(tagbad(w[0], T) | tagbad(w[1], T) | tagbad(w[2], T) | tagbad(w[3], T))) break;
      STAMP(21);
      __builtin_amdgcn_s_sleep(16);
    }
    L[L_QB + lane] = cvtpk(q0, q1);
    lds_signal(L + L_FL + F_Q, lane);
    STAMP(3);
  } else {
    st_sc(rW, c < 9 ? (unsigned)((g >> 1) * 288 + c * 32 + (g & 1) * 16) : OOB, GU_OFF + bid * 640, __builtin_bit_cast(v4u, ga));
    STAMP(12);
    lds_wait4(L + L_FL + F_KD, lane);
    T = __builtin_amdgcn_readfirstlane(L[L_FL + F_T]);
    float mw[4], lw[4];
#pragma unroll
    for (int i = 0; i < 4; ++i) { mw[i] = Lf[L_ML + 2 * i]; lw[i] = Lf[L_ML + 2 * i + 1]; }
    const float mg = fmaxf(fmaxf(mw[0], mw[1]), fmaxf(mw[2], mw[3]));
    float lg = 0.f;
#pragma unroll
    for (int i = 0; i < 4; ++i) lg += lw[i] * ex2((mw[i] - mg) * LOG2E);
    VMWAIT(0);
    st_sc(rW, lane == 0 ? ML_OFF + h * 128 + j * 16 : OOB, 0, gran(mg, lg, T));
    v4u v;
#pragma unroll 1
    for (int spin = 0; spin < SPIN_CAP; ++spin) {
      __builtin_amdgcn_s_sleep(MLSLEEP);
      MEMBAR();
      v = ld_sc(rW, ((lane & 7) ^ (b * 4)) * 16, ML_OFF + h * 128);
      if (none_bad(tagbad(v, T))) break;
    }
    MEMBAR();
#pragma unroll
    for (int i = 0; i < 8; ++i) w[i] = ld_sc(rW, lane < 36 ? (unsigned)(lane * 16) : OOB, GU_OFF + (i * 32 + h) * 640);
#pragma unroll
    for (int hf = 0; hf < 2; ++hf) {
      v4u hv;
#pragma unroll
      for (int e = 0; e < 4; ++e) {
        const float gt = rbf(u2f(w[2 * hf][e]) + u2f(w[4 + 2 * hf][e])), up = rbf(u2f(w[1 + 2 * hf][e]) + u2f(w[5 + 2 * hf][e]));
        const float t2 = 1.5957691216057308f * (gt + 0.044715f * gt * gt * gt);
        const float hh = rbf(gt / (1.0f + ex2(-t2 * LOG2E))) * up;
        hv[e] = cvtpk(hh, hh);
      }
      if (lane < 36) *(LAS v4u*)(L + L_HD + (lane >= 18 ? HD_S - 72 : 0) + hf * 72 + lane * 4) = hv;
    }
    STAMP(13);
    const float ms = u2f(v.x);
    float M = fmaxf(ms, dpp<XOR1>(ms)); M = fmaxf(M, dpp<XOR2>(M));
    float Ls = u2f(v.z) * ex2((ms - M) * LOG2E);
    Ls += dpp<XOR1>(Ls); Ls += dpp<XOR2>(Ls);
    const float inv = 1.0f / Ls;
    if (lane == 0) *(LAS f32x4*)(Lf + L_FW) = f32x4{ex2((mw[0] - M) * LOG2E) * inv, ex2((mw[1] - M) * LOG2E) * inv, ex2((mw[2] - M) * LOG2E) * inv, ex2((mw[3] - M) * LOG2E) * inv};
    lds_signal(L + L_FL + F_FW, lane);
    STAMP(5);
  }
}

extern "C" void kernel_launch(void* const* d_in, const int* in_sizes, int n_in, void* d_out, int out_size, void* d_ws, size_t ws_size,
                              hipStream_t stream) {
  fused_decode<<<256, 384, 0, stream>>>((const char*)d_in[0], (const char*)d_in[1], (const char*)d_in[2], (const char*)d_in[3],
                                        (const char*)d_in[4], (const char*)d_in[6], (const char*)d_in[7], (const char*)d_in[8],
                                        (char*)d_out, (char*)d_ws);
}
```

```cpp
#define PH 0
#include <hip/hip_runtime.h>
#include <hip/hip_bf16.h>

#define WIN 16
#define NPRE 2
#define PINFL 6
#define TAUK 240
#define TAUV 216
#define LSTEP 80
#define WC0 3
#define WC1 4
#define WC2 5
#define WC3 7
#define WC4 9
#define WC5 12
#define QDELAY 32
#define PPOLL 0
#define TBAR 1
#define TPOS 0
#define MLSLEEP 80
#define DIAG 0

#define GAS __attribute__((address_space(1)))
#define LAS __attribute__((address_space(3)))
typedef unsigned v4u __attribute__((ext_vector_type(4)));
typedef unsigned v3u __attribute__((ext_vector_type(3)));
typedef unsigned v2u __attribute__((ext_vector_type(2)));
typedef float f32x4 __attribute__((ext_vector_type(4)));
typedef float f32x2 __attribute__((ext_vector_type(2)));
typedef __bf16 bf16x8 __attribute__((ext_vector_type(8)));
typedef __bf16 bf16x2 __attribute__((ext_vector_type(2)));
typedef __amdgpu_buffer_rsrc_t rsrc_t;

constexpr int E_OFF = 0;
constexpr int QG_OFF = 4096;
constexpr int ML_OFF = QG_OFF + 262144;
constexpr int GU_OFF = ML_OFF + 4096;
constexpr int OG_OFF = GU_OFF + 163840;
constexpr int YG_OFF = OG_OFF + 262144;
constexpr int WS_USED = YG_OFF + 294912;
constexpr int TS_OFF = 4 << 20;

constexpr int L_XN = 0, XN_S = 580;
constexpr int L_HD = 1160, HD_S = 148;
constexpr int L_QB = L_HD + 2 * HD_S;
constexpr int L_OW = L_QB + 64;
constexpr int L_YW = L_OW + 512;
constexpr int L_ML = L_YW + 1056;
constexpr int L_FW = L_ML + 8;
constexpr int L_FL = L_FW + 4;
constexpr int F_KD = 0, F_Q = 4, F_T = 5, F_TOK = 6, F_XN = 7, F_FW = 8;
constexpr int L_END = L_FL + 12;
static_assert(L_FL % 4 == 0 && L_OW % 4 == 0 && L_YW % 4 == 0 && L_QB % 4 == 0 && L_FW % 4 == 0, "16-byte aligned LDS pieces");

constexpr int AUX_NT = 2, AUX_SC1 = 16;
constexpr unsigned OOB = 0x80000000u;
constexpr unsigned CODE_BYTES = 17920;
constexpr int SPIN_CAP = 1 << 18;
constexpr float LOG2E = 1.4426950408889634f;

__device__ __forceinline__ rsrc_t mk(const void* p, unsigned bytes) { return __builtin_amdgcn_make_buffer_rsrc((void*)p, 0, bytes, 0x00020000); }
__device__ __forceinline__ v4u ld_nt(rsrc_t r, unsigned vo, unsigned so) { return __builtin_amdgcn_raw_buffer_load_b128(r, vo, so, AUX_NT); }
__device__ __forceinline__ v4u ld_sc(rsrc_t r, unsigned vo, unsigned so) { return __builtin_amdgcn_raw_buffer_load_b128(r, vo, so, AUX_SC1); }
__device__ __forceinline__ void st_sc(rsrc_t r, unsigned vo, unsigned so, v4u v) { __builtin_amdgcn_raw_buffer_store_b128(v, r, vo, so, AUX_SC1); }
__device__ __forceinline__ float u2f(unsigned u) { return __builtin_bit_cast(float, u); }
__device__ __forceinline__ unsigned f2u(float f) { return __builtin_bit_cast(unsigned, f); }
__device__ __forceinline__ unsigned cvtpk(float lo, float hi) {
  bf16x2 r = {(__bf16)lo, (__bf16)hi};
  return __builtin_bit_cast(unsigned, r);
}
__device__ __forceinline__ float rbf(float f) { return (float)(__bf16)f; }
__device__ __forceinline__ float ex2(float f) { return __builtin_amdgcn_exp2f(f); }
__device__ __forceinline__ float dot2(unsigned a, unsigned b, float c) {
  return __builtin_amdgcn_fdot2_f32_bf16(__builtin_bit_cast(bf16x2, a), __builtin_bit_cast(bf16x2, b), c, false);
}
__device__ __forceinline__ f32x4 mfma(v4u a, v4u b, f32x4 c) {
  return __builtin_amdgcn_mfma_f32_16x16x32_bf16(__builtin_bit_cast(bf16x8, a), __builtin_bit_cast(bf16x8, b), c, 0, 0, 0);
}
template <int CTRL> __device__ __forceinline__ float dpp(float x) { return u2f(__builtin_amdgcn_mov_dpp(f2u(x), CTRL, 0xf, 0xf, true)); }
template <int CTRL> __device__ __forceinline__ unsigned dppu(unsigned x) { return __builtin_amdgcn_mov_dpp(x, CTRL, 0xf, 0xf, true); }
constexpr int XOR1 = 0xB1, XOR2 = 0x4E, XOR7 = 0x141, XOR8 = 0x128, ROR4 = 0x124, BCAST = 0x150;
__device__ __forceinline__ float add16(float a, float b) {
  auto s = __builtin_amdgcn_permlane16_swap(f2u(a), f2u(b), false, false); return u2f(s[0]) + u2f(s[1]); }
__device__ __forceinline__ float add32(float a, float b) {
  auto s = __builtin_amdgcn_permlane32_swap(f2u(a), f2u(b), false, false); return u2f(s[0]) + u2f(s[1]); }
__device__ __forceinline__ float sum16x(float x) { return add16(x, x); }
__device__ __forceinline__ float sum32x(float x) { return add32(x, x); }
__device__ __forceinline__ float max16x(float x) { auto s = __builtin_amdgcn_permlane16_swap(f2u(x), f2u(x), false, false); return fmaxf(u2f(s[0]), u2f(s[1])); }
__device__ __forceinline__ float max32x(float x) { auto s = __builtin_amdgcn_permlane32_swap(f2u(x), f2u(x), false, false); return fmaxf(u2f(s[0]), u2f(s[1])); }
__device__ __forceinline__ float rowsum(float x) { x += dpp<XOR1>(x); x += dpp<XOR2>(x); x += dpp<XOR7>(x); x += dpp<XOR8>(x); return x; }
__device__ __forceinline__ float rowmax(float x) { x = fmaxf(x, dpp<XOR1>(x)); x = fmaxf(x, dpp<XOR2>(x)); x = fmaxf(x, dpp<XOR7>(x)); x = fmaxf(x, dpp<XOR8>(x)); return x; }
#define SB() __builtin_amdgcn_sched_barrier(0)
#define MEMBAR() asm volatile("" ::: "memory")
#define LDS_FENCE() asm volatile("s_waitcnt lgkmcnt(0)" ::: "memory")
#define BARRIER() do { SB(); asm volatile("s_barrier" ::: "memory"); SB(); } while (0)
#define LDS_BARRIER() do { SB(); asm volatile("s_waitcnt lgkmcnt(0)\n\ts_barrier" ::: "memory"); SB(); } while (0)
#define VMWAIT(n) asm volatile("s_waitcnt vmcnt(%0)" :: "i"(n) : "memory")
__device__ __forceinline__ unsigned lds_ld(LAS unsigned* p) { return __hip_atomic_load(p, __ATOMIC_RELAXED, __HIP_MEMORY_SCOPE_WORKGROUP); }
__device__ __forceinline__ void lds_wait(LAS unsigned* p) {
  while (__builtin_amdgcn_readfirstlane(lds_ld(p)) == 0) __builtin_amdgcn_s_sleep(1);
  MEMBAR();
}
__device__ __forceinline__ void lds_wait4(LAS unsigned* p, int lane) {
  while (__builtin_amdgcn_ballot_w64(lds_ld(p + (lane & 3)) == 0) != 0ull) __builtin_amdgcn_s_sleep(4);
  MEMBAR();
}
__device__ __forceinline__ void lds_signal(LAS unsigned* p, int lane) {
  LDS_FENCE();
  if (lane == 0) __hip_atomic_store(p, 1u, __ATOMIC_RELAXED, __HIP_MEMORY_SCOPE_WORKGROUP);
}
__device__ __forceinline__ bool none_bad(unsigned bad) { return __builtin_amdgcn_ballot_w64(bad != 0u) == 0ull; }
__device__ __forceinline__ unsigned tagbad(const v4u& v, unsigned T) { return (v.y ^ T) | (v.w ^ T); }
__device__ __forceinline__ v4u gran(float a, float b, unsigned T) { return v4u{f2u(a), T, f2u(b), T}; }
__device__ __forceinline__ v4u afrag(unsigned dup, const v4u& m) { return v4u{dup & m.x, dup & m.y, dup & m.z, dup & m.w}; }
template <int D> __device__ __forceinline__ void poll2(rsrc_t rW, unsigned vo, unsigned so, unsigned T, v4u& r0, v4u& r1) {
#pragma unroll 1
  for (int spin = 0; spin < SPIN_CAP; ++spin) {
    MEMBAR();
    r0 = ld_sc(rW, vo, so); r1 = ld_sc(rW, vo, so + D);
    if (none_bad(tagbad(r0, T) | tagbad(r1, T))) break;
  }
}

#define STAMP(slot) do {} while (0)

#define PACE(t16) do { const unsigned need_ = tk0 + ((unsigned)(t16) >> 4); int lag_; \
    while ((lag_ = (int)((unsigned)__builtin_amdgcn_s_memrealtime() - need_)) < 0) __builtin_amdgcn_s_sleep(1); \
    const int cls_ = (int)(((unsigned)lag_ * (65536u / LSTEP)) >> 16); \
    asm volatile("s_cmp_gt_i32 %0, 5\n\ts_cbranch_scc1 1f\n\ts_waitcnt vmcnt(%1)\n\ts_cmp_eq_i32 %0, 5\n\ts_cbranch_scc1 1f\n\ts_waitcnt vmcnt(%2)\n\t" \
                 "s_cmp_eq_i32 %0, 4\n\ts_cbranch_scc1 1f\n\ts_waitcnt vmcnt(%3)\n\ts_cmp_eq_i32 %0, 3\n\ts_cbranch_scc1 1f\n\ts_waitcnt vmcnt(%4)\n\t" \
                 "s_cmp_eq_i32 %0, 2\n\ts_cbranch_scc1 1f\n\ts_waitcnt vmcnt(%5)\n\ts_cmp_eq_i32 %0, 1\n\ts_cbranch_scc1 1f\n\ts_waitcnt vmcnt(%6)\n1:" \
                 :: "s"(cls_), "i"(WC5 - 1), "i"(WC4 - 1), "i"(WC3 - 1), "i"(WC2 - 1), "i"(WC1 - 1), "i"(WC0 - 1) : "scc", "memory"); } while (0)

template <int N, int M> __device__ __forceinline__ f32x4 gemv(f32x4 a0, const unsigned (&xv)[N], const v4u& am, const v4u (&w)[M]) {
  f32x4 a1 = {0.f, 0.f, 0.f, 0.f};
#pragma unroll
  for (int i = 0; i < N; ++i) { if (i & 1) a1 = mfma(afrag(xv[i], am), w[i], a1); else a0 = mfma(afrag(xv[i], am), w[i], a0); }
  return a0 + a1;
}

__global__ void __launch_bounds__(384, 2)
fused_decode(const char* xg, const char* kc, const char* vc, const char* wq, const char* wo, const char* wg, const char* wu, const char* wd,
             char* out, char* ws) {
  __shared__ __attribute__((aligned(16))) unsigned smem[L_END];
  LAS unsigned* L = (LAS unsigned*)smem;
  LAS float* Lf = (LAS float*)smem;
  const unsigned tk0 = (unsigned)__builtin_amdgcn_s_memrealtime();
  unsigned tph = tk0;
  const int bid = blockIdx.x, xc = bid & 7, r = bid >> 3;
  int lane = threadIdx.x & 63;
  const int wave = __builtin_amdgcn_readfirstlane(threadIdx.x >> 6);
  const unsigned cv = __builtin_amdgcn_raw_buffer_load_b32(mk((const void*)(__builtin_amdgcn_s_getpc() & ~255ull), CODE_BYTES), 0, min((unsigned)(r * 6 + wave) * 128u, CODE_BYTES - 128u), 0);
  const int h = xc + 8 * (r & 3), j = r >> 2, s = j & 3, b = j >> 2;
  const size_t cb = (size_t)((b * 32 + h) * 4 + s) * 524288;
  const unsigned vo = wave * 1024 + lane * 16;
  SB();

  if (__builtin_expect(wave < 4, 1)) {
    const int sw = wave;
    const char* kb = kc + cb;
    const rsrc_t rK = mk(kb, 524288);
    constexpr int NVB = 8 - NPRE;
    v4u a[WIN], pre[NPRE * 16];
#pragma unroll
    for (int i = 0; i < PINFL; ++i) a[i] = ld_nt(rK, vo, i * 4096);
    BARRIER();
    asm volatile("" : "+v"(lane));
    const char* vb = vc + cb;
    const rsrc_t rV = mk(vb, 524288);
#pragma unroll
    for (int i = 0; i < NPRE * 16; ++i) { VMWAIT(PINFL - 1); if (PH == 24 && i == 0) tph = (unsigned)__builtin_amdgcn_s_memrealtime(); pre[i] = ld_nt(rV, vo, NVB * 65536 + i * 4096); SB(); }
#pragma unroll
    for (int i = PINFL; i < WIN; ++i) { VMWAIT(PINFL - 1); a[i] = ld_nt(rK, vo, i * 4096); SB(); }
    if (PH == 16) tph = (unsigned)__builtin_amdgcn_s_memrealtime();
    const int g = lane >> 4, c = lane & 15;
    lds_wait(L + L_FL + F_Q);
    asm volatile("" :: "v"(cv));
    const v4u q = *(LAS v4u*)(L + L_QB + c * 4);
    if (PH == 17) tph = (unsigned)__builtin_amdgcn_s_memrealtime();
    float sc[8];
#pragma unroll
    for (int i = 0; i < 8; ++i) sc[i] = 0.f;
#pragma unroll 1
    for (int blk = 0; blk < 8; ++blk) {
      const rsrc_t rN = mk(blk < 7 ? kb : vb, 524288);
      const unsigned sN = blk < 7 ? blk * 65536 + 65536 : 0;
      float x[16];
#pragma unroll
      for (int i = 0; i < 16; ++i) {
        const v4u k = a[i];
        float d = dot2(k.x, q.x, 0.f); d = dot2(k.y, q.y, d); d = dot2(k.z, q.z, d); d = dot2(k.w, q.w, d);
        x[i] = d;
        asm volatile("" : "+v"(x[i]));
        SB();
        if (i & 1) {
          PACE((NPRE * 16 + 7 + blk * 16 + i) * TAUK);
          a[i - 1] = ld_nt(rN, vo, sN + (i - 1) * 4096);
          a[i] = ld_nt(rN, vo, sN + i * 4096);
          SB();
        }
      }
#define TR_STEP(HALF, CTRL) _Pragma("unroll") for (int u = 0; u < (HALF); ++u) { const float lo_ = x[u], hi_ = x[u + (HALF)]; \
        const float t1_ = lo_ + dpp<CTRL>(lo_), t2_ = hi_ + dpp<CTRL>(hi_); x[u] = (c & (HALF)) ? t2_ : t1_; }
      TR_STEP(8, XOR8) TR_STEP(4, XOR7) TR_STEP(2, XOR2) TR_STEP(1, XOR1)
#undef TR_STEP
#pragma unroll
      for (int i = 0; i < 7; ++i) sc[i] = sc[i + 1];
      sc[7] = rbf(rbf(x[0]) * 0.08838834764831845f);
    }
    float m = sc[0];
#pragma unroll
    for (int i = 1; i < 8; ++i) m = fmaxf(m, sc[i]);
    m = max32x(max16x(rowmax(m)));
    float l = 0.f;
    unsigned pp[8];
#pragma unroll
    for (int i = 0; i < 8; ++i) { const float p = ex2((sc[i] - m) * LOG2E); l += p; pp[i] = cvtpk(p, p); }
    l = sum32x(sum16x(rowsum(l)));
    if (lane == 0) { Lf[L_ML + 2 * sw] = m; Lf[L_ML + 2 * sw + 1] = l; }
    lds_signal(L + L_FL + F_KD + sw, lane);
    if (PH == 18) tph = (unsigned)__builtin_amdgcn_s_memrealtime();
    const unsigned hm = (c & 1) ? 0xffff0000u : 0x0000ffffu;
    const int mt = (c & 7) >> 1;
    const v4u am = {mt == 0 ? hm : 0u, mt == 1 ? hm : 0u, mt == 2 ? hm : 0u, mt == 3 ? hm : 0u};
    const v4u pm = {c < 8 ? am.x : 0u, c < 8 ? am.y : 0u, c < 8 ? am.z : 0u, c < 8 ? am.w : 0u};
    f32x4 acc = {0.f, 0.f, 0.f, 0.f};
#define PVM(I, P, V) { acc = mfma(afrag(dppu<BCAST + (I)>(P), pm), (V), acc); asm volatile("" : "+v"(acc)); SB(); }
#define PV2(I) { PVM(I, pp[0], a[I]) PVM((I) + 1, pp[0], a[(I) + 1]) PACE((NPRE * 16 + 128) * TAUK + (blk * 16 + (I) + 8) * TAUV); \
      a[I] = ld_nt(rV, vo, sN + (I) * 4096); a[(I) + 1] = ld_nt(rV, vo, sN + ((I) + 1) * 4096); SB(); }
#define PV16(P, A, O) PVM(0, P, A[O + 0]) PVM(1, P, A[O + 1]) PVM(2, P, A[O + 2]) PVM(3, P, A[O + 3]) PVM(4, P, A[O + 4]) PVM(5, P, A[O + 5]) \
      PVM(6, P, A[O + 6]) PVM(7, P, A[O + 7]) PVM(8, P, A[O + 8]) PVM(9, P, A[O + 9]) PVM(10, P, A[O + 10]) PVM(11, P, A[O + 11]) \
      PVM(12, P, A[O + 12]) PVM(13, P, A[O + 13]) PVM(14, P, A[O + 14]) PVM(15, P, A[O + 15])
    const unsigned pq0 = pp[NVB], pq1 = pp[NVB + 1];
#pragma unroll 1
    for (int blk = 0; blk < NVB - 1; ++blk) {
      const unsigned sN = blk * 65536 + 65536;
      PV2(0) PV2(2) PV2(4) PV2(6) PV2(8) PV2(10) PV2(12) PV2(14)
#pragma unroll
      for (int i = 0; i < NVB - 1; ++i) pp[i] = pp[i + 1];
      if (blk == 0) { PV16(pq0, pre, 0) }
      if (blk == 1) { PV16(pq1, pre, 16) }
    }
    const int hp = 4 * (h & 7) + (h >> 3);
    v4u wz[9], wy[8];
    const rsrc_t rD = mk(wd + (size_t)(144 * hp + 36 * sw) * 1152 + j * 144, 36 * 1152 - j * 144);
    const rsrc_t rO = mk(wo + (size_t)(128 * h + 32 * sw) * 1152 + j * 144, 32 * 1152 - j * 144);
    const unsigned to = (c < 9 ? (unsigned)(c * 16) : OOB) + g * 1152;
#define TILES(P) if (TPOS == (P)) { _Pragma("unroll") for (int i = 0; i < 9; ++i) wz[i] = ld_nt(rD, to, i * 4608); \
      _Pragma("unroll") for (int i = 0; i < 8; ++i) wy[i] = ld_nt(rO, to, i * 4608); SB(); }
    if (TBAR && TPOS == 0) BARRIER();
    TILES(0)
    const rsrc_t rW = mk(ws, WS_USED);
    lds_wait(L + L_FL + F_FW);
    const unsigned T = __builtin_amdgcn_readfirstlane(L[L_FL + F_T]);
    float fw = Lf[L_FW + sw];
    unsigned hx[9];
#pragma unroll
    for (int i = 0; i < 9; ++i) hx[i] = L[L_HD + (c >> 3) * HD_S + 36 * sw + g + 4 * i];
    const unsigned src = (c >> 3) ? 0x0302u : 0x0100u, sel = (c & 1) ? (src << 16) | 0x0c0cu : 0x0c0c0000u | src;
    v4u sl = {mt == 0 ? sel : 0x0c0c0c0cu, mt == 1 ? sel : 0x0c0c0c0cu, mt == 2 ? sel : 0x0c0c0c0cu, mt == 3 ? sel : 0x0c0c0c0cu};
    unsigned a_ow = (L_OW + sw * 128 + c * 8 + g * 4) * 4, a_or = (L_OW + sw * 32 + (c & 7) * 4 + (c >> 3) * 2) * 4, a_og = g == 0 ? c * 16 : OOB;
    const int yk = (sw & 1) * 16 + (lane >> 2), ykb = yk >= 9 ? 1 : 0;
    unsigned a_yr = (L_YW + ((ykb * 2 + ((lane & 3) >> 1)) * 16 + yk - ykb * 9) * 4 + (lane & 1) * 2) * 4, a_yg = (sw == 0 || lane < 8) ? lane * 16 : OOB;
    const int oo = (h >= 9 ? 1152 - 144 : 0) + 144 * j + 16 * h + (lane & 3) * 4;
    unsigned xres = 0;
    if (sw == 2 && h < 18) xres = *(const GAS unsigned*)(xg + oo);
    asm volatile("" : "+v"(fw), "+v"(sl), "+v"(a_ow), "+v"(a_or), "+v"(a_og), "+v"(a_yr), "+v"(a_yg));
    asm volatile("" : "+v"(hx[0]), "+v"(hx[1]), "+v"(hx[2]), "+v"(hx[3]), "+v"(hx[4]), "+v"(hx[5]), "+v"(hx[6]), "+v"(hx[7]), "+v"(hx[8]));
    SB();
    PVM(0, pp[0], a[0]) PVM(1, pp[0], a[1]) PVM(2, pp[0], a[2]) PVM(3, pp[0], a[3]) PVM(4, pp[0], a[4]) PVM(5, pp[0], a[5]) PVM(6, pp[0], a[6]) PVM(7, pp[0], a[7])
    PVM(8, pp[0], a[8]) PVM(9, pp[0], a[9]) PVM(10, pp[0], a[10]) PVM(11, pp[0], a[11]) TILES(12) PVM(12, pp[0], a[12]) TILES(13) PVM(13, pp[0], a[13]) TILES(14)
    PVM(14, pp[0], a[14]) TILES(15) PVM(15, pp[0], a[15])
    if (lane < 32) *(LAS f32x4*)((LAS char*)L + a_ow) = acc * fw;
    if (PH == 19) tph = (unsigned)__builtin_amdgcn_s_memrealtime();
    TILES(16)
    LDS_BARRIER();
    {
      f32x2 t = *(LAS f32x2*)((LAS char*)L + a_or);
#pragma unroll
      for (int i = 1; i < 4; ++i) t += *(LAS f32x2*)((LAS char*)L + a_or + i * 512);
      st_sc(rW, a_og, OG_OFF + h * 8192 + sw * 2048 + j * 256, gran(t.x, t.y, T));
      SB();
    }
    if (sw == 0) STAMP(6);
    TILES(17)
    f32x4 y = gemv<9>(f32x4{0.f, 0.f, 0.f, 0.f}, hx, am, wz);
    asm volatile("" : "+v"(y));
    if (sw == 0) STAMP(25);
    v4u p0, p1;
    poll2<1024>(rW, lane * 16, OG_OFF + h * 8192 + sw * 2048, T, p0, p1);
    if (sw == 0) STAMP(7);
    {
      const float e = add32(add16(u2f(p0.x), u2f(p0.z)), add16(u2f(p1.x), u2f(p1.z)));
      auto eb = __builtin_amdgcn_permlane32_swap(f2u(e), f2u(e), false, false);
      unsigned pk = cvtpk(u2f(eb[0]), u2f(eb[1]));
      pk = __builtin_amdgcn_update_dpp(pk, pk, XOR8, 0xc, 0xf, false);
      f32x4 y1 = {0.f, 0.f, 0.f, 0.f};
#define YM(I, ACC) { const unsigned bc = dppu<BCAST + (I)>(pk); ACC = mfma(v4u{__builtin_amdgcn_perm(0u, bc, sl.x), __builtin_amdgcn_perm(0u, bc, sl.y), \
        __builtin_amdgcn_perm(0u, bc, sl.z), __builtin_amdgcn_perm(0u, bc, sl.w)}, wy[I], ACC); }
      YM(0, y) YM(1, y1) YM(2, y) YM(3, y1) YM(4, y) YM(5, y1) YM(6, y) YM(7, y1)
      y += y1;
    }
    *(LAS f32x4*)(Lf + L_YW + sw * 256 + lane * 4) = y;
    LDS_BARRIER();
    if (sw < 2) {
      f32x2 t = *(LAS f32x2*)((LAS char*)L + a_yr);
#pragma unroll
      for (int i = 1; i < 4; ++i) t += *(LAS f32x2*)((LAS char*)L + a_yr + i * 1024);
      st_sc(rW, a_yg, YG_OFF + (j * 32 + h) * 1152 + sw * 1024, gran(t.x, t.y, T));
      if (sw == 0) STAMP(20);
    } else if (sw == 2 && h < 18) {
      poll2<16 * 1152>(rW, (lane >> 2) * 1152 + (lane & 3) * 16, YG_OFF + j * 32 * 1152 + h * 64, T, p0, p1);
      float y0 = u2f(p0.x) + u2f(p1.x), y1 = u2f(p0.z) + u2f(p1.z);
      y0 += dpp<ROR4>(y0); y1 += dpp<ROR4>(y1);
      y0 += dpp<XOR8>(y0); y1 += dpp<XOR8>(y1);
      y0 = sum32x(add16(y0, y1));
      auto yy = __builtin_amdgcn_permlane16_swap(f2u(y0), f2u(y0), false, false);
      if (lane < 4) *(GAS unsigned*)(out + oo) = cvtpk(u2f(xres << 16) + u2f(yy[0]), u2f(xres & 0xffff0000u) + u2f(yy[1]));
      if (bid == 0 && lane == 0) __builtin_amdgcn_raw_buffer_store_b32(T, rW, E_OFF, 0, AUX_SC1);
      STAMP(15);
    }
    if (PH == 15) tph = (unsigned)__builtin_amdgcn_s_memrealtime();
    while ((unsigned)__builtin_amdgcn_s_memrealtime() - tph < 8000u) __builtin_amdgcn_s_sleep(2);
    return;
  }

  const bool isA = wave == 4;
  STAMP(isA ? 0 : 8);
  const int g = lane >> 4, c = lane & 15;
  const int hp = 4 * (h & 7) + (h >> 3);
  const rsrc_t rW = mk(ws, WS_USED);
  v4u w[36];
  unsigned E = 0;
  const int u = (r & 15) + 16 * (xc & 3), qq = (r >> 4) + 2 * (xc >> 2);
  const int gm = j & 1, half = (j >> 1) & 1, rh = j >> 2;
  const unsigned stride = isA ? 8192 : 9216, cofs = isA ? u * 128 : hp * 288 + half * 144;
  int row0 = isA ? 144 * qq : 288 * rh;
  const rsrc_t rG = mk((isA ? wq : gm ? wu : wg) + cofs, 576 * stride - cofs);
  const unsigned gvo = (c < (isA ? 8 : 9) ? (unsigned)(c * 16) : OOB) + g * stride;
  if (isA) {
    __builtin_amdgcn_s_setprio(3);
    E = __builtin_amdgcn_raw_buffer_load_b32(rW, E_OFF, 0, AUX_SC1);
    BARRIER();
  } else {
    __builtin_amdgcn_s_setprio(2);
    v3u xr[3];
    const rsrc_t rX = mk(xg, 2304);
#pragma unroll
    for (int k = 0; k < 3; ++k) xr[k] = __builtin_amdgcn_raw_buffer_load_b96(rX, (lane >> 5) * 1152 + (lane & 31) * 12, k * 384, 0);
    if (lane < 12) L[L_FL + lane] = 0u;
    LDS_BARRIER();
    float xv[18];
#pragma unroll
    for (int k = 0; k < 3; ++k)
#pragma unroll
      for (int e = 0; e < 3; ++e) { xv[k * 6 + 2 * e] = u2f(xr[k][e] << 16); xv[k * 6 + 2 * e + 1] = u2f(xr[k][e] & 0xffff0000u); }
    float sum = 0.f;
#pragma unroll
    for (int i = 0; i < 18; ++i) sum += xv[i];
    const float mean = sum16x(rowsum(sum)) * (1.0f / 576.0f);
    float sq = 0.f;
#pragma unroll
    for (int i = 0; i < 18; ++i) { const float d = xv[i] - mean; sq = fmaf(d, d, sq); }
    const float var = sum16x(rowsum(sq)) * (1.0f / 576.0f);
    const float mb = rbf(mean), sd = rbf(__builtin_sqrtf(rbf(rbf(var) + 1e-5f))), rs = 1.0f / sd;
    LAS v2u* dst = (LAS v2u*)(L + L_XN + (lane >> 5) * XN_S + (lane & 31) * 6);
#pragma unroll
    for (int k = 0; k < 3; ++k)
#pragma unroll
      for (int e = 0; e < 3; ++e) {
        float n[2];
#pragma unroll
        for (int f = 0; f < 2; ++f) { const float d = rbf(xv[k * 6 + 2 * e + f] - mb); const float q0 = d * rs; n[f] = fmaf(fmaf(-q0, sd, d), rs, q0); }
        dst[k * 96 + e] = v2u{cvtpk(n[0], n[0]), cvtpk(n[1], n[1])};
      }
    lds_signal(L + L_FL + F_XN, lane);
    STAMP(9);
    __builtin_amdgcn_s_setprio(0);
  }
  const unsigned hm = (c & 1) ? 0xffff0000u : 0x0000ffffu;
  const int mt = (c & 7) >> 1;
  const v4u am = {mt == 0 ? hm : 0u, mt == 1 ? hm : 0u, mt == 2 ? hm : 0u, mt == 3 ? hm : 0u};
  unsigned T = 0;
  f32x4 ga = {0.f, 0.f, 0.f, 0.f};
#pragma unroll 1
  for (int t = 0; t < (isA ? 1 : 2); ++t, row0 += 144) {
    unsigned so = row0 * stride;
#pragma unroll
    for (int i = 0; i < 36; ++i) { w[i] = ld_nt(rG, gvo, so); so += 4 * stride; }
    SB();
    if (isA) {
      T = __builtin_amdgcn_readfirstlane(E) + 0x9E3779B9u;
      if (lane == 0) L[L_FL + F_T] = T;
      lds_wait(L + L_FL + F_XN);
      STAMP(1);
    }
    asm volatile("" :: "v"(cv));
    unsigned xv[36];
#pragma unroll
    for (int i = 0; i < 36; ++i) xv[i] = L[L_XN + (c >> 3) * XN_S + row0 + g + 4 * i];
    SB();
    ga = gemv<36>(ga, xv, am, w);
  }
  if (isA) {
    const float s2 = dpp<XOR8>(ga[2]), s3 = dpp<XOR8>(ga[3]);
    const unsigned o = QG_OFF + (((u >> 1) * 2 + (g >> 1)) * 4 + qq) * 1024 + ((u & 1) * 64 + (c & 7) * 8 + (g & 1) * 4 + (c >> 3) * 2) * 8;
    st_sc(rW, o, 0, gran(c < 8 ? ga[0] : s2, c < 8 ? ga[1] : s3, T));
    STAMP(2);
    __builtin_amdgcn_s_sleep(QDELAY);
    float q0, q1;
#pragma unroll 1
    for (int spin = 0; spin < SPIN_CAP; ++spin) {
      MEMBAR();
#pragma unroll
      for (int i = 0; i < 4; ++i) w[i] = ld_sc(rW, lane * 16, QG_OFF + ((h * 2 + b) * 4 + i) * 1024);
      q0 = (u2f(w[0].x) + u2f(w[1].x)) + (u2f(w[2].x) + u2f(w[3].x));
      q1 = (u2f(w[0].z) + u2f(w[1].z)) + (u2f(w[2].z) + u2f(w[3].z));
      if (none_bad(tagbad(w[0], T) | tagbad(w[1], T) | tagbad(w[2], T) | tagbad(w[3], T))) break;
      STAMP(21);
      __builtin_amdgcn_s_sleep(16);
    }
    L[L_QB + lane] = cvtpk(q0, q1);
    lds_signal(L + L_FL + F_Q, lane);
    STAMP(3);
  } else {
    st_sc(rW, c < 9 ? (unsigned)((g >> 1) * 288 + c * 32 + (g & 1) * 16) : OOB, GU_OFF + bid * 640, __builtin_bit_cast(v4u, ga));
    STAMP(12);
    lds_wait4(L + L_FL + F_KD, lane);
    T = __builtin_amdgcn_readfirstlane(L[L_FL + F_T]);
    float mw[4], lw[4];
#pragma unroll
    for (int i = 0; i < 4; ++i) { mw[i] = Lf[L_ML + 2 * i]; lw[i] = Lf[L_ML + 2 * i + 1]; }
    const float mg = fmaxf(fmaxf(mw[0], mw[1]), fmaxf(mw[2], mw[3]));
    float lg = 0.f;
#pragma unroll
    for (int i = 0; i < 4; ++i) lg += lw[i] * ex2((mw[i] - mg) * LOG2E);
    VMWAIT(0);
    st_sc(rW, lane == 0 ? ML_OFF + h * 128 + j * 16 : OOB, 0, gran(mg, lg, T));
    v4u v;
#pragma unroll 1
    for (int spin = 0; spin < SPIN_CAP; ++spin) {
      __builtin_amdgcn_s_sleep(MLSLEEP);
      MEMBAR();
      v = ld_sc(rW, ((lane & 7) ^ (b * 4)) * 16, ML_OFF + h * 128);
      if (none_bad(tagbad(v, T))) break;
    }
    MEMBAR();
#pragma unroll
    for (int i = 0; i < 8; ++i) w[i] = ld_sc(rW, lane < 36 ? (unsigned)(lane * 16) : OOB, GU_OFF + (i * 32 + h) * 640);
#pragma unroll
    for (int hf = 0; hf < 2; ++hf) {
      v4u hv;
#pragma unroll
      for (int e = 0; e < 4; ++e) {
        const float gt = rbf(u2f(w[2 * hf][e]) + u2f(w[4 + 2 * hf][e])), up = rbf(u2f(w[1 + 2 * hf][e]) + u2f(w[5 + 2 * hf][e]));
        const float t2 = 1.5957691216057308f * (gt + 0.044715f * gt * gt * gt);
        const float hh = rbf(gt / (1.0f + ex2(-t2 * LOG2E))) * up;
        hv[e] = cvtpk(hh, hh);
      }
      if (lane < 36) *(LAS v4u*)(L + L_HD + (lane >= 18 ? HD_S - 72 : 0) + hf * 72 + lane * 4) = hv;
    }
    STAMP(13);
    const float ms = u2f(v.x);
    float M = fmaxf(ms, dpp<XOR1>(ms)); M = fmaxf(M, dpp<XOR2>(M));
    float Ls = u2f(v.z) * ex2((ms - M) * LOG2E);
    Ls += dpp<XOR1>(Ls); Ls += dpp<XOR2>(Ls);
    const float inv = 1.0f / Ls;
    if (lane == 0) *(LAS f32x4*)(Lf + L_FW) = f32x4{ex2((mw[0] - M) * LOG2E) * inv, ex2((mw[1] - M) * LOG2E) * inv, ex2((mw[2] - M) * LOG2E) * inv, ex2((mw[3] - M) * LOG2E) * inv};
    lds_signal(L + L_FL + F_FW, lane);
    STAMP(5);
  }
}

extern "C" void kernel_launch(void* const* d_in, const int* in_sizes, int n_in, void* d_out, int out_size, void* d_ws, size_t ws_size,
                              hipStream_t stream) {
  fused_decode<<<256, 384, 0, stream>>>((const char*)d_in[0], (const char*)d_in[1], (const char*)d_in[2], (const char*)d_in[3],
                                        (const char*)d_in[4], (const char*)d_in[6], (const char*)d_in[7], (const char*)d_in[8],
                                        (char*)d_out, (char*)d_ws);
}
```

```cpp
#define TOUCH 0
#define LATEPF 1
#include <hip/hip_runtime.h>
#include <hip/hip_bf16.h>

#define WIN 16
#define NPRE 2
#define PINFL 6
#define TAUK 240
#define TAUV 216
#define LSTEP 80
#define WC0 3
#define WC1 4
#define WC2 5
#define WC3 7
#define WC4 9
#define WC5 12
#define QDELAY 32
#define PPOLL 0
#define TBAR 1
#define TPOS 0
#define MLSLEEP 80
#define OUTSC 0
#define DIAG 0

#define GAS __attribute__((address_space(1)))
#define LAS __attribute__((address_space(3)))
typedef unsigned v4u __attribute__((ext_vector_type(4)));
typedef unsigned v3u __attribute__((ext_vector_type(3)));
typedef unsigned v2u __attribute__((ext_vector_type(2)));
typedef float f32x4 __attribute__((ext_vector_type(4)));
typedef float f32x2 __attribute__((ext_vector_type(2)));
typedef __bf16 bf16x8 __attribute__((ext_vector_type(8)));
typedef __bf16 bf16x2 __attribute__((ext_vector_type(2)));
typedef __amdgpu_buffer_rsrc_t rsrc_t;

constexpr int E_OFF = 0;
constexpr int QG_OFF = 4096;
constexpr int ML_OFF = QG_OFF + 262144;
constexpr int GU_OFF = ML_OFF + 4096;
constexpr int OG_OFF = GU_OFF + 163840;
constexpr int YG_OFF = OG_OFF + 262144;
constexpr int WS_USED = YG_OFF + 294912;
constexpr int TS_OFF = 4 << 20;

constexpr int L_XN = 0, XN_S = 580;
constexpr int L_HD = 1160, HD_S = 148;
constexpr int L_QB = L_HD + 2 * HD_S;
constexpr int L_OW = L_QB + 64;
constexpr int L_YW = L_OW + 512;
constexpr int L_ML = L_YW + 1056;
constexpr int L_FW = L_ML + 8;
constexpr int L_FL = L_FW + 4;
constexpr int F_KD = 0, F_Q = 4, F_T = 5, F_TOK = 6, F_XN = 7, F_FW = 8;
constexpr int L_END = L_FL + 12;
static_assert(L_FL % 4 == 0 && L_OW % 4 == 0 && L_YW % 4 == 0 && L_QB % 4 == 0 && L_FW % 4 == 0, "16-byte aligned LDS pieces");

constexpr int AUX_NT = 2, AUX_SC1 = 16;
constexpr unsigned OOB = 0x80000000u;
constexpr unsigned CODE_BYTES = 17920;
constexpr int SPIN_CAP = 1 << 18;
constexpr float LOG2E = 1.4426950408889634f;

__device__ __forceinline__ rsrc_t mk(const void* p, unsigned bytes) { return __builtin_amdgcn_make_buffer_rsrc((void*)p, 0, bytes, 0x00020000); }
__device__ __forceinline__ v4u ld_nt(rsrc_t r, unsigned vo, unsigned so) { return __builtin_amdgcn_raw_buffer_load_b128(r, vo, so, AUX_NT); }
__device__ __forceinline__ v4u ld_sc(rsrc_t r, unsigned vo, unsigned so) { return __builtin_amdgcn_raw_buffer_load_b128(r, vo, so, AUX_SC1); }
__device__ __forceinline__ void st_sc(rsrc_t r, unsigned vo, unsigned so, v4u v) { __builtin_amdgcn_raw_buffer_store_b128(v, r, vo, so, AUX_SC1); }
__device__ __forceinline__ float u2f(unsigned u) { return __builtin_bit_cast(float, u); }
__device__ __forceinline__ unsigned f2u(float f) { return __builtin_bit_cast(unsigned, f); }
__device__ __forceinline__ unsigned cvtpk(float lo, float hi) {
  bf16x2 r = {(__bf16)lo, (__bf16)hi};
  return __builtin_bit_cast(unsigned, r);
}
__device__ __forceinline__ float rbf(float f) { return (float)(__bf16)f; }
__device__ __forceinline__ float ex2(float f) { return __builtin_amdgcn_exp2f(f); }
__device__ __forceinline__ float dot2(unsigned a, unsigned b, float c) {
  return __builtin_amdgcn_fdot2_f32_bf16(__builtin_bit_cast(bf16x2, a), __builtin_bit_cast(bf16x2, b), c, false);
}
__device__ __forceinline__ f32x4 mfma(v4u a, v4u b, f32x4 c) {
  return __builtin_amdgcn_mfma_f32_16x16x32_bf16(__builtin_bit_cast(bf16x8, a), __builtin_bit_cast(bf16x8, b), c, 0, 0, 0);
}
template <int CTRL> __device__ __forceinline__ float dpp(float x) { return u2f(__builtin_amdgcn_mov_dpp(f2u(x), CTRL, 0xf, 0xf, true)); }
template <int CTRL> __device__ __forceinline__ unsigned dppu(unsigned x) { return __builtin_amdgcn_mov_dpp(x, CTRL, 0xf, 0xf, true); }
constexpr int XOR1 = 0xB1, XOR2 = 0x4E, XOR7 = 0x141, XOR8 = 0x128, ROR4 = 0x124, BCAST = 0x150;
__device__ __forceinline__ float add16(float a, float b) {
  auto s = __builtin_amdgcn_permlane16_swap(f2u(a), f2u(b), false, false); return u2f(s[0]) + u2f(s[1]); }
__device__ __forceinline__ float add32(float a, float b) {
  auto s = __builtin_amdgcn_permlane32_swap(f2u(a), f2u(b), false, false); return u2f(s[0]) + u2f(s[1]); }
__device__ __forceinline__ float sum16x(float x) { return add16(x, x); }
__device__ __forceinline__ float sum32x(float x) { return add32(x, x); }
__device__ __forceinline__ float max16x(float x) { auto s = __builtin_amdgcn_permlane16_swap(f2u(x), f2u(x), false, false); return fmaxf(u2f(s[0]), u2f(s[1])); }
__device__ __forceinline__ float max32x(float x) { auto s = __builtin_amdgcn_permlane32_swap(f2u(x), f2u(x), false, false); return fmaxf(u2f(s[0]), u2f(s[1])); }
__device__ __forceinline__ float rowsum(float x) { x += dpp<XOR1>(x); x += dpp<XOR2>(x); x += dpp<XOR7>(x); x += dpp<XOR8>(x); return x; }
__device__ __forceinline__ float rowmax(float x) { x = fmaxf(x, dpp<XOR1>(x)); x = fmaxf(x, dpp<XOR2>(x)); x = fmaxf(x, dpp<XOR7>(x)); x = fmaxf(x, dpp<XOR8>(x)); return x; }
#define SB() __builtin_amdgcn_sched_barrier(0)
#define MEMBAR() asm volatile("" ::: "memory")
#define LDS_FENCE() asm volatile("s_waitcnt lgkmcnt(0)" ::: "memory")
#define BARRIER() do { SB(); asm volatile("s_barrier" ::: "memory"); SB(); } while (0)
#define LDS_BARRIER() do { SB(); asm volatile("s_waitcnt lgkmcnt(0)\n\ts_barrier" ::: "memory"); SB(); } while (0)
#define VMWAIT(n) asm volatile("s_waitcnt vmcnt(%0)" :: "i"(n) : "memory")
__device__ __forceinline__ unsigned lds_ld(LAS unsigned* p) { return __hip_atomic_load(p, __ATOMIC_RELAXED, __HIP_MEMORY_SCOPE_WORKGROUP); }
__device__ __forceinline__ void lds_wait(LAS unsigned* p) {
  while (__builtin_amdgcn_readfirstlane(lds_ld(p)) == 0) __builtin_amdgcn_s_sleep(1);
  MEMBAR();
}
__device__ __forceinline__ void lds_wait4(LAS unsigned* p, int lane) {
  while (__builtin_amdgcn_ballot_w64(lds_ld(p + (lane & 3)) == 0) != 0ull) __builtin_amdgcn_s_sleep(4);
  MEMBAR();
}
__device__ __forceinline__ void lds_signal(LAS unsigned* p, int lane) {
  LDS_FENCE();
  if (lane == 0) __hip_atomic_store(p, 1u, __ATOMIC_RELAXED, __HIP_MEMORY_SCOPE_WORKGROUP);
}
__device__ __forceinline__ bool none_bad(unsigned bad) { return __builtin_amdgcn_ballot_w64(bad != 0u) == 0ull; }
__device__ __forceinline__ unsigned tagbad(const v4u& v, unsigned T) { return (v.y ^ T) | (v.w ^ T); }
__device__ __forceinline__ v4u gran(float a, float b, unsigned T) { return v4u{f2u(a), T, f2u(b), T}; }
__device__ __forceinline__ v4u afrag(unsigned dup, const v4u& m) { return v4u{dup & m.x, dup & m.y, dup & m.z, dup & m.w}; }
template <int D> __device__ __forceinline__ void poll2(rsrc_t rW, unsigned vo, unsigned so, unsigned T, v4u& r0, v4u& r1) {
#pragma unroll 1
  for (int spin = 0; spin < SPIN_CAP; ++spin) {
    MEMBAR();
    r0 = ld_sc(rW, vo, so); r1 = ld_sc(rW, vo, so + D);
    if (none_bad(tagbad(r0, T) | tagbad(r1, T))) break;
  }
}

#define STAMP(slot) do {} while (0)

#define PACE(t16) do { const unsigned need_ = tk0 + ((unsigned)(t16) >> 4); int lag_; \
    while ((lag_ = (int)((unsigned)__builtin_amdgcn_s_memrealtime() - need_)) < 0) __builtin_amdgcn_s_sleep(1); \
    const int cls_ = (int)(((unsigned)lag_ * (65536u / LSTEP)) >> 16); \
    asm volatile("s_cmp_gt_i32 %0, 5\n\ts_cbranch_scc1 1f\n\ts_waitcnt vmcnt(%1)\n\ts_cmp_eq_i32 %0, 5\n\ts_cbranch_scc1 1f\n\ts_waitcnt vmcnt(%2)\n\t" \
                 "s_cmp_eq_i32 %0, 4\n\ts_cbranch_scc1 1f\n\ts_waitcnt vmcnt(%3)\n\ts_cmp_eq_i32 %0, 3\n\ts_cbranch_scc1 1f\n\ts_waitcnt vmcnt(%4)\n\t" \
                 "s_cmp_eq_i32 %0, 2\n\ts_cbranch_scc1 1f\n\ts_waitcnt vmcnt(%5)\n\ts_cmp_eq_i32 %0, 1\n\ts_cbranch_scc1 1f\n\ts_waitcnt vmcnt(%6)\n1:" \
                 :: "s"(cls_), "i"(WC5 - 1), "i"(WC4 - 1), "i"(WC3 - 1), "i"(WC2 - 1), "i"(WC1 - 1), "i"(WC0 - 1) : "scc", "memory"); } while (0)

template <int N, int M> __device__ __forceinline__ f32x4 gemv(f32x4 a0, const unsigned (&xv)[N], const v4u& am, const v4u (&w)[M]) {
  f32x4 a1 = {0.f, 0.f, 0.f, 0.f};
#pragma unroll
  for (int i = 0; i < N; ++i) { if (i & 1) a1 = mfma(afrag(xv[i], am), w[i], a1); else a0 = mfma(afrag(xv[i], am), w[i], a0); }
  return a0 + a1;
}

__global__ void __launch_bounds__(384, 2)
fused_decode(const char* xg, const char* kc, const char* vc, const char* wq, const char* wo, const char* wg, const char* wu, const char* wd,
             char* out, char* ws) {
  __shared__ __attribute__((aligned(16))) unsigned smem[L_END];
  LAS unsigned* L = (LAS unsigned*)smem;
  LAS float* Lf = (LAS float*)smem;
  const unsigned tk0 = (unsigned)__builtin_amdgcn_s_memrealtime();
  const int bid = blockIdx.x, xc = bid & 7, r = bid >> 3;
  int lane = threadIdx.x & 63;
  const int wave = __builtin_amdgcn_readfirstlane(threadIdx.x >> 6);
  const rsrc_t rC = mk((const void*)(__builtin_amdgcn_s_getpc() & ~255ull), CODE_BYTES);
  const unsigned cv = __builtin_amdgcn_raw_buffer_load_b32(rC, 0, min((unsigned)(r * 6 + wave) * 128u, CODE_BYTES - 128u), 0);
  const int h = xc + 8 * (r & 3), j = r >> 2, s = j & 3, b = j >> 2;
  const size_t cb = (size_t)((b * 32 + h) * 4 + s) * 524288;
  const unsigned vo = wave * 1024 + lane * 16;
  SB();

  if (__builtin_expect(wave < 4, 1)) {
    const int sw = wave;
    const char* kb = kc + cb;
    const rsrc_t rK = mk(kb, 524288);
    constexpr int NVB = 8 - NPRE;
    v4u a[WIN], pre[NPRE * 16];
#pragma unroll
    for (int i = 0; i < PINFL; ++i) a[i] = ld_nt(rK, vo, i * 4096);
    BARRIER();
    asm volatile("" : "+v"(lane));
    const char* vb = vc + cb;
    const rsrc_t rV = mk(vb, 524288);
#pragma unroll
    for (int i = 0; i < NPRE * 16; ++i) { VMWAIT(PINFL - 1); pre[i] = ld_nt(rV, vo, NVB * 65536 + i * 4096); SB(); }
#pragma unroll
    for (int i = PINFL; i < WIN; ++i) { VMWAIT(PINFL - 1); a[i] = ld_nt(rK, vo, i * 4096); SB(); }
    if (sw == 0) STAMP(16);
    const int g = lane >> 4, c = lane & 15;
    lds_wait(L + L_FL + F_Q);
    asm volatile("" :: "v"(cv));
    const v4u q = *(LAS v4u*)(L + L_QB + c * 4);
    if (sw == 0) STAMP(17);
    float sc[8];
#pragma unroll
    for (int i = 0; i < 8; ++i) sc[i] = 0.f;
#pragma unroll 1
    for (int blk = 0; blk < 8; ++blk) {
      const rsrc_t rN = mk(blk < 7 ? kb : vb, 524288);
      const unsigned sN = blk < 7 ? blk * 65536 + 65536 : 0;
      float x[16];
#pragma unroll
      for (int i = 0; i < 16; ++i) {
        const v4u k = a[i];
        float d = dot2(k.x, q.x, 0.f); d = dot2(k.y, q.y, d); d = dot2(k.z, q.z, d); d = dot2(k.w, q.w, d);
        x[i] = d;
        asm volatile("" : "+v"(x[i]));
        SB();
        if (i & 1) {
          PACE((NPRE * 16 + 7 + blk * 16 + i) * TAUK);
          a[i - 1] = ld_nt(rN, vo, sN + (i - 1) * 4096);
          a[i] = ld_nt(rN, vo, sN + i * 4096);
          SB();
        }
      }
#define TR_STEP(HALF, CTRL) _Pragma("unroll") for (int u = 0; u < (HALF); ++u) { const float lo_ = x[u], hi_ = x[u + (HALF)]; \
        const float t1_ = lo_ + dpp<CTRL>(lo_), t2_ = hi_ + dpp<CTRL>(hi_); x[u] = (c & (HALF)) ? t2_ : t1_; }
      TR_STEP(8, XOR8) TR_STEP(4, XOR7) TR_STEP(2, XOR2) TR_STEP(1, XOR1)
#undef TR_STEP
#pragma unroll
      for (int i = 0; i < 7; ++i) sc[i] = sc[i + 1];
      sc[7] = rbf(rbf(x[0]) * 0.08838834764831845f);
    }
    float m = sc[0];
#pragma unroll
    for (int i = 1; i < 8; ++i) m = fmaxf(m, sc[i]);
    m = max32x(max16x(rowmax(m)));
    float l = 0.f;
    unsigned pp[8];
#pragma unroll
    for (int i = 0; i < 8; ++i) { const float p = ex2((sc[i] - m) * LOG2E); l += p; pp[i] = cvtpk(p, p); }
    l = sum32x(sum16x(rowsum(l)));
    if (lane == 0) { Lf[L_ML + 2 * sw] = m; Lf[L_ML + 2 * sw + 1] = l; }
    lds_signal(L + L_FL + F_KD + sw, lane);
    if (sw == 0) STAMP(18);
    const unsigned hm = (c & 1) ? 0xffff0000u : 0x0000ffffu;
    const int mt = (c & 7) >> 1;
    const v4u am = {mt == 0 ? hm : 0u, mt == 1 ? hm : 0u, mt == 2 ? hm : 0u, mt == 3 ? hm : 0u};
    const v4u pm = {c < 8 ? am.x : 0u, c < 8 ? am.y : 0u, c < 8 ? am.z : 0u, c < 8 ? am.w : 0u};
    f32x4 acc = {0.f, 0.f, 0.f, 0.f};
#define PVM(I, P, V) { acc = mfma(afrag(dppu<BCAST + (I)>(P), pm), (V), acc); asm volatile("" : "+v"(acc)); SB(); }
#define PV2(I) { PVM(I, pp[0], a[I]) PVM((I) + 1, pp[0], a[(I) + 1]) PACE((NPRE * 16 + 128) * TAUK + (blk * 16 + (I) + 8) * TAUV); \
      a[I] = ld_nt(rV, vo, sN + (I) * 4096); a[(I) + 1] = ld_nt(rV, vo, sN + ((I) + 1) * 4096); SB(); }
#define PV16(P, A, O) PVM(0, P, A[O + 0]) PVM(1, P, A[O + 1]) PVM(2, P, A[O + 2]) PVM(3, P, A[O + 3]) PVM(4, P, A[O + 4]) PVM(5, P, A[O + 5]) \
      PVM(6, P, A[O + 6]) PVM(7, P, A[O + 7]) PVM(8, P, A[O + 8]) PVM(9, P, A[O + 9]) PVM(10, P, A[O + 10]) PVM(11, P, A[O + 11]) \
      PVM(12, P, A[O + 12]) PVM(13, P, A[O + 13]) PVM(14, P, A[O + 14]) PVM(15, P, A[O + 15])
    const unsigned pq0 = pp[NVB], pq1 = pp[NVB + 1];
#pragma unroll 1
    for (int blk = 0; blk < NVB - 1; ++blk) {
      const unsigned sN = blk * 65536 + 65536;
      PV2(0) PV2(2) PV2(4) PV2(6) PV2(8) PV2(10) PV2(12) PV2(14)
#pragma unroll
      for (int i = 0; i < NVB - 1; ++i) pp[i] = pp[i + 1];
      if (blk == 0) { PV16(pq0, pre, 0) }
      if (blk == 1) { PV16(pq1, pre, 16) }
    }
    const int hp = 4 * (h & 7) + (h >> 3);
    v4u wz[9], wy[8];
    const rsrc_t rD = mk(wd + (size_t)(144 * hp + 36 * sw) * 1152 + j * 144, 36 * 1152 - j * 144);
    const rsrc_t rO = mk(wo + (size_t)(128 * h + 32 * sw) * 1152 + j * 144, 32 * 1152 - j * 144);
    const unsigned to = (c < 9 ? (unsigned)(c * 16) : OOB) + g * 1152;
#define TILES(P) if (TPOS == (P)) { _Pragma("unroll") for (int i = 0; i < 9; ++i) wz[i] = ld_nt(rD, to, i * 4608); \
      _Pragma("unroll") for (int i = 0; i < 8; ++i) wy[i] = ld_nt(rO, to, i * 4608); SB(); }
    if (TBAR && TPOS == 0) BARRIER();
    TILES(0)
    const rsrc_t rW = mk(ws, WS_USED);
    lds_wait(L + L_FL + F_FW);
    const unsigned T = __builtin_amdgcn_readfirstlane(L[L_FL + F_T]);
    float fw = Lf[L_FW + sw];
    unsigned hx[9];
#pragma unroll
    for (int i = 0; i < 9; ++i) hx[i] = L[L_HD + (c >> 3) * HD_S + 36 * sw + g + 4 * i];
    const unsigned src = (c >> 3) ? 0x0302u : 0x0100u, sel = (c & 1) ? (src << 16) | 0x0c0cu : 0x0c0c0000u | src;
    v4u sl = {mt == 0 ? sel : 0x0c0c0c0cu, mt == 1 ? sel : 0x0c0c0c0cu, mt == 2 ? sel : 0x0c0c0c0cu, mt == 3 ? sel : 0x0c0c0c0cu};
    unsigned a_ow = (L_OW + sw * 128 + c * 8 + g * 4) * 4, a_or = (L_OW + sw * 32 + (c & 7) * 4 + (c >> 3) * 2) * 4, a_og = g == 0 ? c * 16 : OOB;
    const int yk = (sw & 1) * 16 + (lane >> 2), ykb = yk >= 9 ? 1 : 0;
    unsigned a_yr = (L_YW + ((ykb * 2 + ((lane & 3) >> 1)) * 16 + yk - ykb * 9) * 4 + (lane & 1) * 2) * 4, a_yg = (sw == 0 || lane < 8) ? lane * 16 : OOB;
    const int oo = (h >= 9 ? 1152 - 144 : 0) + 144 * j + 16 * h + (lane & 3) * 4;
    unsigned xres = 0;
    if (sw == 2 && h < 18) xres = *(const GAS unsigned*)(xg + oo);
    asm volatile("" : "+v"(fw), "+v"(sl), "+v"(a_ow), "+v"(a_or), "+v"(a_og), "+v"(a_yr), "+v"(a_yg));
    asm volatile("" : "+v"(hx[0]), "+v"(hx[1]), "+v"(hx[2]), "+v"(hx[3]), "+v"(hx[4]), "+v"(hx[5]), "+v"(hx[6]), "+v"(hx[7]), "+v"(hx[8]));
    SB();
    PVM(0, pp[0], a[0]) PVM(1, pp[0], a[1]) PVM(2, pp[0], a[2]) PVM(3, pp[0], a[3]) PVM(4, pp[0], a[4]) PVM(5, pp[0], a[5]) PVM(6, pp[0], a[6]) PVM(7, pp[0], a[7])
    PVM(8, pp[0], a[8]) PVM(9, pp[0], a[9]) PVM(10, pp[0], a[10]) PVM(11, pp[0], a[11]) TILES(12) PVM(12, pp[0], a[12]) TILES(13) PVM(13, pp[0], a[13]) TILES(14)
    PVM(14, pp[0], a[14]) TILES(15) PVM(15, pp[0], a[15])
    if (lane < 32) *(LAS f32x4*)((LAS char*)L + a_ow) = acc * fw;
    if (sw == 0) STAMP(19);
    TILES(16)
    LDS_BARRIER();
    {
      f32x2 t = *(LAS f32x2*)((LAS char*)L + a_or);
#pragma unroll
      for (int i = 1; i < 4; ++i) t += *(LAS f32x2*)((LAS char*)L + a_or + i * 512);
      st_sc(rW, a_og, OG_OFF + h * 8192 + sw * 2048 + j * 256, gran(t.x, t.y, T));
      SB();
    }
    if (sw == 0) STAMP(6);
    TILES(17)
    f32x4 y = gemv<9>(f32x4{0.f, 0.f, 0.f, 0.f}, hx, am, wz);
    asm volatile("" : "+v"(y));
    if (sw == 0) STAMP(25);
    v4u p0, p1;
    poll2<1024>(rW, lane * 16, OG_OFF + h * 8192 + sw * 2048, T, p0, p1);
    if (sw == 0) STAMP(7);
    {
      const float e = add32(add16(u2f(p0.x), u2f(p0.z)), add16(u2f(p1.x), u2f(p1.z)));
      auto eb = __builtin_amdgcn_permlane32_swap(f2u(e), f2u(e), false, false);
      unsigned pk = cvtpk(u2f(eb[0]), u2f(eb[1]));
      pk = __builtin_amdgcn_update_dpp(pk, pk, XOR8, 0xc, 0xf, false);
      f32x4 y1 = {0.f, 0.f, 0.f, 0.f};
#define YM(I, ACC) { const unsigned bc = dppu<BCAST + (I)>(pk); ACC = mfma(v4u{__builtin_amdgcn_perm(0u, bc, sl.x), __builtin_amdgcn_perm(0u, bc, sl.y), \
        __builtin_amdgcn_perm(0u, bc, sl.z), __builtin_amdgcn_perm(0u, bc, sl.w)}, wy[I], ACC); }
      YM(0, y) YM(1, y1) YM(2, y) YM(3, y1) YM(4, y) YM(5, y1) YM(6, y) YM(7, y1)
      y += y1;
    }
    *(LAS f32x4*)(Lf + L_YW + sw * 256 + lane * 4) = y;
    LDS_BARRIER();
    if (sw < 2) {
      f32x2 t = *(LAS f32x2*)((LAS char*)L + a_yr);
#pragma unroll
      for (int i = 1; i < 4; ++i) t += *(LAS f32x2*)((LAS char*)L + a_yr + i * 1024);
      st_sc(rW, a_yg, YG_OFF + (j * 32 + h) * 1152 + sw * 1024, gran(t.x, t.y, T));
      if (sw == 0) STAMP(20);
    } else if (sw == 2 && h < 18) {
      poll2<16 * 1152>(rW, (lane >> 2) * 1152 + (lane & 3) * 16, YG_OFF + j * 32 * 1152 + h * 64, T, p0, p1);
      float y0 = u2f(p0.x) + u2f(p1.x), y1 = u2f(p0.z) + u2f(p1.z);
      y0 += dpp<ROR4>(y0); y1 += dpp<ROR4>(y1);
      y0 += dpp<XOR8>(y0); y1 += dpp<XOR8>(y1);
      y0 = sum32x(add16(y0, y1));
      auto yy = __builtin_amdgcn_permlane16_swap(f2u(y0), f2u(y0), false, false);
      const unsigned res = cvtpk(u2f(xres << 16) + u2f(yy[0]), u2f(xres & 0xffff0000u) + u2f(yy[1]));
      __builtin_amdgcn_raw_buffer_store_b32(res, mk(out, 2304), lane < 4 ? (unsigned)oo : OOB, 0, OUTSC);
      if (bid == 0 && lane == 0) __builtin_amdgcn_raw_buffer_store_b32(T, rW, E_OFF, 0, AUX_SC1);
      STAMP(15);
    }
    return;
  }

  const bool isA = wave == 4;
  STAMP(isA ? 0 : 8);
  const int g = lane >> 4, c = lane & 15;
  const int hp = 4 * (h & 7) + (h >> 3);
  const rsrc_t rW = mk(ws, WS_USED);
  v4u w[36];
  unsigned E = 0;
  const int u = (r & 15) + 16 * (xc & 3), qq = (r >> 4) + 2 * (xc >> 2);
  const int gm = j & 1, half = (j >> 1) & 1, rh = j >> 2;
  const unsigned stride = isA ? 8192 : 9216, cofs = isA ? u * 128 : hp * 288 + half * 144;
  int row0 = isA ? 144 * qq : 288 * rh;
  const rsrc_t rG = mk((isA ? wq : gm ? wu : wg) + cofs, 576 * stride - cofs);
  const unsigned gvo = (c < (isA ? 8 : 9) ? (unsigned)(c * 16) : OOB) + g * stride;
  if (isA) {
    __builtin_amdgcn_s_setprio(3);
    E = __builtin_amdgcn_raw_buffer_load_b32(rW, E_OFF, 0, AUX_SC1);
    BARRIER();
  } else {
    __builtin_amdgcn_s_setprio(2);
    v3u xr[3];
    const rsrc_t rX = mk(xg, 2304);
#pragma unroll
    for (int k = 0; k < 3; ++k) xr[k] = __builtin_amdgcn_raw_buffer_load_b96(rX, (lane >> 5) * 1152 + (lane & 31) * 12, k * 384, 0);
    if (lane < 12) L[L_FL + lane] = 0u;
    LDS_BARRIER();
    float xv[18];
#pragma unroll
    for (int k = 0; k < 3; ++k)
#pragma unroll
      for (int e = 0; e < 3; ++e) { xv[k * 6 + 2 * e] = u2f(xr[k][e] << 16); xv[k * 6 + 2 * e + 1] = u2f(xr[k][e] & 0xffff0000u); }
    float sum = 0.f;
#pragma unroll
    for (int i = 0; i < 18; ++i) sum += xv[i];
    const float mean = sum16x(rowsum(sum)) * (1.0f / 576.0f);
    float sq = 0.f;
#pragma unroll
    for (int i = 0; i < 18; ++i) { const float d = xv[i] - mean; sq = fmaf(d, d, sq); }
    const float var = sum16x(rowsum(sq)) * (1.0f / 576.0f);
    const float mb = rbf(mean), sd = rbf(__builtin_sqrtf(rbf(rbf(var) + 1e-5f))), rs = 1.0f / sd;
    LAS v2u* dst = (LAS v2u*)(L + L_XN + (lane >> 5) * XN_S + (lane & 31) * 6);
#pragma unroll
    for (int k = 0; k < 3; ++k)
#pragma unroll
      for (int e = 0; e < 3; ++e) {
        float n[2];
#pragma unroll
        for (int f = 0; f < 2; ++f) { const float d = rbf(xv[k * 6 + 2 * e + f] - mb); const float q0 = d * rs; n[f] = fmaf(fmaf(-q0, sd, d), rs, q0); }
        dst[k * 96 + e] = v2u{cvtpk(n[0], n[0]), cvtpk(n[1], n[1])};
      }
    lds_signal(L + L_FL + F_XN, lane);
    STAMP(9);
    __builtin_amdgcn_s_setprio(0);
  }
  const unsigned hm = (c & 1) ? 0xffff0000u : 0x0000ffffu;
  const int mt = (c & 7) >> 1;
  const v4u am = {mt == 0 ? hm : 0u, mt == 1 ? hm : 0u, mt == 2 ? hm : 0u, mt == 3 ? hm : 0u};
  unsigned T = 0;
  f32x4 ga = {0.f, 0.f, 0.f, 0.f};
#pragma unroll 1
  for (int t = 0; t < (isA ? 1 : 2); ++t, row0 += 144) {
    unsigned so = row0 * stride;
#pragma unroll
    for (int i = 0; i < 36; ++i) { w[i] = ld_nt(rG, gvo, so); so += 4 * stride; }
    SB();
    if (isA) {
      T = __builtin_amdgcn_readfirstlane(E) + 0x9E3779B9u;
      if (lane == 0) L[L_FL + F_T] = T;
      lds_wait(L + L_FL + F_XN);
      STAMP(1);
    }
    asm volatile("" :: "v"(cv));
    unsigned xv[36];
#pragma unroll
    for (int i = 0; i < 36; ++i) xv[i] = L[L_XN + (c >> 3) * XN_S + row0 + g + 4 * i];
    SB();
    ga = gemv<36>(ga, xv, am, w);
  }
  if (isA) {
    const float s2 = dpp<XOR8>(ga[2]), s3 = dpp<XOR8>(ga[3]);
    const unsigned o = QG_OFF + (((u >> 1) * 2 + (g >> 1)) * 4 + qq) * 1024 + ((u & 1) * 64 + (c & 7) * 8 + (g & 1) * 4 + (c >> 3) * 2) * 8;
    st_sc(rW, o, 0, gran(c < 8 ? ga[0] : s2, c < 8 ? ga[1] : s3, T));
    STAMP(2);
    __builtin_amdgcn_s_sleep(QDELAY);
    float q0, q1;
#pragma unroll 1
    for (int spin = 0; spin < SPIN_CAP; ++spin) {
      MEMBAR();
#pragma unroll
      for (int i = 0; i < 4; ++i) w[i] = ld_sc(rW, lane * 16, QG_OFF + ((h * 2 + b) * 4 + i) * 1024);
      q0 = (u2f(w[0].x) + u2f(w[1].x)) + (u2f(w[2].x) + u2f(w[3].x));
      q1 = (u2f(w[0].z) + u2f(w[1].z)) + (u2f(w[2].z) + u2f(w[3].z));
      if (none_bad(tagbad(w[0], T) | tagbad(w[1], T) | tagbad(w[2], T) | tagbad(w[3], T))) break;
      STAMP(21);
      __builtin_amdgcn_s_sleep(16);
    }
    L[L_QB + lane] = cvtpk(q0, q1);
    lds_signal(L + L_FL + F_Q, lane);
    STAMP(3);
  } else {
    st_sc(rW, c < 9 ? (unsigned)((g >> 1) * 288 + c * 32 + (g & 1) * 16) : OOB, GU_OFF + bid * 640, __builtin_bit_cast(v4u, ga));
    STAMP(12);
    lds_wait4(L + L_FL + F_KD, lane);
    T = __builtin_amdgcn_readfirstlane(L[L_FL + F_T]);
    float mw[4], lw[4];
#pragma unroll
    for (int i = 0; i < 4; ++i) { mw[i] = Lf[L_ML + 2 * i]; lw[i] = Lf[L_ML + 2 * i + 1]; }
    const float mg = fmaxf(fmaxf(mw[0], mw[1]), fmaxf(mw[2], mw[3]));
    float lg = 0.f;
#pragma unroll
    for (int i = 0; i < 4; ++i) lg += lw[i] * ex2((mw[i] - mg) * LOG2E);
    VMWAIT(0);
    st_sc(rW, lane == 0 ? ML_OFF + h * 128 + j * 16 : OOB, 0, gran(mg, lg, T));
    v4u v;
#pragma unroll 1
    for (int spin = 0; spin < SPIN_CAP; ++spin) {
      __builtin_amdgcn_s_sleep(MLSLEEP);
      MEMBAR();
      v = ld_sc(rW, ((lane & 7) ^ (b * 4)) * 16, ML_OFF + h * 128);
      if (none_bad(tagbad(v, T))) break;
    }
    MEMBAR();
#pragma unroll
    for (int i = 0; i < 8; ++i) w[i] = ld_sc(rW, lane < 36 ? (unsigned)(lane * 16) : OOB, GU_OFF + (i * 32 + h) * 640);
#pragma unroll
    for (int hf = 0; hf < 2; ++hf) {
      v4u hv;
#pragma unroll
      for (int e = 0; e < 4; ++e) {
        const float gt = rbf(u2f(w[2 * hf][e]) + u2f(w[4 + 2 * hf][e])), up = rbf(u2f(w[1 + 2 * hf][e]) + u2f(w[5 + 2 * hf][e]));
        const float t2 = 1.5957691216057308f * (gt + 0.044715f * gt * gt * gt);
        const float hh = rbf(gt / (1.0f + ex2(-t2 * LOG2E))) * up;
        hv[e] = cvtpk(hh, hh);
      }
      if (lane < 36) *(LAS v4u*)(L + L_HD + (lane >= 18 ? HD_S - 72 : 0) + hf * 72 + lane * 4) = hv;
    }
    STAMP(13);
    const float ms = u2f(v.x);
    float M = fmaxf(ms, dpp<XOR1>(ms)); M = fmaxf(M, dpp<XOR2>(M));
    float Ls = u2f(v.z) * ex2((ms - M) * LOG2E);
    Ls += dpp<XOR1>(Ls); Ls += dpp<XOR2>(Ls);
    const float inv = 1.0f / Ls;
    if (lane == 0) *(LAS f32x4*)(Lf + L_FW) = f32x4{ex2((mw[0] - M) * LOG2E) * inv, ex2((mw[1] - M) * LOG2E) * inv, ex2((mw[2] - M) * LOG2E) * inv, ex2((mw[3] - M) * LOG2E) * inv};
    lds_signal(L + L_FL + F_FW, lane);
    {
      const unsigned t = __builtin_amdgcn_raw_buffer_load_b32(rC, lane < 6 ? (unsigned)(r * 6 + lane) * 128u : OOB, 0, 0);
      asm volatile("" :: "v"(t));
    }
    STAMP(5);
  }
}

extern "C" void kernel_launch(void* const* d_in, const int* in_sizes, int n_in, void* d_out, int out_size, void* d_ws, size_t ws_size,
                              hipStream_t stream) {
  fused_decode<<<256, 384, 0, stream>>>((const char*)d_in[0], (const char*)d_in[1], (const char*)d_in[2], (const char*)d_in[3],
                                        (const char*)d_in[4], (const char*)d_in[6], (const char*)d_in[7], (const char*)d_in[8],
                                        (char*)d_out, (char*)d_ws);
}
```

```cpp
#define TG0 12
#define TGN 2
#include <hip/hip_runtime.h>
#include <hip/hip_bf16.h>

#define WIN 16
#define NPRE 2
#define PINFL 6
#define TAUK 240
#define TAUV 216
#define LSTEP 80
#define WC0 3
#define WC1 4
#define WC2 5
#define WC3 7
#define WC4 9
#define WC5 12
#define QDELAY 32
#define PPOLL 0
#define TBAR 1
#define TPOS 99
#define MLSLEEP 80
#define TOUCH 1
#define LATEPF 1
#define OUTSC 0
#define TOFF 40
#define XBIAS 0
#define WSPF 1
#define DIAG 0

#define GAS __attribute__((address_space(1)))
#define LAS __attribute__((address_space(3)))
typedef unsigned v4u __attribute__((ext_vector_type(4)));
typedef unsigned v3u __attribute__((ext_vector_type(3)));
typedef unsigned v2u __attribute__((ext_vector_type(2)));
typedef float f32x4 __attribute__((ext_vector_type(4)));
typedef float f32x2 __attribute__((ext_vector_type(2)));
typedef __bf16 bf16x8 __attribute__((ext_vector_type(8)));
typedef __bf16 bf16x2 __attribute__((ext_vector_type(2)));
typedef unsigned long long v8q __attribute__((ext_vector_type(8)));
typedef unsigned long long v2q __attribute__((ext_vector_type(2)));
typedef __amdgpu_buffer_rsrc_t rsrc_t;

constexpr int E_OFF = 0;
constexpr int QG_OFF = 4096;
constexpr int ML_OFF = QG_OFF + 262144;
constexpr int GU_OFF = ML_OFF + 4096;
constexpr int OG_OFF = GU_OFF + 163840;
constexpr int YG_OFF = OG_OFF + 262144;
constexpr int WS_USED = YG_OFF + 294912;
constexpr int TS_OFF = 4 << 20;

constexpr int L_XN = 0, XN_S = 580;
constexpr int L_HD = 1160, HD_S = 148;
constexpr int L_QB = L_HD + 2 * HD_S;
constexpr int L_OW = L_QB + 64;
constexpr int L_YW = L_OW + 512;
constexpr int L_ML = L_YW + 1056;
constexpr int L_FW = L_ML + 8;
constexpr int L_FL = L_FW + 4;
constexpr int F_KD = 0, F_Q = 4, F_T = 5, F_TOK = 6, F_XN = 7, F_FW = 8;
constexpr int L_END = L_FL + 12;
static_assert(L_FL % 4 == 0 && L_OW % 4 == 0 && L_YW % 4 == 0 && L_QB % 4 == 0 && L_FW % 4 == 0, "16-byte aligned LDS pieces");

constexpr int AUX_NT = 2, AUX_SC1 = 16;
constexpr unsigned OOB = 0x80000000u;
constexpr unsigned CODE_BYTES = 18432;
constexpr int SPIN_CAP = 1 << 18;
constexpr float LOG2E = 1.4426950408889634f;

__device__ __forceinline__ rsrc_t mk(const void* p, unsigned bytes) { return __builtin_amdgcn_make_buffer_rsrc((void*)p, 0, bytes, 0x00020000); }
__device__ __forceinline__ v4u ld_nt(rsrc_t r, unsigned vo, unsigned so) { return __builtin_amdgcn_raw_buffer_load_b128(r, vo, so, AUX_NT); }
__device__ __forceinline__ v4u ld_sc(rsrc_t r, unsigned vo, unsigned so) { return __builtin_amdgcn_raw_buffer_load_b128(r, vo, so, AUX_SC1); }
__device__ __forceinline__ void st_sc(rsrc_t r, unsigned vo, unsigned so, v4u v) { __builtin_amdgcn_raw_buffer_store_b128(v, r, vo, so, AUX_SC1); }
__device__ __forceinline__ float u2f(unsigned u) { return __builtin_bit_cast(float, u); }
__device__ __forceinline__ unsigned f2u(float f) { return __builtin_bit_cast(unsigned, f); }
__device__ __forceinline__ unsigned cvtpk(float lo, float hi) {
  bf16x2 r = {(__bf16)lo, (__bf16)hi};
  return __builtin_bit_cast(unsigned, r);
}
__device__ __forceinline__ float rbf(float f) { return (float)(__bf16)f; }
__device__ __forceinline__ float ex2(float f) { return __builtin_amdgcn_exp2f(f); }
__device__ __forceinline__ float dot2(unsigned a, unsigned b, float c) {
  return __builtin_amdgcn_fdot2_f32_bf16(__builtin_bit_cast(bf16x2, a), __builtin_bit_cast(bf16x2, b), c, false);
}
__device__ __forceinline__ f32x4 mfma(v4u a, v4u b, f32x4 c) {
  return __builtin_amdgcn_mfma_f32_16x16x32_bf16(__builtin_bit_cast(bf16x8, a), __builtin_bit_cast(bf16x8, b), c, 0, 0, 0);
}
template <int CTRL> __device__ __forceinline__ float dpp(float x) { return u2f(__builtin_amdgcn_mov_dpp(f2u(x), CTRL, 0xf, 0xf, true)); }
template <int CTRL> __device__ __forceinline__ unsigned dppu(unsigned x) { return __builtin_amdgcn_mov_dpp(x, CTRL, 0xf, 0xf, true); }
constexpr int XOR1 = 0xB1, XOR2 = 0x4E, XOR7 = 0x141, XOR8 = 0x128, ROR4 = 0x124, BCAST = 0x150;
__device__ __forceinline__ float add16(float a, float b) {
  auto s = __builtin_amdgcn_permlane16_swap(f2u(a), f2u(b), false, false); return u2f(s[0]) + u2f(s[1]); }
__device__ __forceinline__ float add32(float a, float b) {
  auto s = __builtin_amdgcn_permlane32_swap(f2u(a), f2u(b), false, false); return u2f(s[0]) + u2f(s[1]); }
__device__ __forceinline__ float sum16x(float x) { return add16(x, x); }
__device__ __forceinline__ float sum32x(float x) { return add32(x, x); }
__device__ __forceinline__ float max16x(float x) { auto s = __builtin_amdgcn_permlane16_swap(f2u(x), f2u(x), false, false); return fmaxf(u2f(s[0]), u2f(s[1])); }
__device__ __forceinline__ float max32x(float x) { auto s = __builtin_amdgcn_permlane32_swap(f2u(x), f2u(x), false, false); return fmaxf(u2f(s[0]), u2f(s[1])); }
__device__ __forceinline__ float rowsum(float x) { x += dpp<XOR1>(x); x += dpp<XOR2>(x); x += dpp<XOR7>(x); x += dpp<XOR8>(x); return x; }
__device__ __forceinline__ float rowmax(float x) { x = fmaxf(x, dpp<XOR1>(x)); x = fmaxf(x, dpp<XOR2>(x)); x = fmaxf(x, dpp<XOR7>(x)); x = fmaxf(x, dpp<XOR8>(x)); return x; }
#define SB() __builtin_amdgcn_sched_barrier(0)
#define MEMBAR() asm volatile("" ::: "memory")
#define LDS_FENCE() asm volatile("s_waitcnt lgkmcnt(0)" ::: "memory")
#define BARRIER() do { SB(); asm volatile("s_barrier" ::: "memory"); SB(); } while (0)
#define LDS_BARRIER() do { SB(); asm volatile("s_waitcnt lgkmcnt(0)\n\ts_barrier" ::: "memory"); SB(); } while (0)
#define VMWAIT(n) asm volatile("s_waitcnt vmcnt(%0)" :: "i"(n) : "memory")
__device__ __forceinline__ unsigned lds_ld(LAS unsigned* p) { return __hip_atomic_load(p, __ATOMIC_RELAXED, __HIP_MEMORY_SCOPE_WORKGROUP); }
__device__ __forceinline__ void lds_wait(LAS unsigned* p) {
  while (__builtin_amdgcn_readfirstlane(lds_ld(p)) == 0) __builtin_amdgcn_s_sleep(1);
  MEMBAR();
}
__device__ __forceinline__ void lds_wait4(LAS unsigned* p, int lane) {
  while (__builtin_amdgcn_ballot_w64(lds_ld(p + (lane & 3)) == 0) != 0ull) __builtin_amdgcn_s_sleep(4);
  MEMBAR();
}
__device__ __forceinline__ void lds_signal(LAS unsigned* p, int lane) {
  LDS_FENCE();
  if (lane == 0) __hip_atomic_store(p, 1u, __ATOMIC_RELAXED, __HIP_MEMORY_SCOPE_WORKGROUP);
}
__device__ __forceinline__ bool none_bad(unsigned bad) { return __builtin_amdgcn_ballot_w64(bad != 0u) == 0ull; }
__device__ __forceinline__ unsigned tagbad(const v4u& v, unsigned T) { return (v.y ^ T) | (v.w ^ T); }
__device__ __forceinline__ v4u gran(float a, float b, unsigned T) { return v4u{f2u(a), T, f2u(b), T}; }
__device__ __forceinline__ v4u afrag(unsigned dup, const v4u& m) { return v4u{dup & m.x, dup & m.y, dup & m.z, dup & m.w}; }
template <int D> __device__ __forceinline__ void poll2(rsrc_t rW, unsigned vo, unsigned so, unsigned T, v4u& r0, v4u& r1) {
#pragma unroll 1
  for (int spin = 0; spin < SPIN_CAP; ++spin) {
    MEMBAR();
    r0 = ld_sc(rW, vo, so); r1 = ld_sc(rW, vo, so + D);
    if (none_bad(tagbad(r0, T) | tagbad(r1, T))) break;
  }
}

#define STAMP(slot) do {} while (0)
#define STAMP15() do {} while (0)

#define PACE(t16) do { const unsigned need_ = tk0 + ((unsigned)(t16) >> 4); int lag_; \
    while ((lag_ = (int)((unsigned)__builtin_amdgcn_s_memrealtime() - need_)) < 0) __builtin_amdgcn_s_sleep(1); \
    const int cls_ = (int)(((unsigned)lag_ * (65536u / LSTEP)) >> 16); \
    asm volatile("s_cmp_gt_i32 %0, 5\n\ts_cbranch_scc1 1f\n\ts_waitcnt vmcnt(%1)\n\ts_cmp_eq_i32 %0, 5\n\ts_cbranch_scc1 1f\n\ts_waitcnt vmcnt(%2)\n\t" \
                 "s_cmp_eq_i32 %0, 4\n\ts_cbranch_scc1 1f\n\ts_waitcnt vmcnt(%3)\n\ts_cmp_eq_i32 %0, 3\n\ts_cbranch_scc1 1f\n\ts_waitcnt vmcnt(%4)\n\t" \
                 "s_cmp_eq_i32 %0, 2\n\ts_cbranch_scc1 1f\n\ts_waitcnt vmcnt(%5)\n\ts_cmp_eq_i32 %0, 1\n\ts_cbranch_scc1 1f\n\ts_waitcnt vmcnt(%6)\n1:" \
                 :: "s"(cls_), "i"(WC5 - 1), "i"(WC4 - 1), "i"(WC3 - 1), "i"(WC2 - 1), "i"(WC1 - 1), "i"(WC0 - 1) : "scc", "memory"); } while (0)

template <int N, int M> __device__ __forceinline__ f32x4 gemv(f32x4 a0, const unsigned (&xv)[N], const v4u& am, const v4u (&w)[M]) {
  f32x4 a1 = {0.f, 0.f, 0.f, 0.f};
#pragma unroll
  for (int i = 0; i < N; ++i) { if (i & 1) a1 = mfma(afrag(xv[i], am), w[i], a1); else a0 = mfma(afrag(xv[i], am), w[i], a0); }
  return a0 + a1;
}

__global__ void __launch_bounds__(384, 2)
fused_decode(const char* xg_, const char* kc_, const char* vc_, const char* wq_, const char* wo_, const char* wg_, const char* wu_, const char* wd_,
             char* out_, char* ws_) {
  v8q a8; v2q a2;
  asm volatile("s_load_dwordx16 %0, %2, 0x0\n\ts_load_dwordx4 %1, %2, 0x40" : "=&s"(a8), "=&s"(a2) : "s"(__builtin_amdgcn_kernarg_segment_ptr()));
  SB();
  __shared__ __attribute__((aligned(16))) unsigned smem[L_END];
  LAS unsigned* L = (LAS unsigned*)smem;
  LAS float* Lf = (LAS float*)smem;
  const int bid = blockIdx.x, xc = bid & 7, r = bid >> 3;
  int lane = threadIdx.x & 63;
  const int wave = __builtin_amdgcn_readfirstlane(threadIdx.x >> 6);
  const rsrc_t rC = mk((const void*)(__builtin_amdgcn_s_getpc() & ~255ull), CODE_BYTES);
  const unsigned cv = __builtin_amdgcn_raw_buffer_load_b32(rC, 0, min((unsigned)(r * 6 + wave) * 128u, CODE_BYTES - 128u), 0);
  const int h = xc + 8 * (r & 3), j = r >> 2, s = j & 3, b = j >> 2;
  unsigned cb = ((b * 32 + h) * 4 + s) * 524288u;
  unsigned vo = wave * 1024 + lane * 16;
  if (lane < 12) L[L_FL + lane] = 0u;
  asm volatile("" : "+v"(vo));
  SB();
  asm volatile("s_waitcnt lgkmcnt(0)\n\ts_barrier" : "+s"(a8), "+s"(a2), "+s"(cb) :: "memory");
  SB();
  const char* xg = (const char*)a8[0]; const char* kc = (const char*)a8[1]; const char* vc = (const char*)a8[2]; const char* wq = (const char*)a8[3];
  const char* wo = (const char*)a8[4]; const char* wg = (const char*)a8[5]; const char* wu = (const char*)a8[6]; const char* wd = (const char*)a8[7];
  char* out = (char*)a2[0]; char* ws = (char*)a2[1];

  if (__builtin_expect(wave < 4, 1)) {
    const int sw = wave;
    const char* kb = kc + cb;
    const char* vb = vc + cb;
    const rsrc_t rK = mk(kb, 524288), rV = mk(vb, 524288);
    constexpr int NVB = 8 - NPRE;
    constexpr int P0 = PINFL / 2;
    v4u a[WIN], pre[NPRE * 16];
#pragma unroll
    for (int i = 0; i < P0; ++i) { a[i] = ld_nt(rK, vo, i * 4096); pre[i] = ld_nt(rV, vo, NVB * 65536 + i * 4096); }
    SB();
    const unsigned tk0 = (unsigned)__builtin_amdgcn_s_memrealtime() - TOFF - (__builtin_amdgcn_s_getreg(20 | (3 << 11)) & 1) * XBIAS;
    asm volatile("" : "+v"(lane));
#pragma unroll
    for (int i = P0; i < NPRE * 16; ++i) { VMWAIT(PINFL - 1); pre[i] = ld_nt(rV, vo, NVB * 65536 + i * 4096); SB(); }
#pragma unroll
    for (int i = P0; i < WIN; ++i) { VMWAIT(PINFL - 1); a[i] = ld_nt(rK, vo, i * 4096); SB(); }
    if (sw == 0) STAMP(16);
    const int g = lane >> 4, c = lane & 15;
    lds_wait(L + L_FL + F_Q);
    asm volatile("" :: "v"(cv));
    const v4u q = *(LAS v4u*)(L + L_QB + c * 4);
    if (sw == 0) STAMP(17);
    float sc[8];
#pragma unroll
    for (int i = 0; i < 8; ++i) sc[i] = 0.f;
#pragma unroll 1
    for (int blk = 0; blk < 8; ++blk) {
      const rsrc_t rN = mk(blk < 7 ? kb : vb, 524288);
      const unsigned sN = blk < 7 ? blk * 65536 + 65536 : 0;
      float x[16];
#pragma unroll
      for (int i = 0; i < 16; ++i) {
        const v4u k = a[i];
        float d = dot2(k.x, q.x, 0.f); d = dot2(k.y, q.y, d); d = dot2(k.z, q.z, d); d = dot2(k.w, q.w, d);
        x[i] = d;
        asm volatile("" : "+v"(x[i]));
        SB();
        if (i & 1) {
          PACE((NPRE * 16 + 7 + blk * 16 + i) * TAUK);
          a[i - 1] = ld_nt(rN, vo, sN + (i - 1) * 4096);
          a[i] = ld_nt(rN, vo, sN + i * 4096);
          SB();
        }
      }
#define TR_STEP(HALF, CTRL) _Pragma("unroll") for (int u = 0; u < (HALF); ++u) { const float lo_ = x[u], hi_ = x[u + (HALF)]; \
        const float t1_ = lo_ + dpp<CTRL>(lo_), t2_ = hi_ + dpp<CTRL>(hi_); x[u] = (c & (HALF)) ? t2_ : t1_; }
      TR_STEP(8, XOR8) TR_STEP(4, XOR7) TR_STEP(2, XOR2) TR_STEP(1, XOR1)
#undef TR_STEP
#pragma unroll
      for (int i = 0; i < 7; ++i) sc[i] = sc[i + 1];
      sc[7] = rbf(rbf(x[0]) * 0.08838834764831845f);
    }
    float m = sc[0];
#pragma unroll
    for (int i = 1; i < 8; ++i) m = fmaxf(m, sc[i]);
    m = max32x(max16x(rowmax(m)));
    float l = 0.f;
    unsigned pp[8];
#pragma unroll
    for (int i = 0; i < 8; ++i) { const float p = ex2((sc[i] - m) * LOG2E); l += p; pp[i] = cvtpk(p, p); }
    l = sum32x(sum16x(rowsum(l)));
    if (lane == 0) { Lf[L_ML + 2 * sw] = m; Lf[L_ML + 2 * sw + 1] = l; }
    lds_signal(L + L_FL + F_KD + sw, lane);
    if (sw == 0) STAMP(18);
    const unsigned hm = (c & 1) ? 0xffff0000u : 0x0000ffffu;
    const int mt = (c & 7) >> 1;
    const v4u am = {mt == 0 ? hm : 0u, mt == 1 ? hm : 0u, mt == 2 ? hm : 0u, mt == 3 ? hm : 0u};
    const v4u pm = {c < 8 ? am.x : 0u, c < 8 ? am.y : 0u, c < 8 ? am.z : 0u, c < 8 ? am.w : 0u};
    f32x4 acc = {0.f, 0.f, 0.f, 0.f};
#define PVM(I, P, V) { acc = mfma(afrag(dppu<BCAST + (I)>(P), pm), (V), acc); asm volatile("" : "+v"(acc)); SB(); }
#define PV2(I) { PVM(I, pp[0], a[I]) PVM((I) + 1, pp[0], a[(I) + 1]) PACE((NPRE * 16 + 128) * TAUK + (blk * 16 + (I) + 8) * TAUV); \
      a[I] = ld_nt(rV, vo, sN + (I) * 4096); a[(I) + 1] = ld_nt(rV, vo, sN + ((I) + 1) * 4096); SB(); }
#define PV16(P, A, O) PVM(0, P, A[O + 0]) PVM(1, P, A[O + 1]) PVM(2, P, A[O + 2]) PVM(3, P, A[O + 3]) PVM(4, P, A[O + 4]) PVM(5, P, A[O + 5]) \
      PVM(6, P, A[O + 6]) PVM(7, P, A[O + 7]) PVM(8, P, A[O + 8]) PVM(9, P, A[O + 9]) PVM(10, P, A[O + 10]) PVM(11, P, A[O + 11]) \
      PVM(12, P, A[O + 12]) PVM(13, P, A[O + 13]) PVM(14, P, A[O + 14]) PVM(15, P, A[O + 15])
    const unsigned pq0 = pp[NVB], pq1 = pp[NVB + 1];
#pragma unroll 1
    for (int blk = 0; blk < NVB - 1; ++blk) {
      const unsigned sN = blk * 65536 + 65536;
      PV2(0) PV2(2) PV2(4) PV2(6) PV2(8) PV2(10) PV2(12) PV2(14)
#pragma unroll
      for (int i = 0; i < NVB - 1; ++i) pp[i] = pp[i + 1];
      if (blk == 0) { PV16(pq0, pre, 0) }
      if (blk == 1) { PV16(pq1, pre, 16) }
    }
    const int hp = 4 * (h & 7) + (h >> 3);
    v4u wz[9], wy[8];
    const rsrc_t rD = mk(wd + (size_t)(144 * hp + 36 * sw) * 1152 + j * 144, 36 * 1152 - j * 144);
    const rsrc_t rO = mk(wo + (size_t)(128 * h + 32 * sw) * 1152 + j * 144, 32 * 1152 - j * 144);
    const unsigned to = (c < 9 ? (unsigned)(c * 16) : OOB) + g * 1152;
#define TILES(P) if (TPOS == (P)) { _Pragma("unroll") for (int i = 0; i < 9; ++i) wz[i] = ld_nt(rD, to, i * 4608); \
      _Pragma("unroll") for (int i = 0; i < 8; ++i) wy[i] = ld_nt(rO, to, i * 4608); SB(); }
#define TL1(n) { if ((n) < 9) wz[(n) < 9 ? (n) : 0] = ld_nt(rD, to, (n) * 4608); else if ((n) < 17) wy[(n) >= 9 && (n) < 17 ? (n) - 9 : 0] = ld_nt(rO, to, ((n) - 9) * 4608); }
#define TG(I) if (TPOS == 99 && (I) >= TG0) { _Pragma("unroll") for (int i = 0; i < TGN; ++i) TL1(((I) - TG0) * TGN + i) SB(); }
    if (TBAR && TPOS == 0) BARRIER();
    TILES(0)
    const rsrc_t rW = mk(ws, WS_USED);
    lds_wait(L + L_FL + F_FW);
    const unsigned T = __builtin_amdgcn_readfirstlane(L[L_FL + F_T]);
    float fw = Lf[L_FW + sw];
    unsigned hx[9];
#pragma unroll
    for (int i = 0; i < 9; ++i) hx[i] = L[L_HD + (c >> 3) * HD_S + 36 * sw + g + 4 * i];
    const unsigned src = (c >> 3) ? 0x0302u : 0x0100u, sel = (c & 1) ? (src << 16) | 0x0c0cu : 0x0c0c0000u | src;
    v4u sl = {mt == 0 ? sel : 0x0c0c0c0cu, mt == 1 ? sel : 0x0c0c0c0cu, mt == 2 ? sel : 0x0c0c0c0cu, mt == 3 ? sel : 0x0c0c0c0cu};
    unsigned a_ow = (L_OW + sw * 128 + c * 8 + g * 4) * 4, a_or = (L_OW + sw * 32 + (c & 7) * 4 + (c >> 3) * 2) * 4, a_og = g == 0 ? c * 16 : OOB;
    const int yk = (sw & 1) * 16 + (lane >> 2), ykb = yk >= 9 ? 1 : 0;
    unsigned a_yr = (L_YW + ((ykb * 2 + ((lane & 3) >> 1)) * 16 + yk - ykb * 9) * 4 + (lane & 1) * 2) * 4, a_yg = (sw == 0 || lane < 8) ? lane * 16 : OOB;
    const int oo = (h >= 9 ? 1152 - 144 : 0) + 144 * j + 16 * h + (lane & 3) * 4;
    unsigned xres = 0;
    if (sw == 2 && h < 18) xres = *(const GAS unsigned*)(xg + oo);
    asm volatile("" : "+v"(fw), "+v"(sl), "+v"(a_ow), "+v"(a_or), "+v"(a_og), "+v"(a_yr), "+v"(a_yg));
    asm volatile("" : "+v"(hx[0]), "+v"(hx[1]), "+v"(hx[2]), "+v"(hx[3]), "+v"(hx[4]), "+v"(hx[5]), "+v"(hx[6]), "+v"(hx[7]), "+v"(hx[8]));
    SB();
    PVM(0, pp[0], a[0]) PVM(1, pp[0], a[1]) PVM(2, pp[0], a[2]) PVM(3, pp[0], a[3]) PVM(4, pp[0], a[4]) PVM(5, pp[0], a[5]) PVM(6, pp[0], a[6]) TG(6) PVM(7, pp[0], a[7]) TG(7)
    PVM(8, pp[0], a[8]) TG(8) PVM(9, pp[0], a[9]) TG(9) PVM(10, pp[0], a[10]) TG(10) PVM(11, pp[0], a[11]) TG(11) TILES(12) PVM(12, pp[0], a[12]) TG(12) TILES(13)
    PVM(13, pp[0], a[13]) TG(13) TILES(14) PVM(14, pp[0], a[14]) TG(14) TILES(15) PVM(15, pp[0], a[15])
    if (lane < 32) *(LAS f32x4*)((LAS char*)L + a_ow) = acc * fw;
    if (sw == 0) STAMP(19);
    TILES(16)
    LDS_BARRIER();
    {
      f32x2 t = *(LAS f32x2*)((LAS char*)L + a_or);
#pragma unroll
      for (int i = 1; i < 4; ++i) t += *(LAS f32x2*)((LAS char*)L + a_or + i * 512);
      st_sc(rW, a_og, OG_OFF + h * 8192 + sw * 2048 + j * 256, gran(t.x, t.y, T));
      SB();
    }
    if (sw == 0) STAMP(6);
    TILES(17)
    if (TPOS == 99) { _Pragma("unroll") for (int i = (15 - TG0) * TGN; i < 17; ++i) TL1(i) SB(); }
    f32x4 y = gemv<9>(f32x4{0.f, 0.f, 0.f, 0.f}, hx, am, wz);
    asm volatile("" : "+v"(y));
    if (sw == 0) STAMP(25);
    v4u p0, p1;
    poll2<1024>(rW, lane * 16, OG_OFF + h * 8192 + sw * 2048, T, p0, p1);
    if (sw == 0) STAMP(7);
    {
      const float e = add32(add16(u2f(p0.x), u2f(p0.z)), add16(u2f(p1.x), u2f(p1.z)));
      auto eb = __builtin_amdgcn_permlane32_swap(f2u(e), f2u(e), false, false);
      unsigned pk = cvtpk(u2f(eb[0]), u2f(eb[1]));
      pk = __builtin_amdgcn_update_dpp(pk, pk, XOR8, 0xc, 0xf, false);
      f32x4 y1 = {0.f, 0.f, 0.f, 0.f};
#define YM(I, ACC) { const unsigned bc = dppu<BCAST + (I)>(pk); ACC = mfma(v4u{__builtin_amdgcn_perm(0u, bc, sl.x), __builtin_amdgcn_perm(0u, bc, sl.y), \
        __builtin_amdgcn_perm(0u, bc, sl.z), __builtin_amdgcn_perm(0u, bc, sl.w)}, wy[I], ACC); }
      YM(0, y) YM(1, y1) YM(2, y) YM(3, y1) YM(4, y) YM(5, y1) YM(6, y) YM(7, y1)
      y += y1;
    }
    *(LAS f32x4*)(Lf + L_YW + sw * 256 + lane * 4) = y;
    LDS_BARRIER();
    if (sw < 2) {
      f32x2 t = *(LAS f32x2*)((LAS char*)L + a_yr);
#pragma unroll
      for (int i = 1; i < 4; ++i) t += *(LAS f32x2*)((LAS char*)L + a_yr + i * 1024);
      st_sc(rW, a_yg, YG_OFF + (j * 32 + h) * 1152 + sw * 1024, gran(t.x, t.y, T));
      if (sw == 0) STAMP(20);
    } else if (sw == 2 && h < 18) {
      poll2<16 * 1152>(rW, (lane >> 2) * 1152 + (lane & 3) * 16, YG_OFF + j * 32 * 1152 + h * 64, T, p0, p1);
      float y0 = u2f(p0.x) + u2f(p1.x), y1 = u2f(p0.z) + u2f(p1.z);
      y0 += dpp<ROR4>(y0); y1 += dpp<ROR4>(y1);
      y0 += dpp<XOR8>(y0); y1 += dpp<XOR8>(y1);
      y0 = sum32x(add16(y0, y1));
      auto yy = __builtin_amdgcn_permlane16_swap(f2u(y0), f2u(y0), false, false);
      const unsigned res = cvtpk(u2f(xres << 16) + u2f(yy[0]), u2f(xres & 0xffff0000u) + u2f(yy[1]));
      __builtin_amdgcn_raw_buffer_store_b32(res, mk(out, 2304), lane < 4 ? (unsigned)oo : OOB, 0, OUTSC);
      if (bid == 0 && lane == 0) __builtin_amdgcn_raw_buffer_store_b32(T, rW, E_OFF, 0, AUX_SC1);
      STAMP15();
    }
    return;
  }

  const bool isA = wave == 4;
  STAMP(isA ? 0 : 8);
  const int g = lane >> 4, c = lane & 15;
  const int hp = 4 * (h & 7) + (h >> 3);
  const rsrc_t rW = mk(ws, WS_USED);
  v4u w[36];
  unsigned E = 0;
  const int u = (r & 15) + 16 * (xc & 3), qq = (r >> 4) + 2 * (xc >> 2);
  const int gm = j & 1, half = (j >> 1) & 1, rh = j >> 2;
  const unsigned stride = isA ? 8192 : 9216, cofs = isA ? u * 128 : hp * 288 + half * 144;
  int row0 = isA ? 144 * qq : 288 * rh;
  const rsrc_t rG = mk((isA ? wq : gm ? wu : wg) + cofs, 576 * stride - cofs);
  const unsigned gvo = (c < (isA ? 8 : 9) ? (unsigned)(c * 16) : OOB) + g * stride;
  if (isA) {
    __builtin_amdgcn_s_setprio(3);
    E = __builtin_amdgcn_raw_buffer_load_b32(rW, E_OFF, 0, AUX_SC1);
  } else {
    __builtin_amdgcn_s_setprio(2);
    v3u xr[3];
    const rsrc_t rX = mk(xg, 2304);
#pragma unroll
    for (int k = 0; k < 3; ++k) xr[k] = __builtin_amdgcn_raw_buffer_load_b96(rX, (lane >> 5) * 1152 + (lane & 31) * 12, k * 384, 0);
    float xv[18];
#pragma unroll
    for (int k = 0; k < 3; ++k)
#pragma unroll
      for (int e = 0; e < 3; ++e) { xv[k * 6 + 2 * e] = u2f(xr[k][e] << 16); xv[k * 6 + 2 * e + 1] = u2f(xr[k][e] & 0xffff0000u); }
    float sum = 0.f;
#pragma unroll
    for (int i = 0; i < 18; ++i) sum += xv[i];
    const float mean = sum16x(rowsum(sum)) * (1.0f / 576.0f);
    float sq = 0.f;
#pragma unroll
    for (int i = 0; i < 18; ++i) { const float d = xv[i] - mean; sq = fmaf(d, d, sq); }
    const float var = sum16x(rowsum(sq)) * (1.0f / 576.0f);
    const float mb = rbf(mean), sd = rbf(__builtin_sqrtf(rbf(rbf(var) + 1e-5f))), rs = 1.0f / sd;
    LAS v2u* dst = (LAS v2u*)(L + L_XN + (lane >> 5) * XN_S + (lane & 31) * 6);
#pragma unroll
    for (int k = 0; k < 3; ++k)
#pragma unroll
      for (int e = 0; e < 3; ++e) {
        float n[2];
#pragma unroll
        for (int f = 0; f < 2; ++f) { const float d = rbf(xv[k * 6 + 2 * e + f] - mb); const float q0 = d * rs; n[f] = fmaf(fmaf(-q0, sd, d), rs, q0); }
        dst[k * 96 + e] = v2u{cvtpk(n[0], n[0]), cvtpk(n[1], n[1])};
      }
    lds_signal(L + L_FL + F_XN, lane);
    STAMP(9);
    __builtin_amdgcn_s_setprio(0);
  }
  const unsigned hm = (c & 1) ? 0xffff0000u : 0x0000ffffu;
  const int mt = (c & 7) >> 1;
  const v4u am = {mt == 0 ? hm : 0u, mt == 1 ? hm : 0u, mt == 2 ? hm : 0u, mt == 3 ? hm : 0u};
  unsigned T = 0;
  f32x4 ga = {0.f, 0.f, 0.f, 0.f};
#pragma unroll 1
  for (int t = 0; t < (isA ? 1 : 2); ++t, row0 += 144) {
    unsigned so = row0 * stride;
#pragma unroll
    for (int i = 0; i < 36; ++i) { w[i] = ld_nt(rG, gvo, so); so += 4 * stride; }
    SB();
    if (isA) {
      T = __builtin_amdgcn_readfirstlane(E) + 0x9E3779B9u;
      if (lane == 0) L[L_FL + F_T] = T;
      lds_wait(L + L_FL + F_XN);
      STAMP(1);
    }
    asm volatile("" :: "v"(cv));
    unsigned xv[36];
#pragma unroll
    for (int i = 0; i < 36; ++i) xv[i] = L[L_XN + (c >> 3) * XN_S + row0 + g + 4 * i];
    SB();
    ga = gemv<36>(ga, xv, am, w);
  }
  if (isA) {
    const float s2 = dpp<XOR8>(ga[2]), s3 = dpp<XOR8>(ga[3]);
    const unsigned o = QG_OFF + (((u >> 1) * 2 + (g >> 1)) * 4 + qq) * 1024 + ((u & 1) * 64 + (c & 7) * 8 + (g & 1) * 4 + (c >> 3) * 2) * 8;
    st_sc(rW, o, 0, gran(c < 8 ? ga[0] : s2, c < 8 ? ga[1] : s3, T));
    STAMP(2);
    __builtin_amdgcn_s_sleep(QDELAY);
    float q0, q1;
#pragma unroll 1
    for (int spin = 0; spin < SPIN_CAP; ++spin) {
      MEMBAR();
#pragma unroll
      for (int i = 0; i < 4; ++i) w[i] = ld_sc(rW, lane * 16, QG_OFF + ((h * 2 + b) * 4 + i) * 1024);
      q0 = (u2f(w[0].x) + u2f(w[1].x)) + (u2f(w[2].x) + u2f(w[3].x));
      q1 = (u2f(w[0].z) + u2f(w[1].z)) + (u2f(w[2].z) + u2f(w[3].z));
      if (none_bad(tagbad(w[0], T) | tagbad(w[1], T) | tagbad(w[2], T) | tagbad(w[3], T))) break;
      STAMP(21);
      __builtin_amdgcn_s_sleep(16);
    }
    L[L_QB + lane] = cvtpk(q0, q1);
    lds_signal(L + L_FL + F_Q, lane);
    STAMP(3);
  } else {
    st_sc(rW, c < 9 ? (unsigned)((g >> 1) * 288 + c * 32 + (g & 1) * 16) : OOB, GU_OFF + bid * 640, __builtin_bit_cast(v4u, ga));
    STAMP(12);
    lds_wait4(L + L_FL + F_KD, lane);
    T = __builtin_amdgcn_readfirstlane(L[L_FL + F_T]);
    float mw[4], lw[4];
#pragma unroll
    for (int i = 0; i < 4; ++i) { mw[i] = Lf[L_ML + 2 * i]; lw[i] = Lf[L_ML + 2 * i + 1]; }
    const float mg = fmaxf(fmaxf(mw[0], mw[1]), fmaxf(mw[2], mw[3]));
    float lg = 0.f;
#pragma unroll
    for (int i = 0; i < 4; ++i) lg += lw[i] * ex2((mw[i] - mg) * LOG2E);
    VMWAIT(0);
    st_sc(rW, lane == 0 ? ML_OFF + h * 128 + j * 16 : OOB, 0, gran(mg, lg, T));
    v4u v;
#pragma unroll 1
    for (int spin = 0; spin < SPIN_CAP; ++spin) {
      __builtin_amdgcn_s_sleep(MLSLEEP);
      MEMBAR();
      v = ld_sc(rW, ((lane & 7) ^ (b * 4)) * 16, ML_OFF + h * 128);
      if (none_bad(tagbad(v, T))) break;
    }
    MEMBAR();
#pragma unroll
    for (int i = 0; i < 8; ++i) w[i] = ld_sc(rW, lane < 36 ? (unsigned)(lane * 16) : OOB, GU_OFF + (i * 32 + h) * 640);
#pragma unroll
    for (int hf = 0; hf < 2; ++hf) {
      v4u hv;
#pragma unroll
      for (int e = 0; e < 4; ++e) {
        const float gt = rbf(u2f(w[2 * hf][e]) + u2f(w[4 + 2 * hf][e])), up = rbf(u2f(w[1 + 2 * hf][e]) + u2f(w[5 + 2 * hf][e]));
        const float t2 = 1.5957691216057308f * (gt + 0.044715f * gt * gt * gt);
        const float hh = rbf(gt / (1.0f + ex2(-t2 * LOG2E))) * up;
        hv[e] = cvtpk(hh, hh);
      }
      if (lane < 36) *(LAS v4u*)(L + L_HD + (lane >= 18 ? HD_S - 72 : 0) + hf * 72 + lane * 4) = hv;
    }
    STAMP(13);
    const float ms = u2f(v.x);
    float M = fmaxf(ms, dpp<XOR1>(ms)); M = fmaxf(M, dpp<XOR2>(M));
    float Ls = u2f(v.z) * ex2((ms - M) * LOG2E);
    Ls += dpp<XOR1>(Ls); Ls += dpp<XOR2>(Ls);
    const float inv = 1.0f / Ls;
    if (lane == 0) *(LAS f32x4*)(Lf + L_FW) = f32x4{ex2((mw[0] - M) * LOG2E) * inv, ex2((mw[1] - M) * LOG2E) * inv, ex2((mw[2] - M) * LOG2E) * inv, ex2((mw[3] - M) * LOG2E) * inv};
    lds_signal(L + L_FL + F_FW, lane);
    {
      const unsigned t = __builtin_amdgcn_raw_buffer_load_b32(rC, lane < 6 ? (unsigned)(r * 6 + lane) * 128u : OOB, 0, 0);
      asm volatile("" :: "v"(t));
    }
    {
      const unsigned o = lane < 8 ? OG_OFF + h * 8192 + (lane >> 1) * 2048 + j * 256 + (lane & 1) * 128 : lane < 17 ? YG_OFF + bid * 1152 + (lane - 8) * 128 : OOB;
      const unsigned t = __builtin_amdgcn_raw_buffer_load_b32(rW, o, 0, WSPF == 1 ? AUX_SC1 : 0);
      asm volatile("" :: "v"(t));
    }
    {
      const char* tp = (lane & 4) ? wo + (size_t)(128 * h + 42 * (lane & 3)) * 1152 + j * 144 : wd + (size_t)(144 * hp + 47 * (lane & 3)) * 1152 + j * 144;
      if (lane & 8) tp = out;
      unsigned t = 0;
      if (lane < 9) t = __builtin_nontemporal_load((const GAS unsigned*)tp);
      asm volatile("" :: "v"(t));
    }
    STAMP(5);
  }
}

extern "C" void kernel_launch(void* const* d_in, const int* in_sizes, int n_in, void* d_out, int out_size, void* d_ws, size_t ws_size,
                              hipStream_t stream) {
  fused_decode<<<256, 384, 0, stream>>>((const char*)d_in[0], (const char*)d_in[1], (const char*)d_in[2], (const char*)d_in[3],
                                        (const char*)d_in[4], (const char*)d_in[6], (const char*)d_in[7], (const char*)d_in[8],
                                        (char*)d_out, (char*)d_ws);
}
```

```cpp
#include <hip/hip_runtime.h>
#include <hip/hip_bf16.h>

#define WIN 16
#define NPRE 2
#define PINFL 6
#define TAUK 240
#define TAUV 216
#define LSTEP 80
#define WC0 3
#define WC1 4
#define WC2 5
#define WC3 7
#define WC4 9
#define WC5 12
#define QDELAY 32
#define PPOLL 0
#define TBAR 1
#define TPOS 99
#define MLSLEEP 80
#define TOUCH 1
#define LATEPF 1
#define OUTSC 0
#define TOFF 40
#define XBIAS 0
#define WSPF 1
#define TG0 13
#define TGN 3
#define CTB 1
#define DIAG 0

#define GAS __attribute__((address_space(1)))
#define LAS __attribute__((address_space(3)))
typedef unsigned v4u __attribute__((ext_vector_type(4)));
typedef unsigned v3u __attribute__((ext_vector_type(3)));
typedef unsigned v2u __attribute__((ext_vector_type(2)));
typedef float f32x4 __attribute__((ext_vector_type(4)));
typedef float f32x2 __attribute__((ext_vector_type(2)));
typedef __bf16 bf16x8 __attribute__((ext_vector_type(8)));
typedef __bf16 bf16x2 __attribute__((ext_vector_type(2)));
typedef unsigned long long v8q __attribute__((ext_vector_type(8)));
typedef unsigned long long v2q __attribute__((ext_vector_type(2)));
typedef __amdgpu_buffer_rsrc_t rsrc_t;

constexpr int E_OFF = 0;
constexpr int RT_OFF = 1024;
constexpr int QG_OFF = 4096;
constexpr int ML_OFF = QG_OFF + 262144;
constexpr int GU_OFF = ML_OFF + 4096;
constexpr int OG_OFF = GU_OFF + 163840;
constexpr int YG_OFF = OG_OFF + 262144;
constexpr int WS_USED = YG_OFF + 294912;
constexpr int TS_OFF = 4 << 20;

constexpr int L_XN = 0, XN_S = 580;
constexpr int L_HD = 1160, HD_S = 148;
constexpr int L_QB = L_HD + 2 * HD_S;
constexpr int L_OW = L_QB + 64;
constexpr int L_YW = L_OW + 512;
constexpr int L_ML = L_YW + 1056;
constexpr int L_FW = L_ML + 8;
constexpr int L_FL = L_FW + 4;
constexpr int F_KD = 0, F_Q = 4, F_T = 5, F_TOK = 6, F_XN = 7, F_FW = 8, F_TK = 9;
constexpr int L_END = L_FL + 12;
static_assert(L_FL % 4 == 0 && L_OW % 4 == 0 && L_YW % 4 == 0 && L_QB % 4 == 0 && L_FW % 4 == 0, "16-byte aligned LDS pieces");

constexpr int AUX_NT = 2, AUX_SC1 = 16;
constexpr unsigned OOB = 0x80000000u;
constexpr unsigned CODE_BYTES = 18432;
constexpr int SPIN_CAP = 1 << 18;
constexpr float LOG2E = 1.4426950408889634f;

__device__ __forceinline__ rsrc_t mk(const void* p, unsigned bytes) { return __builtin_amdgcn_make_buffer_rsrc((void*)p, 0, bytes, 0x00020000); }
__device__ __forceinline__ v4u ld_nt(rsrc_t r, unsigned vo, unsigned so) { return __builtin_amdgcn_raw_buffer_load_b128(r, vo, so, AUX_NT); }
__device__ __forceinline__ v4u ld_sc(rsrc_t r, unsigned vo, unsigned so) { return __builtin_amdgcn_raw_buffer_load_b128(r, vo, so, AUX_SC1); }
__device__ __forceinline__ void st_sc(rsrc_t r, unsigned vo, unsigned so, v4u v) { __builtin_amdgcn_raw_buffer_store_b128(v, r, vo, so, AUX_SC1); }
__device__ __forceinline__ float u2f(unsigned u) { return __builtin_bit_cast(float, u); }
__device__ __forceinline__ unsigned f2u(float f) { return __builtin_bit_cast(unsigned, f); }
__device__ __forceinline__ unsigned cvtpk(float lo, float hi) {
  bf16x2 r = {(__bf16)lo, (__bf16)hi};
  return __builtin_bit_cast(unsigned, r);
}
__device__ __forceinline__ float rbf(float f) { return (float)(__bf16)f; }
__device__ __forceinline__ float ex2(float f) { return __builtin_amdgcn_exp2f(f); }
__device__ __forceinline__ float dot2(unsigned a, unsigned b, float c) {
  return __builtin_amdgcn_fdot2_f32_bf16(__builtin_bit_cast(bf16x2, a), __builtin_bit_cast(bf16x2, b), c, false);
}
__device__ __forceinline__ f32x4 mfma(v4u a, v4u b, f32x4 c) {
  return __builtin_amdgcn_mfma_f32_16x16x32_bf16(__builtin_bit_cast(bf16x8, a), __builtin_bit_cast(bf16x8, b), c, 0, 0, 0);
}
template <int CTRL> __device__ __forceinline__ float dpp(float x) { return u2f(__builtin_amdgcn_mov_dpp(f2u(x), CTRL, 0xf, 0xf, true)); }
template <int CTRL> __device__ __forceinline__ unsigned dppu(unsigned x) { return __builtin_amdgcn_mov_dpp(x, CTRL, 0xf, 0xf, true); }
constexpr int XOR1 = 0xB1, XOR2 = 0x4E, XOR7 = 0x141, XOR8 = 0x128, ROR4 = 0x124, BCAST = 0x150;
__device__ __forceinline__ float add16(float a, float b) {
  auto s = __builtin_amdgcn_permlane16_swap(f2u(a), f2u(b), false, false); return u2f(s[0]) + u2f(s[1]); }
__device__ __forceinline__ float add32(float a, float b) {
  auto s = __builtin_amdgcn_permlane32_swap(f2u(a), f2u(b), false, false); return u2f(s[0]) + u2f(s[1]); }
__device__ __forceinline__ float sum16x(float x) { return add16(x, x); }
__device__ __forceinline__ float sum32x(float x) { return add32(x, x); }
__device__ __forceinline__ float max16x(float x) { auto s = __builtin_amdgcn_permlane16_swap(f2u(x), f2u(x), false, false); return fmaxf(u2f(s[0]), u2f(s[1])); }
__device__ __forceinline__ float max32x(float x) { auto s = __builtin_amdgcn_permlane32_swap(f2u(x), f2u(x), false, false); return fmaxf(u2f(s[0]), u2f(s[1])); }
__device__ __forceinline__ float rowsum(float x) { x += dpp<XOR1>(x); x += dpp<XOR2>(x); x += dpp<XOR7>(x); x += dpp<XOR8>(x); return x; }
__device__ __forceinline__ float rowmax(float x) { x = fmaxf(x, dpp<XOR1>(x)); x = fmaxf(x, dpp<XOR2>(x)); x = fmaxf(x, dpp<XOR7>(x)); x = fmaxf(x, dpp<XOR8>(x)); return x; }
#define SB() __builtin_amdgcn_sched_barrier(0)
#define MEMBAR() asm volatile("" ::: "memory")
#define LDS_FENCE() asm volatile("s_waitcnt lgkmcnt(0)" ::: "memory")
#define BARRIER() do { SB(); asm volatile("s_barrier" ::: "memory"); SB(); } while (0)
#define LDS_BARRIER() do { SB(); asm volatile("s_waitcnt lgkmcnt(0)\n\ts_barrier" ::: "memory"); SB(); } while (0)
#define VMWAIT(n) asm volatile("s_waitcnt vmcnt(%0)" :: "i"(n) : "memory")
__device__ __forceinline__ unsigned lds_ld(LAS unsigned* p) { return __hip_atomic_load(p, __ATOMIC_RELAXED, __HIP_MEMORY_SCOPE_WORKGROUP); }
__device__ __forceinline__ void lds_wait(LAS unsigned* p) {
  while (__builtin_amdgcn_readfirstlane(lds_ld(p)) == 0) __builtin_amdgcn_s_sleep(1);
  MEMBAR();
}
__device__ __forceinline__ void lds_wait4(LAS unsigned* p, int lane) {
  while (__builtin_amdgcn_ballot_w64(lds_ld(p + (lane & 3)) == 0) != 0ull) __builtin_amdgcn_s_sleep(4);
  MEMBAR();
}
__device__ __forceinline__ void lds_signal(LAS unsigned* p, int lane) {
  LDS_FENCE();
  if (lane == 0) __hip_atomic_store(p, 1u, __ATOMIC_RELAXED, __HIP_MEMORY_SCOPE_WORKGROUP);
}
__device__ __forceinline__ bool none_bad(unsigned bad) { return __builtin_amdgcn_ballot_w64(bad != 0u) == 0ull; }
__device__ __forceinline__ unsigned tagbad(const v4u& v, unsigned T) { return (v.y ^ T) | (v.w ^ T); }
__device__ __forceinline__ v4u gran(float a, float b, unsigned T) { return v4u{f2u(a), T, f2u(b), T}; }
__device__ __forceinline__ v4u afrag(unsigned dup, const v4u& m) { return v4u{dup & m.x, dup & m.y, dup & m.z, dup & m.w}; }
template <int D> __device__ __forceinline__ void poll2(rsrc_t rW, unsigned vo, unsigned so, unsigned T, v4u& r0, v4u& r1) {
#pragma unroll 1
  for (int spin = 0; spin < SPIN_CAP; ++spin) {
    MEMBAR();
    r0 = ld_sc(rW, vo, so); r1 = ld_sc(rW, vo, so + D);
    if (none_bad(tagbad(r0, T) | tagbad(r1, T))) break;
  }
}

#define STAMP(slot) do {} while (0)
#define STAMP15() do {} while (0)

#define PACE(t16) do { const unsigned need_ = tk0 + ((unsigned)(t16) >> 4); int lag_; \
    while ((lag_ = (int)((unsigned)__builtin_amdgcn_s_memrealtime() - need_)) < 0) __builtin_amdgcn_s_sleep(1); \
    const int cls_ = (int)(((unsigned)lag_ * (65536u / LSTEP)) >> 16); \
    asm volatile("s_cmp_gt_i32 %0, 5\n\ts_cbranch_scc1 1f\n\ts_waitcnt vmcnt(%1)\n\ts_cmp_eq_i32 %0, 5\n\ts_cbranch_scc1 1f\n\ts_waitcnt vmcnt(%2)\n\t" \
                 "s_cmp_eq_i32 %0, 4\n\ts_cbranch_scc1 1f\n\ts_waitcnt vmcnt(%3)\n\ts_cmp_eq_i32 %0, 3\n\ts_cbranch_scc1 1f\n\ts_waitcnt vmcnt(%4)\n\t" \
                 "s_cmp_eq_i32 %0, 2\n\ts_cbranch_scc1 1f\n\ts_waitcnt vmcnt(%5)\n\ts_cmp_eq_i32 %0, 1\n\ts_cbranch_scc1 1f\n\ts_waitcnt vmcnt(%6)\n1:" \
                 :: "s"(cls_), "i"(WC5 - 1), "i"(WC4 - 1), "i"(WC3 - 1), "i"(WC2 - 1), "i"(WC1 - 1), "i"(WC0 - 1) : "scc", "memory"); } while (0)

template <int N, int M> __device__ __forceinline__ f32x4 gemv(f32x4 a0, const unsigned (&xv)[N], const v4u& am, const v4u (&w)[M]) {
  f32x4 a1 = {0.f, 0.f, 0.f, 0.f};
#pragma unroll
  for (int i = 0; i < N; ++i) { if (i & 1) a1 = mfma(afrag(xv[i], am), w[i], a1); else a0 = mfma(afrag(xv[i], am), w[i], a0); }
  return a0 + a1;
}

__global__ void __launch_bounds__(384, 2)
fused_decode(const char* xg_, const char* kc_, const char* vc_, const char* wq_, const char* wo_, const char* wg_, const char* wu_, const char* wd_,
             char* out_, char* ws_) {
  v8q a8; v2q a2;
  asm volatile("s_load_dwordx16 %0, %2, 0x0\n\ts_load_dwordx4 %1, %2, 0x40" : "=&s"(a8), "=&s"(a2) : "s"(__builtin_amdgcn_kernarg_segment_ptr()));
  SB();
  __shared__ __attribute__((aligned(16))) unsigned smem[L_END];
  LAS unsigned* L = (LAS unsigned*)smem;
  LAS float* Lf = (LAS float*)smem;
  const int bid = blockIdx.x, xc = bid & 7, r = bid >> 3;
  int lane = threadIdx.x & 63;
  const int wave = __builtin_amdgcn_readfirstlane(threadIdx.x >> 6);
  const rsrc_t rC = mk((const void*)(__builtin_amdgcn_s_getpc() & ~255ull), CODE_BYTES);
  const unsigned cv = __builtin_amdgcn_raw_buffer_load_b32(rC, 0, min((unsigned)(r * 6 + wave) * 128u, CODE_BYTES - 128u), 0);
  const int h = xc + 8 * (r & 3), j = r >> 2, s = j & 3, b = j >> 2;
  unsigned cb = ((b * 32 + h) * 4 + s) * 524288u;
  unsigned vo = wave * 1024 + lane * 16;
  if (lane < 12) L[L_FL + lane] = 0u;
  asm volatile("" : "+v"(vo));
  SB();
  asm volatile("s_waitcnt lgkmcnt(0)\n\ts_barrier" : "+s"(a8), "+s"(a2), "+s"(cb) :: "memory");
  SB();
  const char* xg = (const char*)a8[0]; const char* kc = (const char*)a8[1]; const char* vc = (const char*)a8[2]; const char* wq = (const char*)a8[3];
  const char* wo = (const char*)a8[4]; const char* wg = (const char*)a8[5]; const char* wu = (const char*)a8[6]; const char* wd = (const char*)a8[7];
  char* out = (char*)a2[0]; char* ws = (char*)a2[1];

  if (__builtin_expect(wave < 4, 1)) {
    const int sw = wave;
    const char* kb = kc + cb;
    const char* vb = vc + cb;
    const rsrc_t rK = mk(kb, 524288), rV = mk(vb, 524288);
    constexpr int NVB = 8 - NPRE;
    constexpr int P0 = PINFL / 2;
    v4u a[WIN], pre[NPRE * 16];
#pragma unroll
    for (int i = 0; i < P0; ++i) { a[i] = ld_nt(rK, vo, i * 4096); pre[i] = ld_nt(rV, vo, NVB * 65536 + i * 4096); }
    SB();
    asm volatile("" : "+v"(lane));
#pragma unroll
    for (int i = P0; i < NPRE * 16; ++i) { VMWAIT(PINFL - 1); pre[i] = ld_nt(rV, vo, NVB * 65536 + i * 4096); SB(); }
#pragma unroll
    for (int i = P0; i < WIN; ++i) { VMWAIT(PINFL - 1); a[i] = ld_nt(rK, vo, i * 4096); SB(); }
    if (sw == 0) STAMP(16);
    const int g = lane >> 4, c = lane & 15;
    lds_wait(L + L_FL + F_Q);
    asm volatile("" :: "v"(cv));
    const v4u q = *(LAS v4u*)(L + L_QB + c * 4);
    const unsigned tk0 = __builtin_amdgcn_readfirstlane(L[L_FL + F_TK]);
    if (sw == 0) STAMP(17);
    float sc[8];
#pragma unroll
    for (int i = 0; i < 8; ++i) sc[i] = 0.f;
#pragma unroll 1
    for (int blk = 0; blk < 8; ++blk) {
      const rsrc_t rN = mk(blk < 7 ? kb : vb, 524288);
      const unsigned sN = blk < 7 ? blk * 65536 + 65536 : 0;
      float x[16];
#pragma unroll
      for (int i = 0; i < 16; ++i) {
        const v4u k = a[i];
        float d = dot2(k.x, q.x, 0.f); d = dot2(k.y, q.y, d); d = dot2(k.z, q.z, d); d = dot2(k.w, q.w, d);
        x[i] = d;
        asm volatile("" : "+v"(x[i]));
        SB();
        if (i & 1) {
          PACE((NPRE * 16 + 7 + blk * 16 + i) * TAUK);
          a[i - 1] = ld_nt(rN, vo, sN + (i - 1) * 4096);
          a[i] = ld_nt(rN, vo, sN + i * 4096);
          SB();
        }
      }
#define TR_STEP(HALF, CTRL) _Pragma("unroll") for (int u = 0; u < (HALF); ++u) { const float lo_ = x[u], hi_ = x[u + (HALF)]; \
        const float t1_ = lo_ + dpp<CTRL>(lo_), t2_ = hi_ + dpp<CTRL>(hi_); x[u] = (c & (HALF)) ? t2_ : t1_; }
      TR_STEP(8, XOR8) TR_STEP(4, XOR7) TR_STEP(2, XOR2) TR_STEP(1, XOR1)
#undef TR_STEP
#pragma unroll
      for (int i = 0; i < 7; ++i) sc[i] = sc[i + 1];
      sc[7] = rbf(rbf(x[0]) * 0.08838834764831845f);
    }
    float m = sc[0];
#pragma unroll
    for (int i = 1; i < 8; ++i) m = fmaxf(m, sc[i]);
    m = max32x(max16x(rowmax(m)));
    float l = 0.f;
    unsigned pp[8];
#pragma unroll
    for (int i = 0; i < 8; ++i) { const float p = ex2((sc[i] - m) * LOG2E); l += p; pp[i] = cvtpk(p, p); }
    l = sum32x(sum16x(rowsum(l)));
    if (lane == 0) { Lf[L_ML + 2 * sw] = m; Lf[L_ML + 2 * sw + 1] = l; }
    lds_signal(L + L_FL + F_KD + sw, lane);
    if (sw == 0) STAMP(18);
    const unsigned hm = (c & 1) ? 0xffff0000u : 0x0000ffffu;
    const int mt = (c & 7) >> 1;
    const v4u am = {mt == 0 ? hm : 0u, mt == 1 ? hm : 0u, mt == 2 ? hm : 0u, mt == 3 ? hm : 0u};
    const v4u pm = {c < 8 ? am.x : 0u, c < 8 ? am.y : 0u, c < 8 ? am.z : 0u, c < 8 ? am.w : 0u};
    f32x4 acc = {0.f, 0.f, 0.f, 0.f};
#define PVM(I, P, V) { acc = mfma(afrag(dppu<BCAST + (I)>(P), pm), (V), acc); asm volatile("" : "+v"(acc)); SB(); }
#define PV2(I) { PVM(I, pp[0], a[I]) PVM((I) + 1, pp[0], a[(I) + 1]) PACE((NPRE * 16 + 128) * TAUK + (blk * 16 + (I) + 8) * TAUV); \
      a[I] = ld_nt(rV, vo, sN + (I) * 4096); a[(I) + 1] = ld_nt(rV, vo, sN + ((I) + 1) * 4096); SB(); }
#define PV16(P, A, O) PVM(0, P, A[O + 0]) PVM(1, P, A[O + 1]) PVM(2, P, A[O + 2]) PVM(3, P, A[O + 3]) PVM(4, P, A[O + 4]) PVM(5, P, A[O + 5]) \
      PVM(6, P, A[O + 6]) PVM(7, P, A[O + 7]) PVM(8, P, A[O + 8]) PVM(9, P, A[O + 9]) PVM(10, P, A[O + 10]) PVM(11, P, A[O + 11]) \
      PVM(12, P, A[O + 12]) PVM(13, P, A[O + 13]) PVM(14, P, A[O + 14]) PVM(15, P, A[O + 15])
    const unsigned pq0 = pp[NVB], pq1 = pp[NVB + 1];
#pragma unroll 1
    for (int blk = 0; blk < NVB - 1; ++blk) {
      const unsigned sN = blk * 65536 + 65536;
      PV2(0) PV2(2) PV2(4) PV2(6) PV2(8) PV2(10) PV2(12) PV2(14)
#pragma unroll
      for (int i = 0; i < NVB - 1; ++i) pp[i] = pp[i + 1];
      if (blk == 0) { PV16(pq0, pre, 0) }
      if (blk == 1) { PV16(pq1, pre, 16) }
    }
    const int hp = 4 * (h & 7) + (h >> 3);
    v4u wz[9], wy[8];
    const rsrc_t rD = mk(wd + (size_t)(144 * hp + 36 * sw) * 1152 + j * 144, 36 * 1152 - j * 144);
    const rsrc_t rO = mk(wo + (size_t)(128 * h + 32 * sw) * 1152 + j * 144, 32 * 1152 - j * 144);
    const unsigned to = (c < 9 ? (unsigned)(c * 16) : OOB) + g * 1152;
#define TILES(P) if (TPOS == (P)) { _Pragma("unroll") for (int i = 0; i < 9; ++i) wz[i] = ld_nt(rD, to, i * 4608); \
      _Pragma("unroll") for (int i = 0; i < 8; ++i) wy[i] = ld_nt(rO, to, i * 4608); SB(); }
#define TL1(n) { if ((n) < 9) wz[(n) < 9 ? (n) : 0] = ld_nt(rD, to, (n) * 4608); else if ((n) < 17) wy[(n) >= 9 && (n) < 17 ? (n) - 9 : 0] = ld_nt(rO, to, ((n) - 9) * 4608); }
#define TG(I) if (TPOS == 99 && (I) >= TG0) { _Pragma("unroll") for (int i = 0; i < TGN; ++i) TL1(((I) - TG0) * TGN + i) SB(); }
    if (TBAR && TPOS == 0) BARRIER();
    TILES(0)
    const rsrc_t rW = mk(ws, WS_USED);
    lds_wait(L + L_FL + F_FW);
    const unsigned T = __builtin_amdgcn_readfirstlane(L[L_FL + F_T]);
    float fw = Lf[L_FW + sw];
    unsigned hx[9];
#pragma unroll
    for (int i = 0; i < 9; ++i) hx[i] = L[L_HD + (c >> 3) * HD_S + 36 * sw + g + 4 * i];
    const unsigned src = (c >> 3) ? 0x0302u : 0x0100u, sel = (c & 1) ? (src << 16) | 0x0c0cu : 0x0c0c0000u | src;
    v4u sl = {mt == 0 ? sel : 0x0c0c0c0cu, mt == 1 ? sel : 0x0c0c0c0cu, mt == 2 ? sel : 0x0c0c0c0cu, mt == 3 ? sel : 0x0c0c0c0cu};
    unsigned a_ow = (L_OW + sw * 128 + c * 8 + g * 4) * 4, a_or = (L_OW + sw * 32 + (c & 7) * 4 + (c >> 3) * 2) * 4, a_og = g == 0 ? c * 16 : OOB;
    const int yk = (sw & 1) * 16 + (lane >> 2), ykb = yk >= 9 ? 1 : 0;
    unsigned a_yr = (L_YW + ((ykb * 2 + ((lane & 3) >> 1)) * 16 + yk - ykb * 9) * 4 + (lane & 1) * 2) * 4, a_yg = (sw == 0 || lane < 8) ? lane * 16 : OOB;
    const int oo = (h >= 9 ? 1152 - 144 : 0) + 144 * j + 16 * h + (lane & 3) * 4;
    unsigned xres = 0;
    if (sw == 2 && h < 18) xres = *(const GAS unsigned*)(xg + oo);
    asm volatile("" : "+v"(fw), "+v"(sl), "+v"(a_ow), "+v"(a_or), "+v"(a_og), "+v"(a_yr), "+v"(a_yg));
    asm volatile("" : "+v"(hx[0]), "+v"(hx[1]), "+v"(hx[2]), "+v"(hx[3]), "+v"(hx[4]), "+v"(hx[5]), "+v"(hx[6]), "+v"(hx[7]), "+v"(hx[8]));
    SB();
    PVM(0, pp[0], a[0]) PVM(1, pp[0], a[1]) PVM(2, pp[0], a[2]) PVM(3, pp[0], a[3]) PVM(4, pp[0], a[4]) PVM(5, pp[0], a[5]) PVM(6, pp[0], a[6]) TG(6) PVM(7, pp[0], a[7]) TG(7)
    PVM(8, pp[0], a[8]) TG(8) PVM(9, pp[0], a[9]) TG(9) PVM(10, pp[0], a[10]) TG(10) PVM(11, pp[0], a[11]) TG(11) TILES(12) PVM(12, pp[0], a[12]) TG(12) TILES(13)
    PVM(13, pp[0], a[13]) TG(13) TILES(14) PVM(14, pp[0], a[14]) TG(14) TILES(15) PVM(15, pp[0], a[15])
    if (lane < 32) *(LAS f32x4*)((LAS char*)L + a_ow) = acc * fw;
    if (sw == 0) STAMP(19);
    TILES(16)
    LDS_BARRIER();
    {
      f32x2 t = *(LAS f32x2*)((LAS char*)L + a_or);
#pragma unroll
      for (int i = 1; i < 4; ++i) t += *(LAS f32x2*)((LAS char*)L + a_or + i * 512);
      st_sc(rW, a_og, OG_OFF + h * 8192 + sw * 2048 + j * 256, gran(t.x, t.y, T));
      SB();
    }
    if (sw == 0) STAMP(6);
    TILES(17)
    if (TPOS == 99) { _Pragma("unroll") for (int i = (15 - TG0) * TGN; i < 17; ++i) TL1(i) SB(); }
    f32x4 y = gemv<9>(f32x4{0.f, 0.f, 0.f, 0.f}, hx, am, wz);
    asm volatile("" : "+v"(y));
    if (sw == 0) STAMP(25);
    v4u p0, p1;
    poll2<1024>(rW, lane * 16, OG_OFF + h * 8192 + sw * 2048, T, p0, p1);
    if (sw == 0) STAMP(7);
    {
      const float e = add32(add16(u2f(p0.x), u2f(p0.z)), add16(u2f(p1.x), u2f(p1.z)));
      auto eb = __builtin_amdgcn_permlane32_swap(f2u(e), f2u(e), false, false);
      unsigned pk = cvtpk(u2f(eb[0]), u2f(eb[1]));
      pk = __builtin_amdgcn_update_dpp(pk, pk, XOR8, 0xc, 0xf, false);
      f32x4 y1 = {0.f, 0.f, 0.f, 0.f};
#define YM(I, ACC) { const unsigned bc = dppu<BCAST + (I)>(pk); ACC = mfma(v4u{__builtin_amdgcn_perm(0u, bc, sl.x), __builtin_amdgcn_perm(0u, bc, sl.y), \
        __builtin_amdgcn_perm(0u, bc, sl.z), __builtin_amdgcn_perm(0u, bc, sl.w)}, wy[I], ACC); }
      YM(0, y) YM(1, y1) YM(2, y) YM(3, y1) YM(4, y) YM(5, y1) YM(6, y) YM(7, y1)
      y += y1;
    }
    *(LAS f32x4*)(Lf + L_YW + sw * 256 + lane * 4) = y;
    LDS_BARRIER();
    if (sw < 2) {
      f32x2 t = *(LAS f32x2*)((LAS char*)L + a_yr);
#pragma unroll
      for (int i = 1; i < 4; ++i) t += *(LAS f32x2*)((LAS char*)L + a_yr + i * 1024);
      st_sc(rW, a_yg, YG_OFF + (j * 32 + h) * 1152 + sw * 1024, gran(t.x, t.y, T));
      if (sw == 0) STAMP(20);
    } else if (sw == 2 && h < 18) {
      poll2<16 * 1152>(rW, (lane >> 2) * 1152 + (lane & 3) * 16, YG_OFF + j * 32 * 1152 + h * 64, T, p0, p1);
      float y0 = u2f(p0.x) + u2f(p1.x), y1 = u2f(p0.z) + u2f(p1.z);
      y0 += dpp<ROR4>(y0); y1 += dpp<ROR4>(y1);
      y0 += dpp<XOR8>(y0); y1 += dpp<XOR8>(y1);
      y0 = sum32x(add16(y0, y1));
      auto yy = __builtin_amdgcn_permlane16_swap(f2u(y0), f2u(y0), false, false);
      const unsigned res = cvtpk(u2f(xres << 16) + u2f(yy[0]), u2f(xres & 0xffff0000u) + u2f(yy[1]));
      __builtin_amdgcn_raw_buffer_store_b32(res, mk(out, 2304), lane < 4 ? (unsigned)oo : OOB, 0, OUTSC);
      if (bid == 0 && lane == 0) __builtin_amdgcn_raw_buffer_store_b32(T, rW, E_OFF, 0, AUX_SC1);
      STAMP15();
    }
    return;
  }

  const bool isA = wave == 4;
  STAMP(isA ? 0 : 8);
  const int g = lane >> 4, c = lane & 15;
  const int hp = 4 * (h & 7) + (h >> 3);
  const rsrc_t rW = mk(ws, WS_USED);
  v4u w[36];
  unsigned E = 0, ta = 0;
  const int u = (r & 15) + 16 * (xc & 3), qq = (r >> 4) + 2 * (xc >> 2);
  const int gm = j & 1, half = (j >> 1) & 1, rh = j >> 2;
  const unsigned stride = isA ? 8192 : 9216, cofs = isA ? u * 128 : hp * 288 + half * 144;
  int row0 = isA ? 144 * qq : 288 * rh;
  const rsrc_t rG = mk((isA ? wq : gm ? wu : wg) + cofs, 576 * stride - cofs);
  const unsigned gvo = (c < (isA ? 8 : 9) ? (unsigned)(c * 16) : OOB) + g * stride;
  if (isA) {
    __builtin_amdgcn_s_setprio(3);
    ta = (unsigned)__builtin_amdgcn_s_memrealtime();
    E = __builtin_amdgcn_raw_buffer_load_b32(rW, E_OFF, 0, AUX_SC1);
  } else {
    __builtin_amdgcn_s_setprio(2);
    v3u xr[3];
    const rsrc_t rX = mk(xg, 2304);
#pragma unroll
    for (int k = 0; k < 3; ++k) xr[k] = __builtin_amdgcn_raw_buffer_load_b96(rX, (lane >> 5) * 1152 + (lane & 31) * 12, k * 384, 0);
    float xv[18];
#pragma unroll
    for (int k = 0; k < 3; ++k)
#pragma unroll
      for (int e = 0; e < 3; ++e) { xv[k * 6 + 2 * e] = u2f(xr[k][e] << 16); xv[k * 6 + 2 * e + 1] = u2f(xr[k][e] & 0xffff0000u); }
    float sum = 0.f;
#pragma unroll
    for (int i = 0; i < 18; ++i) sum += xv[i];
    const float mean = sum16x(rowsum(sum)) * (1.0f / 576.0f);
    float sq = 0.f;
#pragma unroll
    for (int i = 0; i < 18; ++i) { const float d = xv[i] - mean; sq = fmaf(d, d, sq); }
    const float var = sum16x(rowsum(sq)) * (1.0f / 576.0f);
    const float mb = rbf(mean), sd = rbf(__builtin_sqrtf(rbf(rbf(var) + 1e-5f))), rs = 1.0f / sd;
    LAS v2u* dst = (LAS v2u*)(L + L_XN + (lane >> 5) * XN_S + (lane & 31) * 6);
#pragma unroll
    for (int k = 0; k < 3; ++k)
#pragma unroll
      for (int e = 0; e < 3; ++e) {
        float n[2];
#pragma unroll
        for (int f = 0; f < 2; ++f) { const float d = rbf(xv[k * 6 + 2 * e + f] - mb); const float q0 = d * rs; n[f] = fmaf(fmaf(-q0, sd, d), rs, q0); }
        dst[k * 96 + e] = v2u{cvtpk(n[0], n[0]), cvtpk(n[1], n[1])};
      }
    lds_signal(L + L_FL + F_XN, lane);
    STAMP(9);
    __builtin_amdgcn_s_setprio(0);
  }
  const unsigned hm = (c & 1) ? 0xffff0000u : 0x0000ffffu;
  const int mt = (c & 7) >> 1;
  const v4u am = {mt == 0 ? hm : 0u, mt == 1 ? hm : 0u, mt == 2 ? hm : 0u, mt == 3 ? hm : 0u};
  unsigned T = 0;
  f32x4 ga = {0.f, 0.f, 0.f, 0.f};
#pragma unroll 1
  for (int t = 0; t < (isA ? 1 : 2); ++t, row0 += 144) {
    unsigned so = row0 * stride;
#pragma unroll
    for (int i = 0; i < 36; ++i) { w[i] = ld_nt(rG, gvo, so); so += 4 * stride; }
    SB();
    if (isA) {
      T = __builtin_amdgcn_readfirstlane(E) + 0x9E3779B9u;
      if (lane == 0) L[L_FL + F_T] = T;
      if (CTB && bid < 8) st_sc(rW, lane == 0 ? RT_OFF + bid * 16 : OOB, 0, v4u{ta, T, ta, T});
      lds_wait(L + L_FL + F_XN);
      STAMP(1);
    }
    asm volatile("" :: "v"(cv));
    unsigned xv[36];
#pragma unroll
    for (int i = 0; i < 36; ++i) xv[i] = L[L_XN + (c >> 3) * XN_S + row0 + g + 4 * i];
    SB();
    ga = gemv<36>(ga, xv, am, w);
  }
  if (isA) {
    const float s2 = dpp<XOR8>(ga[2]), s3 = dpp<XOR8>(ga[3]);
    const unsigned o = QG_OFF + (((u >> 1) * 2 + (g >> 1)) * 4 + qq) * 1024 + ((u & 1) * 64 + (c & 7) * 8 + (g & 1) * 4 + (c >> 3) * 2) * 8;
    st_sc(rW, o, 0, gran(c < 8 ? ga[0] : s2, c < 8 ? ga[1] : s3, T));
    STAMP(2);
    __builtin_amdgcn_s_sleep(QDELAY);
    float q0, q1;
#pragma unroll 1
    for (int spin = 0; spin < SPIN_CAP; ++spin) {
      MEMBAR();
#pragma unroll
      for (int i = 0; i < 4; ++i) w[i] = ld_sc(rW, lane * 16, QG_OFF + ((h * 2 + b) * 4 + i) * 1024);
      if (CTB) w[4] = ld_sc(rW, (lane & 7) * 16, RT_OFF);
      q0 = (u2f(w[0].x) + u2f(w[1].x)) + (u2f(w[2].x) + u2f(w[3].x));
      q1 = (u2f(w[0].z) + u2f(w[1].z)) + (u2f(w[2].z) + u2f(w[3].z));
      if (none_bad(tagbad(w[0], T) | tagbad(w[1], T) | tagbad(w[2], T) | tagbad(w[3], T))) break;
      STAMP(21);
      __builtin_amdgcn_s_sleep(16);
    }
    L[L_QB + lane] = cvtpk(q0, q1);
    {
      int d = 0;
      if (CTB) {
        d = (int)(w[4].x - ta);
        d = min(d, (int)dppu<XOR1>(d)); d = min(d, (int)dppu<XOR2>(d)); d = min(d, (int)dppu<XOR7>(d));
        d = none_bad(w[4].y ^ T) ? min(max(d, -500), 500) : 0;
      }
      if (lane == 0) L[L_FL + F_TK] = ta + d - TOFF;
    }
    lds_signal(L + L_FL + F_Q, lane);
    STAMP(3);
  } else {
    st_sc(rW, c < 9 ? (unsigned)((g >> 1) * 288 + c * 32 + (g & 1) * 16) : OOB, GU_OFF + bid * 640, __builtin_bit_cast(v4u, ga));
    STAMP(12);
    lds_wait4(L + L_FL + F_KD, lane);
    T = __builtin_amdgcn_readfirstlane(L[L_FL + F_T]);
    float mw[4], lw[4];
#pragma unroll
    for (int i = 0; i < 4; ++i) { mw[i] = Lf[L_ML + 2 * i]; lw[i] = Lf[L_ML + 2 * i + 1]; }
    const float mg = fmaxf(fmaxf(mw[0], mw[1]), fmaxf(mw[2], mw[3]));
    float lg = 0.f;
#pragma unroll
    for (int i = 0; i < 4; ++i) lg += lw[i] * ex2((mw[i] - mg) * LOG2E);
    VMWAIT(0);
    st_sc(rW, lane == 0 ? ML_OFF + h * 128 + j * 16 : OOB, 0, gran(mg, lg, T));
    v4u v;
#pragma unroll 1
    for (int spin = 0; spin < SPIN_CAP; ++spin) {
      __builtin_amdgcn_s_sleep(MLSLEEP);
      MEMBAR();
      v = ld_sc(rW, ((lane & 7) ^ (b * 4)) * 16, ML_OFF + h * 128);
      if (none_bad(tagbad(v, T))) break;
    }
    MEMBAR();
#pragma unroll
    for (int i = 0; i < 8; ++i) w[i] = ld_sc(rW, lane < 36 ? (unsigned)(lane * 16) : OOB, GU_OFF + (i * 32 + h) * 640);
#pragma unroll
    for (int hf = 0; hf < 2; ++hf) {
      v4u hv;
#pragma unroll
      for (int e = 0; e < 4; ++e) {
        const float gt = rbf(u2f(w[2 * hf][e]) + u2f(w[4 + 2 * hf][e])), up = rbf(u2f(w[1 + 2 * hf][e]) + u2f(w[5 + 2 * hf][e]));
        const float t2 = 1.5957691216057308f * (gt + 0.044715f * gt * gt * gt);
        const float hh = rbf(gt / (1.0f + ex2(-t2 * LOG2E))) * up;
        hv[e] = cvtpk(hh, hh);
      }
      if (lane < 36) *(LAS v4u*)(L + L_HD + (lane >= 18 ? HD_S - 72 : 0) + hf * 72 + lane * 4) = hv;
    }
    STAMP(13);
    const float ms = u2f(v.x);
    float M = fmaxf(ms, dpp<XOR1>(ms)); M = fmaxf(M, dpp<XOR2>(M));
    float Ls = u2f(v.z) * ex2((ms - M) * LOG2E);
    Ls += dpp<XOR1>(Ls); Ls += dpp<XOR2>(Ls);
    const float inv = 1.0f / Ls;
    if (lane == 0) *(LAS f32x4*)(Lf + L_FW) = f32x4{ex2((mw[0] - M) * LOG2E) * inv, ex2((mw[1] - M) * LOG2E) * inv, ex2((mw[2] - M) * LOG2E) * inv, ex2((mw[3] - M) * LOG2E) * inv};
    lds_signal(L + L_FL + F_FW, lane);
    {
      const unsigned t = __builtin_amdgcn_raw_buffer_load_b32(rC, lane < 6 ? (unsigned)(r * 6 + lane) * 128u : OOB, 0, 0);
      asm volatile("" :: "v"(t));
    }
    {
      const unsigned o = lane < 8 ? OG_OFF + h * 8192 + (lane >> 1) * 2048 + j * 256 + (lane & 1) * 128 : lane < 17 ? YG_OFF + bid * 1152 + (lane - 8) * 128 : OOB;
      const unsigned t = __builtin_amdgcn_raw_buffer_load_b32(rW, o, 0, WSPF == 1 ? AUX_SC1 : 0);
      asm volatile("" :: "v"(t));
    }
    {
      const char* tp = (lane & 4) ? wo + (size_t)(128 * h + 42 * (lane & 3)) * 1152 + j * 144 : wd + (size_t)(144 * hp + 47 * (lane & 3)) * 1152 + j * 144;
      if (lane & 8) tp = out;
      unsigned t = 0;
      if (lane < 9) t = __builtin_nontemporal_load((const GAS unsigned*)tp);
      asm volatile("" :: "v"(t));
    }
    STAMP(5);
  }
}

extern "C" void kernel_launch(void* const* d_in, const int* in_sizes, int n_in, void* d_out, int out_size, void* d_ws, size_t ws_size,
                              hipStream_t stream) {
  fused_decode<<<256, 384, 0, stream>>>((const char*)d_in[0], (const char*)d_in[1], (const char*)d_in[2], (const char*)d_in[3],
                                        (const char*)d_in[4], (const char*)d_in[6], (const char*)d_in[7], (const char*)d_in[8],
                                        (char*)d_out, (char*)d_ws);
}
```

```cpp
#include <hip/hip_runtime.h>
#include <hip/hip_bf16.h>

#define WIN 16
#define NPRE 2
#define PINFL 6
#define TAUK 224
#define TAUV 224
#define LSTEP 60
#define WC0 3
#define WC1 4
#define WC2 5
#define WC3 7
#define WC4 9
#define WC5 12
#define QDELAY 32
#define PPOLL 0
#define TBAR 1
#define TPOS 99
#define MLSLEEP 80
#define TOUCH 1
#define LATEPF 1
#define OUTSC 0
#define TOFF (-150)
#define XBIAS 0
#define WSPF 1
#define TG0 13
#define TGN 3
#define CTB 1
#define RC 1
#define RCT 170
#define RCN 2
#define RCA 1
#define RCS 6
#define RCW 64
#define DIAG 0

#define GAS __attribute__((address_space(1)))
#define LAS __attribute__((address_space(3)))
typedef unsigned v4u __attribute__((ext_vector_type(4)));
typedef unsigned v3u __attribute__((ext_vector_type(3)));
typedef unsigned v2u __attribute__((ext_vector_type(2)));
typedef float f32x4 __attribute__((ext_vector_type(4)));
typedef float f32x2 __attribute__((ext_vector_type(2)));
typedef __bf16 bf16x8 __attribute__((ext_vector_type(8)));
typedef __bf16 bf16x2 __attribute__((ext_vector_type(2)));
typedef unsigned long long v8q __attribute__((ext_vector_type(8)));
typedef unsigned long long v2q __attribute__((ext_vector_type(2)));
typedef __amdgpu_buffer_rsrc_t rsrc_t;

constexpr int E_OFF = 0;
constexpr int RT_OFF = 1024;
constexpr int QG_OFF = 4096;
constexpr int ML_OFF = QG_OFF + 262144;
constexpr int GU_OFF = ML_OFF + 4096;
constexpr int OG_OFF = GU_OFF + 163840;
constexpr int YG_OFF = OG_OFF + 262144;
constexpr int WS_USED = YG_OFF + 294912;
constexpr int TS_OFF = 4 << 20;

constexpr int L_XN = 0, XN_S = 580;
constexpr int L_HD = 1160, HD_S = 148;
constexpr int L_QB = L_HD + 2 * HD_S;
constexpr int L_OW = L_QB + 64;
constexpr int L_YW = L_OW + 512;
constexpr int L_ML = L_YW + 1056;
constexpr int L_FW = L_ML + 8;
constexpr int L_FL = L_FW + 4;
constexpr int F_KD = 0, F_Q = 4, F_T = 5, F_TOK = 6, F_XN = 7, F_FW = 8, F_TK = 9, F_PB = 10, F_PS = 11;
constexpr int L_END = L_FL + 12;
static_assert(L_FL % 4 == 0 && L_OW % 4 == 0 && L_YW % 4 == 0 && L_QB % 4 == 0 && L_FW % 4 == 0, "16-byte aligned LDS pieces");

constexpr int AUX_NT = 2, AUX_SC1 = 16;
constexpr unsigned OOB = 0x80000000u;
constexpr unsigned CODE_BYTES = 19200;
constexpr int SPIN_CAP = 1 << 18;
constexpr float LOG2E = 1.4426950408889634f;

__device__ __forceinline__ rsrc_t mk(const void* p, unsigned bytes) { return __builtin_amdgcn_make_buffer_rsrc((void*)p, 0, bytes, 0x00020000); }
__device__ __forceinline__ v4u ld_nt(rsrc_t r, unsigned vo, unsigned so) { return __builtin_amdgcn_raw_buffer_load_b128(r, vo, so, AUX_NT); }
__device__ __forceinline__ v4u ld_sc(rsrc_t r, unsigned vo, unsigned so) { return __builtin_amdgcn_raw_buffer_load_b128(r, vo, so, AUX_SC1); }
__device__ __forceinline__ void st_sc(rsrc_t r, unsigned vo, unsigned so, v4u v) { __builtin_amdgcn_raw_buffer_store_b128(v, r, vo, so, AUX_SC1); }
__device__ __forceinline__ float u2f(unsigned u) { return __builtin_bit_cast(float, u); }
__device__ __forceinline__ unsigned f2u(float f) { return __builtin_bit_cast(unsigned, f); }
__device__ __forceinline__ unsigned cvtpk(float lo, float hi) {
  bf16x2 r = {(__bf16)lo, (__bf16)hi};
  return __builtin_bit_cast(unsigned, r);
}
__device__ __forceinline__ float rbf(float f) { return (float)(__bf16)f; }
__device__ __forceinline__ float ex2(float f) { return __builtin_amdgcn_exp2f(f); }
__device__ __forceinline__ float dot2(unsigned a, unsigned b, float c) {
  return __builtin_amdgcn_fdot2_f32_bf16(__builtin_bit_cast(bf16x2, a), __builtin_bit_cast(bf16x2, b), c, false);
}
__device__ __forceinline__ f32x4 mfma(v4u a, v4u b, f32x4 c) {
  return __builtin_amdgcn_mfma_f32_16x16x32_bf16(__builtin_bit_cast(bf16x8, a), __builtin_bit_cast(bf16x8, b), c, 0, 0, 0);
}
template <int CTRL> __device__ __forceinline__ float dpp(float x) { return u2f(__builtin_amdgcn_mov_dpp(f2u(x), CTRL, 0xf, 0xf, true)); }
template <int CTRL> __device__ __forceinline__ unsigned dppu(unsigned x) { return __builtin_amdgcn_mov_dpp(x, CTRL, 0xf, 0xf, true); }
constexpr int XOR1 = 0xB1, XOR2 = 0x4E, XOR7 = 0x141, XOR8 = 0x128, ROR4 = 0x124, BCAST = 0x150;
__device__ __forceinline__ float add16(float a, float b) {
  auto s = __builtin_amdgcn_permlane16_swap(f2u(a), f2u(b), false, false); return u2f(s[0]) + u2f(s[1]); }
__device__ __forceinline__ float add32(float a, float b) {
  auto s = __builtin_amdgcn_permlane32_swap(f2u(a), f2u(b), false, false); return u2f(s[0]) + u2f(s[1]); }
__device__ __forceinline__ float sum16x(float x) { return add16(x, x); }
__device__ __forceinline__ float sum32x(float x) { return add32(x, x); }
__device__ __forceinline__ float max16x(float x) { auto s = __builtin_amdgcn_permlane16_swap(f2u(x), f2u(x), false, false); return fmaxf(u2f(s[0]), u2f(s[1])); }
__device__ __forceinline__ float max32x(float x) { auto s = __builtin_amdgcn_permlane32_swap(f2u(x), f2u(x), false, false); return fmaxf(u2f(s[0]), u2f(s[1])); }
__device__ __forceinline__ float rowsum(float x) { x += dpp<XOR1>(x); x += dpp<XOR2>(x); x += dpp<XOR7>(x); x += dpp<XOR8>(x); return x; }
__device__ __forceinline__ float rowmax(float x) { x = fmaxf(x, dpp<XOR1>(x)); x = fmaxf(x, dpp<XOR2>(x)); x = fmaxf(x, dpp<XOR7>(x)); x = fmaxf(x, dpp<XOR8>(x)); return x; }
#define SB() __builtin_amdgcn_sched_barrier(0)
#define MEMBAR() asm volatile("" ::: "memory")
#define LDS_FENCE() asm volatile("s_waitcnt lgkmcnt(0)" ::: "memory")
#define BARRIER() do { SB(); asm volatile("s_barrier" ::: "memory"); SB(); } while (0)
#define LDS_BARRIER() do { SB(); asm volatile("s_waitcnt lgkmcnt(0)\n\ts_barrier" ::: "memory"); SB(); } while (0)
#define VMWAIT(n) asm volatile("s_waitcnt vmcnt(%0)" :: "i"(n) : "memory")
__device__ __forceinline__ unsigned lds_ld(LAS unsigned* p) { return __hip_atomic_load(p, __ATOMIC_RELAXED, __HIP_MEMORY_SCOPE_WORKGROUP); }
__device__ __forceinline__ void lds_wait(LAS unsigned* p) {
  while (__builtin_amdgcn_readfirstlane(lds_ld(p)) == 0) __builtin_amdgcn_s_sleep(1);
  MEMBAR();
}
__device__ __forceinline__ void lds_wait4(LAS unsigned* p, int lane) {
  while (__builtin_amdgcn_ballot_w64(lds_ld(p + (lane & 3)) == 0) != 0ull) __builtin_amdgcn_s_sleep(4);
  MEMBAR();
}
__device__ __forceinline__ void lds_signal(LAS unsigned* p, int lane) {
  LDS_FENCE();
  if (lane == 0) __hip_atomic_store(p, 1u, __ATOMIC_RELAXED, __HIP_MEMORY_SCOPE_WORKGROUP);
}
__device__ __forceinline__ bool none_bad(unsigned bad) { return __builtin_amdgcn_ballot_w64(bad != 0u) == 0ull; }
__device__ __forceinline__ unsigned tagbad(const v4u& v, unsigned T) { return (v.y ^ T) | (v.w ^ T); }
__device__ __forceinline__ v4u gran(float a, float b, unsigned T) { return v4u{f2u(a), T, f2u(b), T}; }
__device__ __forceinline__ v4u afrag(unsigned dup, const v4u& m) { return v4u{dup & m.x, dup & m.y, dup & m.z, dup & m.w}; }
template <int D> __device__ __forceinline__ void poll2(rsrc_t rW, unsigned vo, unsigned so, unsigned T, v4u& r0, v4u& r1) {
#pragma unroll 1
  for (int spin = 0; spin < SPIN_CAP; ++spin) {
    MEMBAR();
    r0 = ld_sc(rW, vo, so); r1 = ld_sc(rW, vo, so + D);
    if (none_bad(tagbad(r0, T) | tagbad(r1, T))) break;
  }
}

#define STAMP(slot) do {} while (0)
#define STAMP15() do {} while (0)

#define PACE(t16) do { const unsigned need_ = tk0 + ((unsigned)(t16) >> 4); \
    while ((lag_ = (int)((unsigned)__builtin_amdgcn_s_memrealtime() - need_)) < 0) __builtin_amdgcn_s_sleep(1); \
    const int cls_ = (int)(((unsigned)lag_ * (65536u / LSTEP)) >> 16); \
    asm volatile("s_cmp_gt_i32 %0, 5\n\ts_cbranch_scc1 1f\n\ts_waitcnt vmcnt(%1)\n\ts_cmp_eq_i32 %0, 5\n\ts_cbranch_scc1 1f\n\ts_waitcnt vmcnt(%2)\n\t" \
                 "s_cmp_eq_i32 %0, 4\n\ts_cbranch_scc1 1f\n\ts_waitcnt vmcnt(%3)\n\ts_cmp_eq_i32 %0, 3\n\ts_cbranch_scc1 1f\n\ts_waitcnt vmcnt(%4)\n\t" \
                 "s_cmp_eq_i32 %0, 2\n\ts_cbranch_scc1 1f\n\ts_waitcnt vmcnt(%5)\n\ts_cmp_eq_i32 %0, 1\n\ts_cbranch_scc1 1f\n\ts_waitcnt vmcnt(%6)\n1:" \
                 :: "s"(cls_), "i"(WC5 - 1), "i"(WC4 - 1), "i"(WC3 - 1), "i"(WC2 - 1), "i"(WC1 - 1), "i"(WC0 - 1) : "scc", "memory"); } while (0)

#define BLOCK_END(n) if (RC) { if (sw == 0 && lane == 0) { L[L_FL + F_PS] = tk0 + lag_; MEMBAR(); __hip_atomic_store(L + L_FL + F_PB, (unsigned)(n), __ATOMIC_RELAXED, __HIP_MEMORY_SCOPE_WORKGROUP); } \
      tk0 = __builtin_amdgcn_readfirstlane(lds_ld(L + L_FL + F_TK)); }

template <int N, int M> __device__ __forceinline__ f32x4 gemv(f32x4 a0, const unsigned (&xv)[N], const v4u& am, const v4u (&w)[M]) {
  f32x4 a1 = {0.f, 0.f, 0.f, 0.f};
#pragma unroll
  for (int i = 0; i < N; ++i) { if (i & 1) a1 = mfma(afrag(xv[i], am), w[i], a1); else a0 = mfma(afrag(xv[i], am), w[i], a0); }
  return a0 + a1;
}

__global__ void __launch_bounds__(384, 2)
fused_decode(const char* xg_, const char* kc_, const char* vc_, const char* wq_, const char* wo_, const char* wg_, const char* wu_, const char* wd_,
             char* out_, char* ws_) {
  v8q a8; v2q a2;
  asm volatile("s_load_dwordx16 %0, %2, 0x0\n\ts_load_dwordx4 %1, %2, 0x40" : "=&s"(a8), "=&s"(a2) : "s"(__builtin_amdgcn_kernarg_segment_ptr()));
  SB();
  __shared__ __attribute__((aligned(16))) unsigned smem[L_END];
  LAS unsigned* L = (LAS unsigned*)smem;
  LAS float* Lf = (LAS float*)smem;
  const int bid = blockIdx.x, xc = bid & 7, r = bid >> 3;
  int lane = threadIdx.x & 63;
  const int wave = __builtin_amdgcn_readfirstlane(threadIdx.x >> 6);
  const rsrc_t rC = mk((const void*)(__builtin_amdgcn_s_getpc() & ~255ull), CODE_BYTES);
  const unsigned cv = __builtin_amdgcn_raw_buffer_load_b32(rC, 0, min((unsigned)(r * 6 + wave) * 128u, CODE_BYTES - 128u), 0);
  const int h = xc + 8 * (r & 3), j = r >> 2, s = j & 3, b = j >> 2;
  unsigned cb = ((b * 32 + h) * 4 + s) * 524288u;
  unsigned vo = wave * 1024 + lane * 16;
  if (lane < 12) L[L_FL + lane] = 0u;
  asm volatile("" : "+v"(vo));
  SB();
  asm volatile("s_waitcnt lgkmcnt(0)\n\ts_barrier" : "+s"(a8), "+s"(a2), "+s"(cb) :: "memory");
  SB();
  const char* xg = (const char*)a8[0]; const char* kc = (const char*)a8[1]; const char* vc = (const char*)a8[2]; const char* wq = (const char*)a8[3];
  const char* wo = (const char*)a8[4]; const char* wg = (const char*)a8[5]; const char* wu = (const char*)a8[6]; const char* wd = (const char*)a8[7];
  char* out = (char*)a2[0]; char* ws = (char*)a2[1];

  if (__builtin_expect(wave < 4, 1)) {
    const int sw = wave;
    const char* kb = kc + cb;
    const char* vb = vc + cb;
    const rsrc_t rK = mk(kb, 524288), rV = mk(vb, 524288);
    constexpr int NVB = 8 - NPRE;
    constexpr int P0 = PINFL / 2;
    v4u a[WIN], pre[NPRE * 16];
#pragma unroll
    for (int i = 0; i < P0; ++i) { a[i] = ld_nt(rK, vo, i * 4096); pre[i] = ld_nt(rV, vo, NVB * 65536 + i * 4096); }
    SB();
    asm volatile("" : "+v"(lane));
#pragma unroll
    for (int i = P0; i < NPRE * 16; ++i) { VMWAIT(PINFL - 1); pre[i] = ld_nt(rV, vo, NVB * 65536 + i * 4096); SB(); }
#pragma unroll
    for (int i = P0; i < WIN; ++i) { VMWAIT(PINFL - 1); a[i] = ld_nt(rK, vo, i * 4096); SB(); }
    if (sw == 0) STAMP(16);
    const int g = lane >> 4, c = lane & 15;
    lds_wait(L + L_FL + F_Q);
    asm volatile("" :: "v"(cv));
    const v4u q = *(LAS v4u*)(L + L_QB + c * 4);
    unsigned tk0 = __builtin_amdgcn_readfirstlane(L[L_FL + F_TK]);
    int lag_ = 0;
    if (sw == 0) STAMP(17);
    float sc[8];
#pragma unroll
    for (int i = 0; i < 8; ++i) sc[i] = 0.f;
#pragma unroll 1
    for (int blk = 0; blk < 8; ++blk) {
      const rsrc_t rN = mk(blk < 7 ? kb : vb, 524288);
      const unsigned sN = blk < 7 ? blk * 65536 + 65536 : 0;
      float x[16];
#pragma unroll
      for (int i = 0; i < 16; ++i) {
        const v4u k = a[i];
        float d = dot2(k.x, q.x, 0.f); d = dot2(k.y, q.y, d); d = dot2(k.z, q.z, d); d = dot2(k.w, q.w, d);
        x[i] = d;
        asm volatile("" : "+v"(x[i]));
        SB();
        if (i & 1) {
          PACE((NPRE * 16 + 7 + blk * 16 + i) * TAUK);
          a[i - 1] = ld_nt(rN, vo, sN + (i - 1) * 4096);
          a[i] = ld_nt(rN, vo, sN + i * 4096);
          SB();
        }
      }
#define TR_STEP(HALF, CTRL) _Pragma("unroll") for (int u = 0; u < (HALF); ++u) { const float lo_ = x[u], hi_ = x[u + (HALF)]; \
        const float t1_ = lo_ + dpp<CTRL>(lo_), t2_ = hi_ + dpp<CTRL>(hi_); x[u] = (c & (HALF)) ? t2_ : t1_; }
      TR_STEP(8, XOR8) TR_STEP(4, XOR7) TR_STEP(2, XOR2) TR_STEP(1, XOR1)
#undef TR_STEP
#pragma unroll
      for (int i = 0; i < 7; ++i) sc[i] = sc[i + 1];
      sc[7] = rbf(rbf(x[0]) * 0.08838834764831845f);
      BLOCK_END(blk + 1)
    }
    float m = sc[0];
#pragma unroll
    for (int i = 1; i < 8; ++i) m = fmaxf(m, sc[i]);
    m = max32x(max16x(rowmax(m)));
    float l = 0.f;
    unsigned pp[8];
#pragma unroll
    for (int i = 0; i < 8; ++i) { const float p = ex2((sc[i] - m) * LOG2E); l += p; pp[i] = cvtpk(p, p); }
    l = sum32x(sum16x(rowsum(l)));
    if (lane == 0) { Lf[L_ML + 2 * sw] = m; Lf[L_ML + 2 * sw + 1] = l; }
    lds_signal(L + L_FL + F_KD + sw, lane);
    if (sw == 0) STAMP(18);
    const unsigned hm = (c & 1) ? 0xffff0000u : 0x0000ffffu;
    const int mt = (c & 7) >> 1;
    const v4u am = {mt == 0 ? hm : 0u, mt == 1 ? hm : 0u, mt == 2 ? hm : 0u, mt == 3 ? hm : 0u};
    const v4u pm = {c < 8 ? am.x : 0u, c < 8 ? am.y : 0u, c < 8 ? am.z : 0u, c < 8 ? am.w : 0u};
    f32x4 acc = {0.f, 0.f, 0.f, 0.f};
#define PVM(I, P, V) { acc = mfma(afrag(dppu<BCAST + (I)>(P), pm), (V), acc); asm volatile("" : "+v"(acc)); SB(); }
#define PV2(I) { PVM(I, pp[0], a[I]) PVM((I) + 1, pp[0], a[(I) + 1]) PACE((NPRE * 16 + 128) * TAUK + (blk * 16 + (I) + 8) * TAUV); \
      a[I] = ld_nt(rV, vo, sN + (I) * 4096); a[(I) + 1] = ld_nt(rV, vo, sN + ((I) + 1) * 4096); SB(); }
#define PV16(P, A, O) PVM(0, P, A[O + 0]) PVM(1, P, A[O + 1]) PVM(2, P, A[O + 2]) PVM(3, P, A[O + 3]) PVM(4, P, A[O + 4]) PVM(5, P, A[O + 5]) \
      PVM(6, P, A[O + 6]) PVM(7, P, A[O + 7]) PVM(8, P, A[O + 8]) PVM(9, P, A[O + 9]) PVM(10, P, A[O + 10]) PVM(11, P, A[O + 11]) \
      PVM(12, P, A[O + 12]) PVM(13, P, A[O + 13]) PVM(14, P, A[O + 14]) PVM(15, P, A[O + 15])
    const unsigned pq0 = pp[NVB], pq1 = pp[NVB + 1];
#pragma unroll 1
    for (int blk = 0; blk < NVB - 1; ++blk) {
      const unsigned sN = blk * 65536 + 65536;
      PV2(0) PV2(2) PV2(4) PV2(6) PV2(8) PV2(10) PV2(12) PV2(14)
#pragma unroll
      for (int i = 0; i < NVB - 1; ++i) pp[i] = pp[i + 1];
      BLOCK_END(blk + 9)
      if (blk == 0) { PV16(pq0, pre, 0) }
      if (blk == 1) { PV16(pq1, pre, 16) }
    }
    const int hp = 4 * (h & 7) + (h >> 3);
    v4u wz[9], wy[8];
    const rsrc_t rD = mk(wd + (size_t)(144 * hp + 36 * sw) * 1152 + j * 144, 36 * 1152 - j * 144);
    const rsrc_t rO = mk(wo + (size_t)(128 * h + 32 * sw) * 1152 + j * 144, 32 * 1152 - j * 144);
    const unsigned to = (c < 9 ? (unsigned)(c * 16) : OOB) + g * 1152;
#define TILES(P) if (TPOS == (P)) { _Pragma("unroll") for (int i = 0; i < 9; ++i) wz[i] = ld_nt(rD, to, i * 4608); \
      _Pragma("unroll") for (int i = 0; i < 8; ++i) wy[i] = ld_nt(rO, to, i * 4608); SB(); }
#define TL1(n) { if ((n) < 9) wz[(n) < 9 ? (n) : 0] = ld_nt(rD, to, (n) * 4608); else if ((n) < 17) wy[(n) >= 9 && (n) < 17 ? (n) - 9 : 0] = ld_nt(rO, to, ((n) - 9) * 4608); }
#define TG(I) if (TPOS == 99 && (I) >= TG0) { _Pragma("unroll") for (int i = 0; i < TGN; ++i) TL1(((I) - TG0) * TGN + i) SB(); }
    if (TBAR && TPOS == 0) BARRIER();
    TILES(0)
    const rsrc_t rW = mk(ws, WS_USED);
    lds_wait(L + L_FL + F_FW);
    const unsigned T = __builtin_amdgcn_readfirstlane(L[L_FL + F_T]);
    float fw = Lf[L_FW + sw];
    unsigned hx[9];
#pragma unroll
    for (int i = 0; i < 9; ++i) hx[i] = L[L_HD + (c >> 3) * HD_S + 36 * sw + g + 4 * i];
    const unsigned src = (c >> 3) ? 0x0302u : 0x0100u, sel = (c & 1) ? (src << 16) | 0x0c0cu : 0x0c0c0000u | src;
    v4u sl = {mt == 0 ? sel : 0x0c0c0c0cu, mt == 1 ? sel : 0x0c0c0c0cu, mt == 2 ? sel : 0x0c0c0c0cu, mt == 3 ? sel : 0x0c0c0c0cu};
    unsigned a_ow = (L_OW + sw * 128 + c * 8 + g * 4) * 4, a_or = (L_OW + sw * 32 + (c & 7) * 4 + (c >> 3) * 2) * 4, a_og = g == 0 ? c * 16 : OOB;
    const int yk = (sw & 1) * 16 + (lane >> 2), ykb = yk >= 9 ? 1 : 0;
    unsigned a_yr = (L_YW + ((ykb * 2 + ((lane & 3) >> 1)) * 16 + yk - ykb * 9) * 4 + (lane & 1) * 2) * 4, a_yg = (sw == 0 || lane < 8) ? lane * 16 : OOB;
    const int oo = (h >= 9 ? 1152 - 144 : 0) + 144 * j + 16 * h + (lane & 3) * 4;
    unsigned xres = 0;
    if (sw == 2 && h < 18) xres = *(const GAS unsigned*)(xg + oo);
    asm volatile("" : "+v"(fw), "+v"(sl), "+v"(a_ow), "+v"(a_or), "+v"(a_og), "+v"(a_yr), "+v"(a_yg));
    asm volatile("" : "+v"(hx[0]), "+v"(hx[1]), "+v"(hx[2]), "+v"(hx[3]), "+v"(hx[4]), "+v"(hx[5]), "+v"(hx[6]), "+v"(hx[7]), "+v"(hx[8]));
    SB();
    PVM(0, pp[0], a[0]) PVM(1, pp[0], a[1]) PVM(2, pp[0], a[2]) PVM(3, pp[0], a[3]) PVM(4, pp[0], a[4]) PVM(5, pp[0], a[5]) PVM(6, pp[0], a[6]) TG(6) PVM(7, pp[0], a[7]) TG(7)
    PVM(8, pp[0], a[8]) TG(8) PVM(9, pp[0], a[9]) TG(9) PVM(10, pp[0], a[10]) TG(10) PVM(11, pp[0], a[11]) TG(11) TILES(12) PVM(12, pp[0], a[12]) TG(12) TILES(13)
    PVM(13, pp[0], a[13]) TG(13) TILES(14) PVM(14, pp[0], a[14]) TG(14) TILES(15) PVM(15, pp[0], a[15])
    if (lane < 32) *(LAS f32x4*)((LAS char*)L + a_ow) = acc * fw;
    if (sw == 0) STAMP(19);
    TILES(16)
    LDS_BARRIER();
    {
      f32x2 t = *(LAS f32x2*)((LAS char*)L + a_or);
#pragma unroll
      for (int i = 1; i < 4; ++i) t += *(LAS f32x2*)((LAS char*)L + a_or + i * 512);
      st_sc(rW, a_og, OG_OFF + h * 8192 + sw * 2048 + j * 256, gran(t.x, t.y, T));
      SB();
    }
    if (sw == 0) STAMP(6);
    TILES(17)
    if (TPOS == 99) { _Pragma("unroll") for (int i = (15 - TG0) * TGN; i < 17; ++i) TL1(i) SB(); }
    f32x4 y = gemv<9>(f32x4{0.f, 0.f, 0.f, 0.f}, hx, am, wz);
    asm volatile("" : "+v"(y));
    if (sw == 0) STAMP(25);
    v4u p0, p1;
    poll2<1024>(rW, lane * 16, OG_OFF + h * 8192 + sw * 2048, T, p0, p1);
    if (sw == 0) STAMP(7);
    {
      const float e = add32(add16(u2f(p0.x), u2f(p0.z)), add16(u2f(p1.x), u2f(p1.z)));
      auto eb = __builtin_amdgcn_permlane32_swap(f2u(e), f2u(e), false, false);
      unsigned pk = cvtpk(u2f(eb[0]), u2f(eb[1]));
      pk = __builtin_amdgcn_update_dpp(pk, pk, XOR8, 0xc, 0xf, false);
      f32x4 y1 = {0.f, 0.f, 0.f, 0.f};
#define YM(I, ACC) { const unsigned bc = dppu<BCAST + (I)>(pk); ACC = mfma(v4u{__builtin_amdgcn_perm(0u, bc, sl.x), __builtin_amdgcn_perm(0u, bc, sl.y), \
        __builtin_amdgcn_perm(0u, bc, sl.z), __builtin_amdgcn_perm(0u, bc, sl.w)}, wy[I], ACC); }
      YM(0, y) YM(1, y1) YM(2, y) YM(3, y1) YM(4, y) YM(5, y1) YM(6, y) YM(7, y1)
      y += y1;
    }
    *(LAS f32x4*)(Lf + L_YW + sw * 256 + lane * 4) = y;
    LDS_BARRIER();
    if (sw < 2) {
      f32x2 t = *(LAS f32x2*)((LAS char*)L + a_yr);
#pragma unroll
      for (int i = 1; i < 4; ++i) t += *(LAS f32x2*)((LAS char*)L + a_yr + i * 1024);
      st_sc(rW, a_yg, YG_OFF + (j * 32 + h) * 1152 + sw * 1024, gran(t.x, t.y, T));
      if (sw == 0) STAMP(20);
    } else if (sw == 2 && h < 18) {
      poll2<16 * 1152>(rW, (lane >> 2) * 1152 + (lane & 3) * 16, YG_OFF + j * 32 * 1152 + h * 64, T, p0, p1);
      float y0 = u2f(p0.x) + u2f(p1.x), y1 = u2f(p0.z) + u2f(p1.z);
      y0 += dpp<ROR4>(y0); y1 += dpp<ROR4>(y1);
      y0 += dpp<XOR8>(y0); y1 += dpp<XOR8>(y1);
      y0 = sum32x(add16(y0, y1));
      auto yy = __builtin_amdgcn_permlane16_swap(f2u(y0), f2u(y0), false, false);
      const unsigned res = cvtpk(u2f(xres << 16) + u2f(yy[0]), u2f(xres & 0xffff0000u) + u2f(yy[1]));
      __builtin_amdgcn_raw_buffer_store_b32(res, mk(out, 2304), lane < 4 ? (unsigned)oo : OOB, 0, OUTSC);
      if (bid == 0 && lane == 0) __builtin_amdgcn_raw_buffer_store_b32(T, rW, E_OFF, 0, AUX_SC1);
      STAMP15();
    }
    return;
  }

  const bool isA = wave == 4;
  STAMP(isA ? 0 : 8);
  const int g = lane >> 4, c = lane & 15;
  const int hp = 4 * (h & 7) + (h >> 3);
  const rsrc_t rW = mk(ws, WS_USED);
  v4u w[36];
  unsigned E = 0, ta = 0;
  const int u = (r & 15) + 16 * (xc & 3), qq = (r >> 4) + 2 * (xc >> 2);
  const int gm = j & 1, half = (j >> 1) & 1, rh = j >> 2;
  const unsigned stride = isA ? 8192 : 9216, cofs = isA ? u * 128 : hp * 288 + half * 144;
  int row0 = isA ? 144 * qq : 288 * rh;
  const rsrc_t rG = mk((isA ? wq : gm ? wu : wg) + cofs, 576 * stride - cofs);
  const unsigned gvo = (c < (isA ? 8 : 9) ? (unsigned)(c * 16) : OOB) + g * stride;
  if (isA) {
    __builtin_amdgcn_s_setprio(3);
    ta = (unsigned)__builtin_amdgcn_s_memrealtime();
    E = __builtin_amdgcn_raw_buffer_load_b32(rW, E_OFF, 0, AUX_SC1);
  } else {
    __builtin_amdgcn_s_setprio(2);
    v3u xr[3];
    const rsrc_t rX = mk(xg, 2304);
#pragma unroll
    for (int k = 0; k < 3; ++k) xr[k] = __builtin_amdgcn_raw_buffer_load_b96(rX, (lane >> 5) * 1152 + (lane & 31) * 12, k * 384, 0);
    float xv[18];
#pragma unroll
    for (int k = 0; k < 3; ++k)
#pragma unroll
      for (int e = 0; e < 3; ++e) { xv[k * 6 + 2 * e] = u2f(xr[k][e] << 16); xv[k * 6 + 2 * e + 1] = u2f(xr[k][e] & 0xffff0000u); }
    float sum = 0.f;
#pragma unroll
    for (int i = 0; i < 18; ++i) sum += xv[i];
    const float mean = sum16x(rowsum(sum)) * (1.0f / 576.0f);
    float sq = 0.f;
#pragma unroll
    for (int i = 0; i < 18; ++i) { const float d = xv[i] - mean; sq = fmaf(d, d, sq); }
    const float var = sum16x(rowsum(sq)) * (1.0f / 576.0f);
    const float mb = rbf(mean), sd = rbf(__builtin_sqrtf(rbf(rbf(var) + 1e-5f))), rs = 1.0f / sd;
    LAS v2u* dst = (LAS v2u*)(L + L_XN + (lane >> 5) * XN_S + (lane & 31) * 6);
#pragma unroll
    for (int k = 0; k < 3; ++k)
#pragma unroll
      for (int e = 0; e < 3; ++e) {
        float n[2];
#pragma unroll
        for (int f = 0; f < 2; ++f) { const float d = rbf(xv[k * 6 + 2 * e + f] - mb); const float q0 = d * rs; n[f] = fmaf(fmaf(-q0, sd, d), rs, q0); }
        dst[k * 96 + e] = v2u{cvtpk(n[0], n[0]), cvtpk(n[1], n[1])};
      }
    lds_signal(L + L_FL + F_XN, lane);
    STAMP(9);
    __builtin_amdgcn_s_setprio(0);
  }
  const unsigned hm = (c & 1) ? 0xffff0000u : 0x0000ffffu;
  const int mt = (c & 7) >> 1;
  const v4u am = {mt == 0 ? hm : 0u, mt == 1 ? hm : 0u, mt == 2 ? hm : 0u, mt == 3 ? hm : 0u};
  unsigned T = 0;
  f32x4 ga = {0.f, 0.f, 0.f, 0.f};
#pragma unroll 1
  for (int t = 0; t < (isA ? 1 : 2); ++t, row0 += 144) {
    unsigned so = row0 * stride;
#pragma unroll
    for (int i = 0; i < 36; ++i) { w[i] = ld_nt(rG, gvo, so); so += 4 * stride; }
    SB();
    if (isA) {
      T = __builtin_amdgcn_readfirstlane(E) + 0x9E3779B9u;
      if (lane == 0) L[L_FL + F_T] = T;
      if (CTB && bid < 8) st_sc(rW, lane == 0 ? RT_OFF + bid * 16 : OOB, 0, v4u{ta, T, ta, T});
      lds_wait(L + L_FL + F_XN);
      STAMP(1);
    }
    asm volatile("" :: "v"(cv));
    unsigned xv[36];
#pragma unroll
    for (int i = 0; i < 36; ++i) xv[i] = L[L_XN + (c >> 3) * XN_S + row0 + g + 4 * i];
    SB();
    ga = gemv<36>(ga, xv, am, w);
  }
  if (isA) {
    const float s2 = dpp<XOR8>(ga[2]), s3 = dpp<XOR8>(ga[3]);
    const unsigned o = QG_OFF + (((u >> 1) * 2 + (g >> 1)) * 4 + qq) * 1024 + ((u & 1) * 64 + (c & 7) * 8 + (g & 1) * 4 + (c >> 3) * 2) * 8;
    st_sc(rW, o, 0, gran(c < 8 ? ga[0] : s2, c < 8 ? ga[1] : s3, T));
    STAMP(2);
    __builtin_amdgcn_s_sleep(QDELAY);
    float q0, q1;
#pragma unroll 1
    for (int spin = 0; spin < SPIN_CAP; ++spin) {
      MEMBAR();
#pragma unroll
      for (int i = 0; i < 4; ++i) w[i] = ld_sc(rW, lane * 16, QG_OFF + ((h * 2 + b) * 4 + i) * 1024);
      if (CTB) w[4] = ld_sc(rW, (lane & 7) * 16, RT_OFF);
      q0 = (u2f(w[0].x) + u2f(w[1].x)) + (u2f(w[2].x) + u2f(w[3].x));
      q1 = (u2f(w[0].z) + u2f(w[1].z)) + (u2f(w[2].z) + u2f(w[3].z));
      if (none_bad(tagbad(w[0], T) | tagbad(w[1], T) | tagbad(w[2], T) | tagbad(w[3], T))) break;
      STAMP(21);
      __builtin_amdgcn_s_sleep(16);
    }
    L[L_QB + lane] = cvtpk(q0, q1);
    {
      int d = 0;
      if (CTB) {
        d = (int)(w[4].x - ta);
        d = min(d, (int)dppu<XOR1>(d)); d = min(d, (int)dppu<XOR2>(d)); d = min(d, (int)dppu<XOR7>(d));
        d = none_bad(w[4].y ^ T) ? min(max(d, -500), 500) : 0;
      }
      if (lane == 0) L[L_FL + F_TK] = ta + d - TOFF;
    }
    lds_signal(L + L_FL + F_Q, lane);
    STAMP(3);
    __builtin_amdgcn_s_setprio(0);
#pragma unroll 1
    for (int k = 1; k <= RCN; ++k) {
      const unsigned pk = RCA + (k - 1) * RCS;
      while (__builtin_amdgcn_readfirstlane(lds_ld(L + L_FL + F_PB)) < pk) __builtin_amdgcn_s_sleep(8);
      MEMBAR();
      const unsigned ps = lds_ld(L + L_FL + F_PS);
      if (bid < 8) st_sc(rW, lane == 0 ? RT_OFF + k * 256 + bid * 16 : OOB, 0, v4u{ps, T, ps, T});
#pragma unroll 1
      for (int t = 0; t < 3; ++t) {
        __builtin_amdgcn_s_sleep(RCW);
        MEMBAR();
        const v4u v = ld_sc(rW, (lane & 7) * 16, RT_OFF + k * 256);
        if (!none_bad(v.y ^ T)) continue;
        int d = (int)(v.x - ps);
        d += (int)dppu<XOR1>(d); d += (int)dppu<XOR2>(d); d += (int)dppu<XOR7>(d);
        if (lane == 0) __hip_atomic_store(L + L_FL + F_TK, ps + min(max(d >> 3, -500), 500) - RCT, __ATOMIC_RELAXED, __HIP_MEMORY_SCOPE_WORKGROUP);
        break;
      }
    }
  } else {
    st_sc(rW, c < 9 ? (unsigned)((g >> 1) * 288 + c * 32 + (g & 1) * 16) : OOB, GU_OFF + bid * 640, __builtin_bit_cast(v4u, ga));
    STAMP(12);
    lds_wait4(L + L_FL + F_KD, lane);
    T = __builtin_amdgcn_readfirstlane(L[L_FL + F_T]);
    float mw[4], lw[4];
#pragma unroll
    for (int i = 0; i < 4; ++i) { mw[i] = Lf[L_ML + 2 * i]; lw[i] = Lf[L_ML + 2 * i + 1]; }
    const float mg = fmaxf(fmaxf(mw[0], mw[1]), fmaxf(mw[2], mw[3]));
    float lg = 0.f;
#pragma unroll
    for (int i = 0; i < 4; ++i) lg += lw[i] * ex2((mw[i] - mg) * LOG2E);
    VMWAIT(0);
    st_sc(rW, lane == 0 ? ML_OFF + h * 128 + j * 16 : OOB, 0, gran(mg, lg, T));
    v4u v;
#pragma unroll 1
    for (int spin = 0; spin < SPIN_CAP; ++spin) {
      __builtin_amdgcn_s_sleep(MLSLEEP);
      MEMBAR();
      v = ld_sc(rW, ((lane & 7) ^ (b * 4)) * 16, ML_OFF + h * 128);
      if (none_bad(tagbad(v, T))) break;
    }
    MEMBAR();
#pragma unroll
    for (int i = 0; i < 8; ++i) w[i] = ld_sc(rW, lane < 36 ? (unsigned)(lane * 16) : OOB, GU_OFF + (i * 32 + h) * 640);
#pragma unroll
    for (int hf = 0; hf < 2; ++hf) {
      v4u hv;
#pragma unroll
      for (int e = 0; e < 4; ++e) {
        const float gt = rbf(u2f(w[2 * hf][e]) + u2f(w[4 + 2 * hf][e])), up = rbf(u2f(w[1 + 2 * hf][e]) + u2f(w[5 + 2 * hf][e]));
        const float t2 = 1.5957691216057308f * (gt + 0.044715f * gt * gt * gt);
        const float hh = rbf(gt / (1.0f + ex2(-t2 * LOG2E))) * up;
        hv[e] = cvtpk(hh, hh);
      }
      if (lane < 36) *(LAS v4u*)(L + L_HD + (lane >= 18 ? HD_S - 72 : 0) + hf * 72 + lane * 4) = hv;
    }
    STAMP(13);
    const float ms = u2f(v.x);
    float M = fmaxf(ms, dpp<XOR1>(ms)); M = fmaxf(M, dpp<XOR2>(M));
    float Ls = u2f(v.z) * ex2((ms - M) * LOG2E);
    Ls += dpp<XOR1>(Ls); Ls += dpp<XOR2>(Ls);
    const float inv = 1.0f / Ls;
    if (lane == 0) *(LAS f32x4*)(Lf + L_FW) = f32x4{ex2((mw[0] - M) * LOG2E) * inv, ex2((mw[1] - M) * LOG2E) * inv, ex2((mw[2] - M) * LOG2E) * inv, ex2((mw[3] - M) * LOG2E) * inv};
    lds_signal(L + L_FL + F_FW, lane);
    {
      const unsigned t = __builtin_amdgcn_raw_buffer_load_b32(rC, lane < 6 ? (unsigned)(r * 6 + lane) * 128u : OOB, 0, 0);
      asm volatile("" :: "v"(t));
    }
    {
      const unsigned o = lane < 8 ? OG_OFF + h * 8192 + (lane >> 1) * 2048 + j * 256 + (lane & 1) * 128 : lane < 17 ? YG_OFF + bid * 1152 + (lane - 8) * 128 : OOB;
      const unsigned t = __builtin_amdgcn_raw_buffer_load_b32(rW, o, 0, WSPF == 1 ? AUX_SC1 : 0);
      asm volatile("" :: "v"(t));
    }
    {
      const char* tp = (lane & 4) ? wo + (size_t)(128 * h + 42 * (lane & 3)) * 1152 + j * 144 : wd + (size_t)(144 * hp + 47 * (lane & 3)) * 1152 + j * 144;
      if (lane & 8) tp = out;
      unsigned t = 0;
      if (lane < 9) t = __builtin_nontemporal_load((const GAS unsigned*)tp);
      asm volatile("" :: "v"(t));
    }
    STAMP(5);
  }
}

extern "C" void kernel_launch(void* const* d_in, const int* in_sizes, int n_in, void* d_out, int out_size, void* d_ws, size_t ws_size,
                              hipStream_t stream) {
  fused_decode<<<256, 384, 0, stream>>>((const char*)d_in[0], (const char*)d_in[1], (const char*)d_in[2], (const char*)d_in[3],
                                        (const char*)d_in[4], (const char*)d_in[6], (const char*)d_in[7], (const char*)d_in[8],
                                        (char*)d_out, (char*)d_ws);
}
```

```cpp
#include <hip/hip_runtime.h>
#include <hip/hip_bf16.h>

#define GAS __attribute__((address_space(1)))
#define LAS __attribute__((address_space(3)))
typedef unsigned v4u __attribute__((ext_vector_type(4)));
typedef unsigned v3u __attribute__((ext_vector_type(3)));
typedef unsigned v2u __attribute__((ext_vector_type(2)));
typedef float f32x4 __attribute__((ext_vector_type(4)));
typedef float f32x2 __attribute__((ext_vector_type(2)));
typedef __bf16 bf16x8 __attribute__((ext_vector_type(8)));
typedef __bf16 bf16x2 __attribute__((ext_vector_type(2)));
typedef unsigned long long v8q __attribute__((ext_vector_type(8)));
typedef unsigned long long v2q __attribute__((ext_vector_type(2)));
typedef __amdgpu_buffer_rsrc_t rsrc_t;

constexpr int NPRE = 2;
constexpr int NVB = 8 - NPRE;
constexpr int PINFL = 6;
constexpr int TAU = 224;
constexpr int LSTEP = 50;
constexpr int LAG0 = 150, LAGC = 140;

constexpr int E_OFF = 0;
constexpr int RT_OFF = 1024;
constexpr int QG_OFF = 4096;
constexpr int ML_OFF = QG_OFF + 262144;
constexpr int GU_OFF = ML_OFF + 4096;
constexpr int OG_OFF = GU_OFF + 163840;
constexpr int YG_OFF = OG_OFF + 262144;
constexpr int WS_USED = YG_OFF + 294912;

constexpr int L_XN = 0, XN_S = 580;
constexpr int L_HD = 1160, HD_S = 148;
constexpr int L_QB = L_HD + 2 * HD_S;
constexpr int L_OW = L_QB + 64;
constexpr int L_YW = L_OW + 512;
constexpr int L_ML = L_YW + 1056;
constexpr int L_FW = L_ML + 8;
constexpr int L_FL = L_FW + 4;
constexpr int F_KD = 0, F_Q = 4, F_T = 5, F_XN = 7, F_FW = 8, F_TK = 9, F_PB = 10, F_PS = 11;
constexpr int L_END = L_FL + 12;
static_assert(L_FL % 4 == 0 && L_OW % 4 == 0 && L_YW % 4 == 0 && L_QB % 4 == 0 && L_FW % 4 == 0 && L_HD % 4 == 0, "16-byte aligned LDS pieces");

constexpr int AUX_NT = 2, AUX_SC1 = 16;
constexpr unsigned OOB = 0x80000000u;
constexpr unsigned CODE_BYTES = 18432;
constexpr int SPIN_CAP = 1 << 18;
constexpr float LOG2E = 1.4426950408889634f;

__device__ __forceinline__ rsrc_t mk(const void* p, unsigned bytes) { return __builtin_amdgcn_make_buffer_rsrc((void*)p, 0, bytes, 0x00020000); }
__device__ __forceinline__ v4u ld_nt(rsrc_t r, unsigned vo, unsigned so) { return __builtin_amdgcn_raw_buffer_load_b128(r, vo, so, AUX_NT); }
__device__ __forceinline__ v4u ld_sc(rsrc_t r, unsigned vo, unsigned so) { return __builtin_amdgcn_raw_buffer_load_b128(r, vo, so, AUX_SC1); }
__device__ __forceinline__ void st_sc(rsrc_t r, unsigned vo, unsigned so, v4u v) { __builtin_amdgcn_raw_buffer_store_b128(v, r, vo, so, AUX_SC1); }
__device__ __forceinline__ float u2f(unsigned u) { return __builtin_bit_cast(float, u); }
__device__ __forceinline__ unsigned f2u(float f) { return __builtin_bit_cast(unsigned, f); }
__device__ __forceinline__ unsigned cvtpk(float lo, float hi) {
  bf16x2 r = {(__bf16)lo, (__bf16)hi};
  return __builtin_bit_cast(unsigned, r);
}
__device__ __forceinline__ float rbf(float f) { return (float)(__bf16)f; }
__device__ __forceinline__ float ex2(float f) { return __builtin_amdgcn_exp2f(f); }
__device__ __forceinline__ float dot2(unsigned a, unsigned b, float c) {
  return __builtin_amdgcn_fdot2_f32_bf16(__builtin_bit_cast(bf16x2, a), __builtin_bit_cast(bf16x2, b), c, false);
}
__device__ __forceinline__ f32x4 mfma(v4u a, v4u b, f32x4 c) {
  return __builtin_amdgcn_mfma_f32_16x16x32_bf16(__builtin_bit_cast(bf16x8, a), __builtin_bit_cast(bf16x8, b), c, 0, 0, 0);
}
template <int CTRL> __device__ __forceinline__ float dpp(float x) { return u2f(__builtin_amdgcn_mov_dpp(f2u(x), CTRL, 0xf, 0xf, true)); }
template <int CTRL> __device__ __forceinline__ unsigned dppu(unsigned x) { return __builtin_amdgcn_mov_dpp(x, CTRL, 0xf, 0xf, true); }
constexpr int XOR1 = 0xB1, XOR2 = 0x4E, XOR7 = 0x141, XOR8 = 0x128, ROR4 = 0x124, BCAST = 0x150;
__device__ __forceinline__ float add16(float a, float b) {
  auto s = __builtin_amdgcn_permlane16_swap(f2u(a), f2u(b), false, false); return u2f(s[0]) + u2f(s[1]); }
__device__ __forceinline__ float add32(float a, float b) {
  auto s = __builtin_amdgcn_permlane32_swap(f2u(a), f2u(b), false, false); return u2f(s[0]) + u2f(s[1]); }
__device__ __forceinline__ float sum16x(float x) { return add16(x, x); }
__device__ __forceinline__ float sum32x(float x) { return add32(x, x); }
__device__ __forceinline__ float max16x(float x) { auto s = __builtin_amdgcn_permlane16_swap(f2u(x), f2u(x), false, false); return fmaxf(u2f(s[0]), u2f(s[1])); }
__device__ __forceinline__ float max32x(float x) { auto s = __builtin_amdgcn_permlane32_swap(f2u(x), f2u(x), false, false); return fmaxf(u2f(s[0]), u2f(s[1])); }
__device__ __forceinline__ float rowsum(float x) { x += dpp<XOR1>(x); x += dpp<XOR2>(x); x += dpp<XOR7>(x); x += dpp<XOR8>(x); return x; }
__device__ __forceinline__ float rowmax(float x) { x = fmaxf(x, dpp<XOR1>(x)); x = fmaxf(x, dpp<XOR2>(x)); x = fmaxf(x, dpp<XOR7>(x)); x = fmaxf(x, dpp<XOR8>(x)); return x; }
#define SB() __builtin_amdgcn_sched_barrier(0)
#define MEMBAR() asm volatile("" ::: "memory")
#define LDS_FENCE() asm volatile("s_waitcnt lgkmcnt(0)" ::: "memory")
#define LDS_BARRIER() do { SB(); asm volatile("s_waitcnt lgkmcnt(0)\n\ts_barrier" ::: "memory"); SB(); } while (0)
#define VMWAIT(n) asm volatile("s_waitcnt vmcnt(%0)" :: "i"(n) : "memory")
__device__ __forceinline__ unsigned now() { return (unsigned)__builtin_amdgcn_s_memrealtime(); }
__device__ __forceinline__ unsigned lds_ld(LAS unsigned* p) { return __hip_atomic_load(p, __ATOMIC_RELAXED, __HIP_MEMORY_SCOPE_WORKGROUP); }
__device__ __forceinline__ void lds_st(LAS unsigned* p, unsigned v) { __hip_atomic_store(p, v, __ATOMIC_RELAXED, __HIP_MEMORY_SCOPE_WORKGROUP); }
__device__ __forceinline__ void lds_wait(LAS unsigned* p) {
  while (__builtin_amdgcn_readfirstlane(lds_ld(p)) == 0) __builtin_amdgcn_s_sleep(1);
  MEMBAR();
}
__device__ __forceinline__ void lds_wait4(LAS unsigned* p, int lane) {
  while (__builtin_amdgcn_ballot_w64(lds_ld(p + (lane & 3)) == 0) != 0ull) __builtin_amdgcn_s_sleep(4);
  MEMBAR();
}
__device__ __forceinline__ void lds_signal(LAS unsigned* p, int lane) {
  LDS_FENCE();
  if (lane == 0) lds_st(p, 1u);
}
__device__ __forceinline__ bool none_bad(unsigned bad) { return __builtin_amdgcn_ballot_w64(bad != 0u) == 0ull; }
__device__ __forceinline__ unsigned tagbad(const v4u& v, unsigned T) { return (v.y ^ T) | (v.w ^ T); }
__device__ __forceinline__ v4u gran(float a, float b, unsigned T) { return v4u{f2u(a), T, f2u(b), T}; }
__device__ __forceinline__ v4u afrag(unsigned dup, const v4u& m) { return v4u{dup & m.x, dup & m.y, dup & m.z, dup & m.w}; }
template <int D> __device__ __forceinline__ void poll2(rsrc_t rW, unsigned vo, unsigned so, unsigned T, v4u& r0, v4u& r1) {
#pragma unroll 1
  for (int spin = 0; spin < SPIN_CAP; ++spin) {
    MEMBAR();
    r0 = ld_sc(rW, vo, so); r1 = ld_sc(rW, vo, so + D);
    if (none_bad(tagbad(r0, T) | tagbad(r1, T))) break;
  }
}

#define PACE(n) do { const unsigned need_ = tk0 + (((unsigned)(n) * TAU) >> 4); \
    while ((lag = (int)(now() - need_)) < 0) __builtin_amdgcn_s_sleep(1); \
    const int cls_ = (int)(((unsigned)lag * (65536u / LSTEP)) >> 16); \
    asm volatile("s_cmp_gt_i32 %0, 5\n\ts_cbranch_scc1 1f\n\ts_waitcnt vmcnt(11)\n\ts_cmp_eq_i32 %0, 5\n\ts_cbranch_scc1 1f\n\ts_waitcnt vmcnt(8)\n\t" \
                 "s_cmp_eq_i32 %0, 4\n\ts_cbranch_scc1 1f\n\ts_waitcnt vmcnt(6)\n\ts_cmp_eq_i32 %0, 3\n\ts_cbranch_scc1 1f\n\ts_waitcnt vmcnt(4)\n\t" \
                 "s_cmp_eq_i32 %0, 2\n\ts_cbranch_scc1 1f\n\ts_waitcnt vmcnt(3)\n\ts_cmp_eq_i32 %0, 1\n\ts_cbranch_scc1 1f\n\ts_waitcnt vmcnt(2)\n1:" \
                 :: "s"(cls_) : "scc", "memory"); } while (0)
#define BLOCK_END(n) do { if (sw == 0 && lane == 0) { L[L_FL + F_PS] = tk0 + lag; MEMBAR(); lds_st(L + L_FL + F_PB, (unsigned)(n)); } \
    tk0 = __builtin_amdgcn_readfirstlane(lds_ld(L + L_FL + F_TK)); } while (0)

template <int N, int M> __device__ __forceinline__ f32x4 gemv(f32x4 a0, const unsigned (&xv)[N], const v4u& am, const v4u (&w)[M]) {
  f32x4 a1 = {0.f, 0.f, 0.f, 0.f};
#pragma unroll
  for (int i = 0; i < N; ++i) { if (i & 1) a1 = mfma(afrag(xv[i], am), w[i], a1); else a0 = mfma(afrag(xv[i], am), w[i], a0); }
  return a0 + a1;
}

__global__ void __launch_bounds__(384, 2)
fused_decode(const char* xg_, const char* kc_, const char* vc_, const char* wq_, const char* wo_, const char* wg_, const char* wu_, const char* wd_,
             char* out_, char* ws_) {
  v8q a8; v2q a2;
  asm volatile("s_load_dwordx16 %0, %2, 0x0\n\ts_load_dwordx4 %1, %2, 0x40" : "=&s"(a8), "=&s"(a2) : "s"(__builtin_amdgcn_kernarg_segment_ptr()));
  unsigned long long pc0;
  asm volatile("s_getpc_b64 %0" : "=s"(pc0));
  SB();
  __shared__ __attribute__((aligned(16))) unsigned smem[L_END];
  LAS unsigned* L = (LAS unsigned*)smem;
  LAS float* Lf = (LAS float*)smem;
  const int bid = blockIdx.x, xc = bid & 7, r = bid >> 3;
  int lane = threadIdx.x & 63;
  const int wave = __builtin_amdgcn_readfirstlane(threadIdx.x >> 6);
  const unsigned cv = __builtin_amdgcn_raw_buffer_load_b32(mk((const void*)(pc0 & ~255ull), CODE_BYTES), 0, min((unsigned)(r * 6 + wave) * 128u, CODE_BYTES - 128u), 0);
  const int h = bid & 31, j = bid >> 5, s = j & 3, b = j >> 2;
  unsigned cb = ((b * 32 + h) * 4 + s) * 524288u;
  unsigned vo = wave * 1024 + lane * 16;
  if (lane < 12) L[L_FL + lane] = 0u;
  asm volatile("" : "+v"(vo));
  SB();
  asm volatile("s_waitcnt lgkmcnt(0)\n\ts_barrier" : "+s"(a8), "+s"(a2), "+s"(cb) :: "memory");
  SB();
  const char* xg = (const char*)a8[0]; const char* kc = (const char*)a8[1]; const char* vc = (const char*)a8[2]; const char* wq = (const char*)a8[3];
  const char* wo = (const char*)a8[4]; const char* wg = (const char*)a8[5]; const char* wu = (const char*)a8[6]; const char* wd = (const char*)a8[7];
  char* out = (char*)a2[0]; char* ws = (char*)a2[1];

  if (__builtin_expect(wave < 4, 1)) {
    const int sw = wave;
    const char* kb = kc + cb;
    const char* vb = vc + cb;
    const rsrc_t rK = mk(kb, 524288), rV = mk(vb, 524288);
    v4u a[16], pre[NPRE * 16];
#pragma unroll
    for (int i = 0; i < PINFL / 2; ++i) { a[i] = ld_nt(rK, vo, i * 4096); pre[i] = ld_nt(rV, vo, NVB * 65536 + i * 4096); }
    SB();
    asm volatile("" : "+v"(lane));
#pragma unroll
    for (int i = PINFL / 2; i < NPRE * 16; ++i) { VMWAIT(PINFL - 1); pre[i] = ld_nt(rV, vo, NVB * 65536 + i * 4096); SB(); }
#pragma unroll
    for (int i = PINFL / 2; i < 16; ++i) { VMWAIT(PINFL - 1); a[i] = ld_nt(rK, vo, i * 4096); SB(); }
    const int g = lane >> 4, c = lane & 15;
    lds_wait(L + L_FL + F_Q);
    asm volatile("" :: "v"(cv));
    const v4u q = *(LAS v4u*)(L + L_QB + c * 4);
    unsigned tk0 = __builtin_amdgcn_readfirstlane(L[L_FL + F_TK]);
    int lag = 0;
    float sc[8];
#pragma unroll
    for (int i = 0; i < 8; ++i) sc[i] = 0.f;
#pragma unroll 1
    for (int blk = 0; blk < 8; ++blk) {
      const rsrc_t rN = mk(blk < 7 ? kb : vb, 524288);
      const unsigned sN = blk < 7 ? blk * 65536 + 65536 : 0;
      float x[16];
#pragma unroll
      for (int i = 0; i < 16; ++i) {
        const v4u k = a[i];
        float d = dot2(k.x, q.x, 0.f); d = dot2(k.y, q.y, d); d = dot2(k.z, q.z, d); d = dot2(k.w, q.w, d);
        x[i] = d;
        asm volatile("" : "+v"(x[i]));
        SB();
        if (i & 1) {
          PACE(NPRE * 16 + 7 + blk * 16 + i);
          a[i - 1] = ld_nt(rN, vo, sN + (i - 1) * 4096);
          a[i] = ld_nt(rN, vo, sN + i * 4096);
          SB();
        }
      }
#define TR_STEP(HALF, CTRL) _Pragma("unroll") for (int u = 0; u < (HALF); ++u) { const float lo_ = x[u], hi_ = x[u + (HALF)]; \
        const float t1_ = lo_ + dpp<CTRL>(lo_), t2_ = hi_ + dpp<CTRL>(hi_); x[u] = (c & (HALF)) ? t2_ : t1_; }
      TR_STEP(8, XOR8) TR_STEP(4, XOR7) TR_STEP(2, XOR2) TR_STEP(1, XOR1)
#undef TR_STEP
#pragma unroll
      for (int i = 0; i < 7; ++i) sc[i] = sc[i + 1];
      sc[7] = rbf(rbf(x[0]) * 0.08838834764831845f);
      BLOCK_END(blk + 1);
    }
    float m = sc[0];
#pragma unroll
    for (int i = 1; i < 8; ++i) m = fmaxf(m, sc[i]);
    m = max32x(max16x(rowmax(m)));
    float l = 0.f;
    unsigned pp[8];
#pragma unroll
    for (int i = 0; i < 8; ++i) { const float p = ex2((sc[i] - m) * LOG2E); l += p; pp[i] = cvtpk(p, p); }
    l = sum32x(sum16x(rowsum(l)));
    if (lane == 0) { Lf[L_ML + 2 * sw] = m; Lf[L_ML + 2 * sw + 1] = l; }
    lds_signal(L + L_FL + F_KD + sw, lane);
    const unsigned hm = (c & 1) ? 0xffff0000u : 0x0000ffffu;
    const int mt = (c & 7) >> 1;
    const v4u am = {mt == 0 ? hm : 0u, mt == 1 ? hm : 0u, mt == 2 ? hm : 0u, mt == 3 ? hm : 0u};
    const v4u pm = {c < 8 ? am.x : 0u, c < 8 ? am.y : 0u, c < 8 ? am.z : 0u, c < 8 ? am.w : 0u};
    f32x4 acc = {0.f, 0.f, 0.f, 0.f};
#define PVM(I, P, V) { acc = mfma(afrag(dppu<BCAST + (I)>(P), pm), (V), acc); asm volatile("" : "+v"(acc)); SB(); }
#define PV2(I) { PVM(I, pp[0], a[I]) PVM((I) + 1, pp[0], a[(I) + 1]) PACE(NPRE * 16 + 136 + blk * 16 + (I)); \
      a[I] = ld_nt(rV, vo, sN + (I) * 4096); a[(I) + 1] = ld_nt(rV, vo, sN + ((I) + 1) * 4096); SB(); }
#define PV14(P, A, O) PVM(0, P, A[O + 0]) PVM(1, P, A[O + 1]) PVM(2, P, A[O + 2]) PVM(3, P, A[O + 3]) PVM(4, P, A[O + 4]) PVM(5, P, A[O + 5]) \
      PVM(6, P, A[O + 6]) PVM(7, P, A[O + 7]) PVM(8, P, A[O + 8]) PVM(9, P, A[O + 9]) PVM(10, P, A[O + 10]) PVM(11, P, A[O + 11]) \
      PVM(12, P, A[O + 12]) PVM(13, P, A[O + 13])
    const unsigned pq0 = pp[NVB], pq1 = pp[NVB + 1];
#pragma unroll 1
    for (int blk = 0; blk < NVB - 1; ++blk) {
      const unsigned sN = blk * 65536 + 65536;
      PV2(0) PV2(2) PV2(4) PV2(6) PV2(8) PV2(10) PV2(12) PV2(14)
#pragma unroll
      for (int i = 0; i < NVB - 1; ++i) pp[i] = pp[i + 1];
      BLOCK_END(blk + 9);
      if (blk == 0) { PV14(pq0, pre, 0) PVM(14, pq0, pre[14]) PVM(15, pq0, pre[15]) }
      if (blk == 1) { PV14(pq1, pre, 16) PVM(14, pq1, pre[30]) PVM(15, pq1, pre[31]) }
    }
    const int hp = 4 * (h & 7) + (h >> 3);
    v4u wz[9], wy[8];
    const rsrc_t rD = mk(wd + (size_t)(144 * hp + 36 * sw) * 1152 + j * 144, 36 * 1152 - j * 144);
    const rsrc_t rO = mk(wo + (size_t)(128 * h + 32 * sw) * 1152 + j * 144, 32 * 1152 - j * 144);
    const unsigned to = (c < 9 ? (unsigned)(c * 16) : OOB) + g * 1152;
    const rsrc_t rW = mk(ws, WS_USED);
    lds_wait(L + L_FL + F_FW);
    const unsigned T = __builtin_amdgcn_readfirstlane(L[L_FL + F_T]);
    float fw = Lf[L_FW + sw];
    unsigned hx[9];
#pragma unroll
    for (int i = 0; i < 9; ++i) hx[i] = L[L_HD + (c >> 3) * HD_S + 36 * sw + g + 4 * i];
    const unsigned src = (c >> 3) ? 0x0302u : 0x0100u, sel = (c & 1) ? (src << 16) | 0x0c0cu : 0x0c0c0000u | src;
    v4u sl = {mt == 0 ? sel : 0x0c0c0c0cu, mt == 1 ? sel : 0x0c0c0c0cu, mt == 2 ? sel : 0x0c0c0c0cu, mt == 3 ? sel : 0x0c0c0c0cu};
    unsigned a_ow = (L_OW + sw * 128 + c * 8 + g * 4) * 4, a_or = (L_OW + sw * 32 + (c & 7) * 4 + (c >> 3) * 2) * 4, a_og = g == 0 ? c * 16 : OOB;
    const int yk = (sw & 1) * 16 + (lane >> 2), ykb = yk >= 9 ? 1 : 0;
    unsigned a_yr = (L_YW + ((ykb * 2 + ((lane & 3) >> 1)) * 16 + yk - ykb * 9) * 4 + (lane & 1) * 2) * 4, a_yg = (sw == 0 || lane < 8) ? lane * 16 : OOB;
    const int oo = (h >= 9 ? 1152 - 144 : 0) + 144 * j + 16 * h + (lane & 3) * 4;
    unsigned xres = 0;
    if (sw == 2 && h < 18) xres = *(const GAS unsigned*)(xg + oo);
    asm volatile("" : "+v"(fw), "+v"(sl), "+v"(a_ow), "+v"(a_or), "+v"(a_og), "+v"(a_yr), "+v"(a_yg));
    asm volatile("" : "+v"(hx[0]), "+v"(hx[1]), "+v"(hx[2]), "+v"(hx[3]), "+v"(hx[4]), "+v"(hx[5]), "+v"(hx[6]), "+v"(hx[7]), "+v"(hx[8]));
    SB();
    PV14(pp[0], a, 0)
#pragma unroll
    for (int i = 0; i < 3; ++i) wz[i] = ld_nt(rD, to, i * 4608);
    SB();
    PVM(14, pp[0], a[14])
#pragma unroll
    for (int i = 3; i < 6; ++i) wz[i] = ld_nt(rD, to, i * 4608);
    SB();
    PVM(15, pp[0], a[15])
    if (lane < 32) *(LAS f32x4*)((LAS char*)L + a_ow) = acc * fw;
    LDS_BARRIER();
    {
      f32x2 t = *(LAS f32x2*)((LAS char*)L + a_or);
#pragma unroll
      for (int i = 1; i < 4; ++i) t += *(LAS f32x2*)((LAS char*)L + a_or + i * 512);
      st_sc(rW, a_og, OG_OFF + h * 8192 + sw * 2048 + j * 256, gran(t.x, t.y, T));
      SB();
    }
#pragma unroll
    for (int i = 6; i < 9; ++i) wz[i] = ld_nt(rD, to, i * 4608);
#pragma unroll
    for (int i = 0; i < 8; ++i) wy[i] = ld_nt(rO, to, i * 4608);
    SB();
    f32x4 y = gemv<9>(f32x4{0.f, 0.f, 0.f, 0.f}, hx, am, wz);
    asm volatile("" : "+v"(y));
    v4u p0, p1;
    poll2<1024>(rW, lane * 16, OG_OFF + h * 8192 + sw * 2048, T, p0, p1);
    {
      const float e = add32(add16(u2f(p0.x), u2f(p0.z)), add16(u2f(p1.x), u2f(p1.z)));
      auto eb = __builtin_amdgcn_permlane32_swap(f2u(e), f2u(e), false, false);
      unsigned pk = cvtpk(u2f(eb[0]), u2f(eb[1]));
      pk = __builtin_amdgcn_update_dpp(pk, pk, XOR8, 0xc, 0xf, false);
      f32x4 y1 = {0.f, 0.f, 0.f, 0.f};
#define YM(I, ACC) { const unsigned bc = dppu<BCAST + (I)>(pk); ACC = mfma(v4u{__builtin_amdgcn_perm(0u, bc, sl.x), __builtin_amdgcn_perm(0u, bc, sl.y), \
        __builtin_amdgcn_perm(0u, bc, sl.z), __builtin_amdgcn_perm(0u, bc, sl.w)}, wy[I], ACC); }
      YM(0, y) YM(1, y1) YM(2, y) YM(3, y1) YM(4, y) YM(5, y1) YM(6, y) YM(7, y1)
      y += y1;
    }
    *(LAS f32x4*)(Lf + L_YW + sw * 256 + lane * 4) = y;
    LDS_BARRIER();
    if (sw < 2) {
      f32x2 t = *(LAS f32x2*)((LAS char*)L + a_yr);
#pragma unroll
      for (int i = 1; i < 4; ++i) t += *(LAS f32x2*)((LAS char*)L + a_yr + i * 1024);
      st_sc(rW, a_yg, YG_OFF + bid * 1152 + sw * 1024, gran(t.x, t.y, T));
    } else if (sw == 2 && h < 18) {
      poll2<16 * 1152>(rW, (lane >> 2) * 1152 + (lane & 3) * 16, YG_OFF + j * 32 * 1152 + h * 64, T, p0, p1);
      float y0 = u2f(p0.x) + u2f(p1.x), y1 = u2f(p0.z) + u2f(p1.z);
      y0 += dpp<ROR4>(y0); y1 += dpp<ROR4>(y1);
      y0 += dpp<XOR8>(y0); y1 += dpp<XOR8>(y1);
      y0 = sum32x(add16(y0, y1));
      auto yy = __builtin_amdgcn_permlane16_swap(f2u(y0), f2u(y0), false, false);
      const unsigned res = cvtpk(u2f(xres << 16) + u2f(yy[0]), u2f(xres & 0xffff0000u) + u2f(yy[1]));
      __builtin_amdgcn_raw_buffer_store_b32(res, mk(out, 2304), lane < 4 ? (unsigned)oo : OOB, 0, 0);
      if (bid == 0 && lane == 0) __builtin_amdgcn_raw_buffer_store_b32(T, rW, E_OFF, 0, AUX_SC1);
    }
    return;
  }

  const bool isA = wave == 4;
  const int g = lane >> 4, c = lane & 15;
  const int hp = 4 * (h & 7) + (h >> 3);
  const rsrc_t rW = mk(ws, WS_USED);
  v4u w[36];
  unsigned E = 0, ta = 0;
  const int u = (r & 15) + 16 * (xc & 3), qq = (r >> 4) + 2 * (xc >> 2);
  const int gm = j & 1, half = (j >> 1) & 1, rh = j >> 2;
  const unsigned stride = isA ? 8192 : 9216, cofs = isA ? u * 128 : hp * 288 + half * 144;
  int row0 = isA ? 144 * qq : 288 * rh;
  const rsrc_t rG = mk((isA ? wq : gm ? wu : wg) + cofs, 576 * stride - cofs);
  const unsigned gvo = (c < (isA ? 8 : 9) ? (unsigned)(c * 16) : OOB) + g * stride;
  if (isA) {
    __builtin_amdgcn_s_setprio(3);
    ta = now();
    E = __builtin_amdgcn_raw_buffer_load_b32(rW, E_OFF, 0, AUX_SC1);
  } else {
    __builtin_amdgcn_s_setprio(2);
    v3u xr[3];
    const rsrc_t rX = mk(xg, 2304);
#pragma unroll
    for (int k = 0; k < 3; ++k) xr[k] = __builtin_amdgcn_raw_buffer_load_b96(rX, (lane >> 5) * 1152 + (lane & 31) * 12, k * 384, 0);
    float xv[18];
#pragma unroll
    for (int k = 0; k < 3; ++k)
#pragma unroll
      for (int e = 0; e < 3; ++e) { xv[k * 6 + 2 * e] = u2f(xr[k][e] << 16); xv[k * 6 + 2 * e + 1] = u2f(xr[k][e] & 0xffff0000u); }
    float sum = 0.f;
#pragma unroll
    for (int i = 0; i < 18; ++i) sum += xv[i];
    const float mean = sum16x(rowsum(sum)) * (1.0f / 576.0f);
    float sq = 0.f;
#pragma unroll
    for (int i = 0; i < 18; ++i) { const float d = xv[i] - mean; sq = fmaf(d, d, sq); }
    const float var = sum16x(rowsum(sq)) * (1.0f / 576.0f);
    const float mb = rbf(mean), sd = rbf(__builtin_sqrtf(rbf(rbf(var) + 1e-5f))), rs = 1.0f / sd;
    LAS v2u* dst = (LAS v2u*)(L + L_XN + (lane >> 5) * XN_S + (lane & 31) * 6);
#pragma unroll
    for (int k = 0; k < 3; ++k)
#pragma unroll
      for (int e = 0; e < 3; ++e) {
        float n[2];
#pragma unroll
        for (int f = 0; f < 2; ++f) { const float d = rbf(xv[k * 6 + 2 * e + f] - mb); const float q0 = d * rs; n[f] = fmaf(fmaf(-q0, sd, d), rs, q0); }
        dst[k * 96 + e] = v2u{cvtpk(n[0], n[0]), cvtpk(n[1], n[1])};
      }
    lds_signal(L + L_FL + F_XN, lane);
    __builtin_amdgcn_s_setprio(0);
  }
  const unsigned hm = (c & 1) ? 0xffff0000u : 0x0000ffffu;
  const int mt = (c & 7) >> 1;
  const v4u am = {mt == 0 ? hm : 0u, mt == 1 ? hm : 0u, mt == 2 ? hm : 0u, mt == 3 ? hm : 0u};
  unsigned T = 0;
  f32x4 ga = {0.f, 0.f, 0.f, 0.f};
#pragma unroll 1
  for (int t = 0; t < (isA ? 1 : 2); ++t, row0 += 144) {
    unsigned so = row0 * stride;
#pragma unroll
    for (int i = 0; i < 36; ++i) { w[i] = ld_nt(rG, gvo, so); so += 4 * stride; }
    SB();
    if (isA) {
      T = __builtin_amdgcn_readfirstlane(E) + 0x9E3779B9u;
      if (lane == 0) L[L_FL + F_T] = T;
      lds_wait(L + L_FL + F_XN);
    }
    asm volatile("" :: "v"(cv));
    unsigned xv[36];
#pragma unroll
    for (int i = 0; i < 36; ++i) xv[i] = L[L_XN + (c >> 3) * XN_S + row0 + g + 4 * i];
    SB();
    ga = gemv<36>(ga, xv, am, w);
  }
  if (isA) {
    const float s2 = dpp<XOR8>(ga[2]), s3 = dpp<XOR8>(ga[3]);
    const unsigned o = QG_OFF + (((u >> 1) * 2 + (g >> 1)) * 4 + qq) * 1024 + ((u & 1) * 64 + (c & 7) * 8 + (g & 1) * 4 + (c >> 3) * 2) * 8;
    st_sc(rW, o, 0, gran(c < 8 ? ga[0] : s2, c < 8 ? ga[1] : s3, T));
    __builtin_amdgcn_s_sleep(32);
    float q0, q1;
#pragma unroll 1
    for (int spin = 0; spin < SPIN_CAP; ++spin) {
      MEMBAR();
#pragma unroll
      for (int i = 0; i < 4; ++i) w[i] = ld_sc(rW, lane * 16, QG_OFF + ((h * 2 + b) * 4 + i) * 1024);
      q0 = (u2f(w[0].x) + u2f(w[1].x)) + (u2f(w[2].x) + u2f(w[3].x));
      q1 = (u2f(w[0].z) + u2f(w[1].z)) + (u2f(w[2].z) + u2f(w[3].z));
      if (none_bad(tagbad(w[0], T) | tagbad(w[1], T) | tagbad(w[2], T) | tagbad(w[3], T))) break;
      __builtin_amdgcn_s_sleep(16);
    }
    L[L_QB + lane] = cvtpk(q0, q1);
    if (lane == 0) L[L_FL + F_TK] = ta + LAG0;
    lds_signal(L + L_FL + F_Q, lane);
    __builtin_amdgcn_s_setprio(0);
#pragma unroll 1
    for (int k = 0; k < 2; ++k) {
      while (__builtin_amdgcn_readfirstlane(lds_ld(L + L_FL + F_PB)) < 1u + 6u * k) __builtin_amdgcn_s_sleep(8);
      MEMBAR();
      const unsigned ps = lds_ld(L + L_FL + F_PS);
      if (bid < 8) st_sc(rW, lane == 0 ? RT_OFF + k * 256 + bid * 16 : OOB, 0, v4u{ps, T, ps, T});
#pragma unroll 1
      for (int t = 0; t < 3; ++t) {
        __builtin_amdgcn_s_sleep(64);
        MEMBAR();
        const v4u v = ld_sc(rW, (lane & 7) * 16, RT_OFF + k * 256);
        if (!none_bad(tagbad(v, T))) continue;
        int d = (int)(v.x - ps);
        d += (int)dppu<XOR1>(d); d += (int)dppu<XOR2>(d); d += (int)dppu<XOR7>(d);
        if (lane == 0) lds_st(L + L_FL + F_TK, ps + min(max(d >> 3, -500), 500) - LAGC);
        break;
      }
    }
  } else {
    st_sc(rW, c < 9 ? (unsigned)((g >> 1) * 288 + c * 32 + (g & 1) * 16) : OOB, GU_OFF + bid * 640, __builtin_bit_cast(v4u, ga));
    lds_wait4(L + L_FL + F_KD, lane);
    T = __builtin_amdgcn_readfirstlane(L[L_FL + F_T]);
    float mw[4], lw[4];
#pragma unroll
    for (int i = 0; i < 4; ++i) { mw[i] = Lf[L_ML + 2 * i]; lw[i] = Lf[L_ML + 2 * i + 1]; }
    const float mg = fmaxf(fmaxf(mw[0], mw[1]), fmaxf(mw[2], mw[3]));
    float lg = 0.f;
#pragma unroll
    for (int i = 0; i < 4; ++i) lg += lw[i] * ex2((mw[i] - mg) * LOG2E);
    VMWAIT(0);
    st_sc(rW, lane == 0 ? ML_OFF + h * 128 + j * 16 : OOB, 0, gran(mg, lg, T));
    v4u v;
#pragma unroll 1
    for (int spin = 0; spin < SPIN_CAP; ++spin) {
      __builtin_amdgcn_s_sleep(80);
      MEMBAR();
      v = ld_sc(rW, ((lane & 7) ^ (b * 4)) * 16, ML_OFF + h * 128);
      if (none_bad(tagbad(v, T))) break;
    }
    MEMBAR();
#pragma unroll
    for (int i = 0; i < 8; ++i) w[i] = ld_sc(rW, lane < 36 ? (unsigned)(lane * 16) : OOB, GU_OFF + (i * 32 + h) * 640);
#pragma unroll
    for (int hf = 0; hf < 2; ++hf) {
      v4u hv;
#pragma unroll
      for (int e = 0; e < 4; ++e) {
        const float gt = rbf(u2f(w[2 * hf][e]) + u2f(w[4 + 2 * hf][e])), up = rbf(u2f(w[1 + 2 * hf][e]) + u2f(w[5 + 2 * hf][e]));
        const float t2 = 1.5957691216057308f * (gt + 0.044715f * gt * gt * gt);
        const float hh = rbf(gt / (1.0f + ex2(-t2 * LOG2E))) * up;
        hv[e] = cvtpk(hh, hh);
      }
      if (lane < 36) *(LAS v4u*)(L + L_HD + (lane >= 18 ? HD_S - 72 : 0) + hf * 72 + lane * 4) = hv;
    }
    const float ms = u2f(v.x);
    float M = fmaxf(ms, dpp<XOR1>(ms)); M = fmaxf(M, dpp<XOR2>(M));
    float Ls = u2f(v.z) * ex2((ms - M) * LOG2E);
    Ls += dpp<XOR1>(Ls); Ls += dpp<XOR2>(Ls);
    const float inv = 1.0f / Ls;
    if (lane == 0) *(LAS f32x4*)(Lf + L_FW) = f32x4{ex2((mw[0] - M) * LOG2E) * inv, ex2((mw[1] - M) * LOG2E) * inv, ex2((mw[2] - M) * LOG2E) * inv, ex2((mw[3] - M) * LOG2E) * inv};
    lds_signal(L + L_FL + F_FW, lane);
    const unsigned o = lane < 8 ? OG_OFF + h * 8192 + (lane >> 1) * 2048 + j * 256 + (lane & 1) * 128 : lane < 17 ? YG_OFF + bid * 1152 + (lane - 8) * 128 : OOB;
    const unsigned t = __builtin_amdgcn_raw_buffer_load_b32(rW, o, 0, AUX_SC1);
    asm volatile("" :: "v"(t));
  }
}

extern "C" void kernel_launch(void* const* d_in, const int* in_sizes, int n_in, void* d_out, int out_size, void* d_ws, size_t ws_size,
                              hipStream_t stream) {
  fused_decode<<<256, 384, 0, stream>>>((const char*)d_in[0], (const char*)d_in[1], (const char*)d_in[2], (const char*)d_in[3],
                                        (const char*)d_in[4], (const char*)d_in[6], (const char*)d_in[7], (const char*)d_in[8],
                                        (char*)d_out, (char*)d_ws);
}
```

```cpp
#include <hip/hip_runtime.h>
#include <hip/hip_bf16.h>

#define GAS __attribute__((address_space(1)))
#define LAS __attribute__((address_space(3)))
typedef unsigned v4u __attribute__((ext_vector_type(4)));
typedef unsigned v3u __attribute__((ext_vector_type(3)));
typedef unsigned v2u __attribute__((ext_vector_type(2)));
typedef float f32x4 __attribute__((ext_vector_type(4)));
typedef float f32x2 __attribute__((ext_vector_type(2)));
typedef __bf16 bf16x8 __attribute__((ext_vector_type(8)));
typedef __bf16 bf16x2 __attribute__((ext_vector_type(2)));
typedef unsigned long long v8q __attribute__((ext_vector_type(8)));
typedef unsigned long long v2q __attribute__((ext_vector_type(2)));
typedef __amdgpu_buffer_rsrc_t rsrc_t;

constexpr int LSTR = 16384;
constexpr int NPRE = 2;
constexpr int NVB = 8 - NPRE;
constexpr int PINFL = 6;
constexpr int TAU = 216;
constexpr int LAG0 = 150, LAGC = 140;

constexpr int E_OFF = 0;
constexpr int RT_OFF = 1024;
constexpr int QG_OFF = 4096;
constexpr int ML_OFF = QG_OFF + 262144;
constexpr int GU_OFF = ML_OFF + 4096;
constexpr int OG_OFF = GU_OFF + 163840;
constexpr int YG_OFF = OG_OFF + 262144;
constexpr int WS_USED = YG_OFF + 294912;

constexpr int L_XN = 0, XN_S = 580;
constexpr int L_HD = 1160, HD_S = 148;
constexpr int L_QB = L_HD + 2 * HD_S;
constexpr int L_OW = L_QB + 64;
constexpr int L_YW = L_OW + 512;
constexpr int L_ML = L_YW + 1056;
constexpr int L_FW = L_ML + 8;
constexpr int L_FL = L_FW + 4;
constexpr int F_KD = 0, F_Q = 4, F_T = 5, F_XN = 7, F_FW = 8, F_TK = 9, F_PB = 10, F_PS = 11;
constexpr int L_END = L_FL + 12;
static_assert(L_FL % 4 == 0 && L_OW % 4 == 0 && L_YW % 4 == 0 && L_QB % 4 == 0 && L_FW % 4 == 0 && L_HD % 4 == 0, "16-byte aligned LDS pieces");

constexpr int AUX_NT = 2, AUX_SC1 = 16;
constexpr unsigned OOB = 0x80000000u;
constexpr unsigned CODE_BYTES = 18432;
constexpr int SPIN_CAP = 1 << 18;
constexpr float LOG2E = 1.4426950408889634f;

__device__ __forceinline__ rsrc_t mk(const void* p, unsigned bytes) { return __builtin_amdgcn_make_buffer_rsrc((void*)p, 0, bytes, 0x00020000); }
__device__ __forceinline__ v4u ld_nt(rsrc_t r, unsigned vo, unsigned so) { return __builtin_amdgcn_raw_buffer_load_b128(r, vo, so, AUX_NT); }
__device__ __forceinline__ v4u ld_sc(rsrc_t r, unsigned vo, unsigned so) { return __builtin_amdgcn_raw_buffer_load_b128(r, vo, so, AUX_SC1); }
__device__ __forceinline__ void st_sc(rsrc_t r, unsigned vo, unsigned so, v4u v) { __builtin_amdgcn_raw_buffer_store_b128(v, r, vo, so, AUX_SC1); }
__device__ __forceinline__ float u2f(unsigned u) { return __builtin_bit_cast(float, u); }
__device__ __forceinline__ unsigned f2u(float f) { return __builtin_bit_cast(unsigned, f); }
__device__ __forceinline__ unsigned cvtpk(float lo, float hi) {
  bf16x2 r = {(__bf16)lo, (__bf16)hi};
  return __builtin_bit_cast(unsigned, r);
}
__device__ __forceinline__ float rbf(float f) { return (float)(__bf16)f; }
__device__ __forceinline__ float ex2(float f) { return __builtin_amdgcn_exp2f(f); }
__device__ __forceinline__ float dot2(unsigned a, unsigned b, float c) {
  return __builtin_amdgcn_fdot2_f32_bf16(__builtin_bit_cast(bf16x2, a), __builtin_bit_cast(bf16x2, b), c, false);
}
__device__ __forceinline__ f32x4 mfma(v4u a, v4u b, f32x4 c) {
  return __builtin_amdgcn_mfma_f32_16x16x32_bf16(__builtin_bit_cast(bf16x8, a), __builtin_bit_cast(bf16x8, b), c, 0, 0, 0);
}
template <int CTRL> __device__ __forceinline__ float dpp(float x) { return u2f(__builtin_amdgcn_mov_dpp(f2u(x), CTRL, 0xf, 0xf, true)); }
template <int CTRL> __device__ __forceinline__ unsigned dppu(unsigned x) { return __builtin_amdgcn_mov_dpp(x, CTRL, 0xf, 0xf, true); }
constexpr int XOR1 = 0xB1, XOR2 = 0x4E, XOR7 = 0x141, XOR8 = 0x128, ROR4 = 0x124, BCAST = 0x150;
__device__ __forceinline__ float add16(float a, float b) {
  auto s = __builtin_amdgcn_permlane16_swap(f2u(a), f2u(b), false, false); return u2f(s[0]) + u2f(s[1]); }
__device__ __forceinline__ float add32(float a, float b) {
  auto s = __builtin_amdgcn_permlane32_swap(f2u(a), f2u(b), false, false); return u2f(s[0]) + u2f(s[1]); }
__device__ __forceinline__ float sum16x(float x) { return add16(x, x); }
__device__ __forceinline__ float sum32x(float x) { return add32(x, x); }
__device__ __forceinline__ float max16x(float x) { auto s = __builtin_amdgcn_permlane16_swap(f2u(x), f2u(x), false, false); return fmaxf(u2f(s[0]), u2f(s[1])); }
__device__ __forceinline__ float max32x(float x) { auto s = __builtin_amdgcn_permlane32_swap(f2u(x), f2u(x), false, false); return fmaxf(u2f(s[0]), u2f(s[1])); }
__device__ __forceinline__ float rowsum(float x) { x += dpp<XOR1>(x); x += dpp<XOR2>(x); x += dpp<XOR7>(x); x += dpp<XOR8>(x); return x; }
__device__ __forceinline__ float rowmax(float x) { x = fmaxf(x, dpp<XOR1>(x)); x = fmaxf(x, dpp<XOR2>(x)); x = fmaxf(x, dpp<XOR7>(x)); x = fmaxf(x, dpp<XOR8>(x)); return x; }
#define SB() __builtin_amdgcn_sched_barrier(0)
#define MEMBAR() asm volatile("" ::: "memory")
#define LDS_FENCE() asm volatile("s_waitcnt lgkmcnt(0)" ::: "memory")
#define LDS_BARRIER() do { SB(); asm volatile("s_waitcnt lgkmcnt(0)\n\ts_barrier" ::: "memory"); SB(); } while (0)
#define VMWAIT(n) asm volatile("s_waitcnt vmcnt(%0)" :: "i"(n) : "memory")
__device__ __forceinline__ unsigned now() { return (unsigned)__builtin_amdgcn_s_memrealtime(); }
__device__ __forceinline__ unsigned lds_ld(LAS unsigned* p) { return __hip_atomic_load(p, __ATOMIC_RELAXED, __HIP_MEMORY_SCOPE_WORKGROUP); }
__device__ __forceinline__ void lds_st(LAS unsigned* p, unsigned v) { __hip_atomic_store(p, v, __ATOMIC_RELAXED, __HIP_MEMORY_SCOPE_WORKGROUP); }
__device__ __forceinline__ void lds_wait(LAS unsigned* p) {
  while (__builtin_amdgcn_readfirstlane(lds_ld(p)) == 0) __builtin_amdgcn_s_sleep(1);
  MEMBAR();
}
__device__ __forceinline__ void lds_wait4(LAS unsigned* p, int lane) {
  while (__builtin_amdgcn_ballot_w64(lds_ld(p + (lane & 3)) == 0) != 0ull) __builtin_amdgcn_s_sleep(4);
  MEMBAR();
}
__device__ __forceinline__ void lds_signal(LAS unsigned* p, int lane) {
  LDS_FENCE();
  if (lane == 0) lds_st(p, 1u);
}
__device__ __forceinline__ bool none_bad(unsigned bad) { return __builtin_amdgcn_ballot_w64(bad != 0u) == 0ull; }
__device__ __forceinline__ unsigned tagbad(const v4u& v, unsigned T) { return (v.y ^ T) | (v.w ^ T); }
__device__ __forceinline__ v4u gran(float a, float b, unsigned T) { return v4u{f2u(a), T, f2u(b), T}; }
__device__ __forceinline__ v4u afrag(unsigned dup, const v4u& m) { return v4u{dup & m.x, dup & m.y, dup & m.z, dup & m.w}; }
template <int D> __device__ __forceinline__ void poll2(rsrc_t rW, unsigned vo, unsigned so, unsigned T, v4u& r0, v4u& r1) {
#pragma unroll 1
  for (int spin = 0; spin < SPIN_CAP; ++spin) {
    MEMBAR();
    r0 = ld_sc(rW, vo, so); r1 = ld_sc(rW, vo, so + D);
    if (none_bad(tagbad(r0, T) | tagbad(r1, T))) break;
  }
}

#define PACE(n) do { const unsigned need_ = tk0 + (((unsigned)(n) * TAU) >> 4); \
    while ((lag = (int)(now() - need_)) < 0) __builtin_amdgcn_s_sleep(1); \
    const int cls_ = (int)(((unsigned)lag * (65536u / 25)) >> 16);     \
    asm volatile("s_cmp_ge_i32 %0, 18\n\ts_cbranch_scc1 1f\n\ts_waitcnt vmcnt(8)\n\ts_cmp_ge_i32 %0, 13\n\ts_cbranch_scc1 1f\n\ts_waitcnt vmcnt(6)\n\t" \
                 "s_cmp_ge_i32 %0, 9\n\ts_cbranch_scc1 1f\n\ts_waitcnt vmcnt(5)\n\ts_cmp_ge_i32 %0, 6\n\ts_cbranch_scc1 1f\n\ts_waitcnt vmcnt(4)\n\t" \
                 "s_cmp_ge_i32 %0, 4\n\ts_cbranch_scc1 1f\n\ts_waitcnt vmcnt(3)\n\ts_cmp_ge_i32 %0, 2\n\ts_cbranch_scc1 1f\n\ts_waitcnt vmcnt(2)\n1:" \
                 :: "s"(cls_) : "scc", "memory"); } while (0)
#define BLOCK_END(n) do { if (sw == 0 && lane == 0) { L[L_FL + F_PS] = tk0 + lag; MEMBAR(); lds_st(L + L_FL + F_PB, (unsigned)(n)); } \
    tk0 = __builtin_amdgcn_readfirstlane(lds_ld(L + L_FL + F_TK)); } while (0)

template <int N, int M> __device__ __forceinline__ f32x4 gemv(f32x4 a0, const unsigned (&xv)[N], const v4u& am, const v4u (&w)[M]) {
  f32x4 a1 = {0.f, 0.f, 0.f, 0.f};
#pragma unroll
  for (int i = 0; i < N; ++i) { if (i & 1) a1 = mfma(afrag(xv[i], am), w[i], a1); else a0 = mfma(afrag(xv[i], am), w[i], a0); }
  return a0 + a1;
}

__global__ void __launch_bounds__(384, 2)
fused_decode(const char* xg_, const char* kc_, const char* vc_, const char* wq_, const char* wo_, const char* wg_, const char* wu_, const char* wd_,
             char* out_, char* ws_) {
  v8q a8; v2q a2;
  asm volatile("s_load_dwordx16 %0, %2, 0x0\n\ts_load_dwordx4 %1, %2, 0x40" : "=&s"(a8), "=&s"(a2) : "s"(__builtin_amdgcn_kernarg_segment_ptr()));
  unsigned long long pc0;
  asm volatile("s_getpc_b64 %0" : "=s"(pc0));
  SB();
  __shared__ __attribute__((aligned(16))) unsigned smem[L_END];
  LAS unsigned* L = (LAS unsigned*)smem;
  LAS float* Lf = (LAS float*)smem;
  const int bid = blockIdx.x, xc = bid & 7, r = bid >> 3;
  int lane = threadIdx.x & 63;
  const int wave = __builtin_amdgcn_readfirstlane(threadIdx.x >> 6);
  const unsigned cv = __builtin_amdgcn_raw_buffer_load_b32(mk((const void*)(pc0 & ~255ull), CODE_BYTES), 0, min((unsigned)(r * 6 + wave) * 128u, CODE_BYTES - 128u), 0);
  const int h = bid & 31, j = bid >> 5, s = j & 3, b = j >> 2;
  unsigned cb = (b * 32 + h) * 2097152u + s * 1024u;
  unsigned vo = wave * 4096 + lane * 16;
  if (lane < 12) L[L_FL + lane] = 0u;
  asm volatile("" : "+v"(vo));
  SB();
  asm volatile("s_waitcnt lgkmcnt(0)\n\ts_barrier" : "+s"(a8), "+s"(a2), "+s"(cb) :: "memory");
  SB();
  const char* xg = (const char*)a8[0]; const char* kc = (const char*)a8[1]; const char* vc = (const char*)a8[2]; const char* wq = (const char*)a8[3];
  const char* wo = (const char*)a8[4]; const char* wg = (const char*)a8[5]; const char* wu = (const char*)a8[6]; const char* wd = (const char*)a8[7];
  char* out = (char*)a2[0]; char* ws = (char*)a2[1];

  if (__builtin_expect(wave < 4, 1)) {
    const int sw = wave;
    const char* kb = kc + cb;
    const char* vb = vc + cb;
    const unsigned span = 2097152u - s * 1024u;
    const rsrc_t rK = mk(kb, span), rV = mk(vb, span);
    v4u a[16], pre[NPRE * 16];
#pragma unroll
    for (int i = 0; i < PINFL / 2; ++i) { a[i] = ld_nt(rK, vo, i * LSTR); pre[i] = ld_nt(rV, vo, (NVB * 16 + i) * LSTR); }
    SB();
    asm volatile("" : "+v"(lane));
#pragma unroll
    for (int i = PINFL / 2; i < NPRE * 16; ++i) { VMWAIT(PINFL - 1); pre[i] = ld_nt(rV, vo, (NVB * 16 + i) * LSTR); SB(); }
#pragma unroll
    for (int i = PINFL / 2; i < 16; ++i) { VMWAIT(PINFL - 1); a[i] = ld_nt(rK, vo, i * LSTR); SB(); }
    const int g = lane >> 4, c = lane & 15;
    lds_wait(L + L_FL + F_Q);
    asm volatile("" :: "v"(cv));
    const v4u q = *(LAS v4u*)(L + L_QB + c * 4);
    unsigned tk0 = __builtin_amdgcn_readfirstlane(L[L_FL + F_TK]);
    int lag = 0;
    float sc[8];
#pragma unroll
    for (int i = 0; i < 8; ++i) sc[i] = 0.f;
#pragma unroll 1
    for (int blk = 0; blk < 8; ++blk) {
      const rsrc_t rN = mk(blk < 7 ? kb : vb, span);
      const unsigned sN = blk < 7 ? (blk + 1) * 16 * LSTR : 0;
      float x[16];
#pragma unroll
      for (int i = 0; i < 16; ++i) {
        const v4u k = a[i];
        float d = dot2(k.x, q.x, 0.f); d = dot2(k.y, q.y, d); d = dot2(k.z, q.z, d); d = dot2(k.w, q.w, d);
        x[i] = d;
        asm volatile("" : "+v"(x[i]));
        SB();
        if (i & 1) {
          PACE(NPRE * 16 + 7 + blk * 16 + i);
          a[i - 1] = ld_nt(rN, vo, sN + (i - 1) * LSTR);
          a[i] = ld_nt(rN, vo, sN + i * LSTR);
          SB();
        }
      }
#define TR_STEP(HALF, CTRL) _Pragma("unroll") for (int u = 0; u < (HALF); ++u) { const float lo_ = x[u], hi_ = x[u + (HALF)]; \
        const float t1_ = lo_ + dpp<CTRL>(lo_), t2_ = hi_ + dpp<CTRL>(hi_); x[u] = (c & (HALF)) ? t2_ : t1_; }
      TR_STEP(8, XOR8) TR_STEP(4, XOR7) TR_STEP(2, XOR2) TR_STEP(1, XOR1)
#undef TR_STEP
#pragma unroll
      for (int i = 0; i < 7; ++i) sc[i] = sc[i + 1];
      sc[7] = rbf(rbf(x[0]) * 0.08838834764831845f);
      BLOCK_END(blk + 1);
    }
    float m = sc[0];
#pragma unroll
    for (int i = 1; i < 8; ++i) m = fmaxf(m, sc[i]);
    m = max32x(max16x(rowmax(m)));
    float l = 0.f;
    unsigned pp[8];
#pragma unroll
    for (int i = 0; i < 8; ++i) { const float p = ex2((sc[i] - m) * LOG2E); l += p; pp[i] = cvtpk(p, p); }
    l = sum32x(sum16x(rowsum(l)));
    if (lane == 0) { Lf[L_ML + 2 * sw] = m; Lf[L_ML + 2 * sw + 1] = l; }
    lds_signal(L + L_FL + F_KD + sw, lane);
    const unsigned hm = (c & 1) ? 0xffff0000u : 0x0000ffffu;
    const int mt = (c & 7) >> 1;
    const v4u am = {mt == 0 ? hm : 0u, mt == 1 ? hm : 0u, mt == 2 ? hm : 0u, mt == 3 ? hm : 0u};
    const v4u pm = {c < 8 ? am.x : 0u, c < 8 ? am.y : 0u, c < 8 ? am.z : 0u, c < 8 ? am.w : 0u};
    f32x4 acc = {0.f, 0.f, 0.f, 0.f};
#define PVM(I, P, V) { acc = mfma(afrag(dppu<BCAST + (I)>(P), pm), (V), acc); asm volatile("" : "+v"(acc)); SB(); }
#define PV2(I) { PVM(I, pp[0], a[I]) PVM((I) + 1, pp[0], a[(I) + 1]) PACE(NPRE * 16 + 136 + blk * 16 + (I)); \
      a[I] = ld_nt(rV, vo, sN + (I) * LSTR); a[(I) + 1] = ld_nt(rV, vo, sN + ((I) + 1) * LSTR); SB(); }
#define PV14(P, A, O) PVM(0, P, A[O + 0]) PVM(1, P, A[O + 1]) PVM(2, P, A[O + 2]) PVM(3, P, A[O + 3]) PVM(4, P, A[O + 4]) PVM(5, P, A[O + 5]) \
      PVM(6, P, A[O + 6]) PVM(7, P, A[O + 7]) PVM(8, P, A[O + 8]) PVM(9, P, A[O + 9]) PVM(10, P, A[O + 10]) PVM(11, P, A[O + 11]) \
      PVM(12, P, A[O + 12]) PVM(13, P, A[O + 13])
    const unsigned pq0 = pp[NVB], pq1 = pp[NVB + 1];
#pragma unroll 1
    for (int blk = 0; blk < NVB - 1; ++blk) {
      const unsigned sN = (blk + 1) * 16 * LSTR;
      PV2(0) PV2(2) PV2(4) PV2(6) PV2(8) PV2(10) PV2(12) PV2(14)
#pragma unroll
      for (int i = 0; i < NVB - 1; ++i) pp[i] = pp[i + 1];
      BLOCK_END(blk + 9);
      if (blk == 0) { PV14(pq0, pre, 0) PVM(14, pq0, pre[14]) PVM(15, pq0, pre[15]) }
      if (blk == 1) { PV14(pq1, pre, 16) PVM(14, pq1, pre[30]) PVM(15, pq1, pre[31]) }
    }
    const int hp = 4 * (h & 7) + (h >> 3);
    v4u wz[9], wy[8];
    const rsrc_t rD = mk(wd + (size_t)(144 * hp + 36 * sw) * 1152 + j * 144, 36 * 1152 - j * 144);
    const rsrc_t rO = mk(wo + (size_t)(128 * h + 32 * sw) * 1152 + j * 144, 32 * 1152 - j * 144);
    const unsigned to = (c < 9 ? (unsigned)(c * 16) : OOB) + g * 1152;
    const rsrc_t rW = mk(ws, WS_USED);
    lds_wait(L + L_FL + F_FW);
    const unsigned T = __builtin_amdgcn_readfirstlane(L[L_FL + F_T]);
    float fw = Lf[L_FW + sw];
    unsigned hx[9];
#pragma unroll
    for (int i = 0; i < 9; ++i) hx[i] = L[L_HD + (c >> 3) * HD_S + 36 * sw + g + 4 * i];
    const unsigned src = (c >> 3) ? 0x0302u : 0x0100u, sel = (c & 1) ? (src << 16) | 0x0c0cu : 0x0c0c0000u | src;
    v4u sl = {mt == 0 ? sel : 0x0c0c0c0cu, mt == 1 ? sel : 0x0c0c0c0cu, mt == 2 ? sel : 0x0c0c0c0cu, mt == 3 ? sel : 0x0c0c0c0cu};
    unsigned a_ow = (L_OW + sw * 128 + c * 8 + g * 4) * 4, a_or = (L_OW + sw * 32 + (c & 7) * 4 + (c >> 3) * 2) * 4, a_og = g == 0 ? c * 16 : OOB;
    const int yk = (sw & 1) * 16 + (lane >> 2), ykb = yk >= 9 ? 1 : 0;
    unsigned a_yr = (L_YW + ((ykb * 2 + ((lane & 3) >> 1)) * 16 + yk - ykb * 9) * 4 + (lane & 1) * 2) * 4, a_yg = (sw == 0 || lane < 8) ? lane * 16 : OOB;
    const int oo = (h >= 9 ? 1152 - 144 : 0) + 144 * j + 16 * h + (lane & 3) * 4;
    unsigned xres = 0;
    if (sw == 2 && h < 18) xres = *(const GAS unsigned*)(xg + oo);
    asm volatile("" : "+v"(fw), "+v"(sl), "+v"(a_ow), "+v"(a_or), "+v"(a_og), "+v"(a_yr), "+v"(a_yg));
    asm volatile("" : "+v"(hx[0]), "+v"(hx[1]), "+v"(hx[2]), "+v"(hx[3]), "+v"(hx[4]), "+v"(hx[5]), "+v"(hx[6]), "+v"(hx[7]), "+v"(hx[8]));
    SB();
    PV14(pp[0], a, 0)
#pragma unroll
    for (int i = 0; i < 3; ++i) wz[i] = ld_nt(rD, to, i * 4608);
    SB();
    PVM(14, pp[0], a[14])
#pragma unroll
    for (int i = 3; i < 6; ++i) wz[i] = ld_nt(rD, to, i * 4608);
    SB();
    PVM(15, pp[0], a[15])
    if (lane < 32) *(LAS f32x4*)((LAS char*)L + a_ow) = acc * fw;
    LDS_BARRIER();
    {
      f32x2 t = *(LAS f32x2*)((LAS char*)L + a_or);
#pragma unroll
      for (int i = 1; i < 4; ++i) t += *(LAS f32x2*)((LAS char*)L + a_or + i * 512);
      st_sc(rW, a_og, OG_OFF + h * 8192 + sw * 2048 + j * 256, gran(t.x, t.y, T));
      SB();
    }
#pragma unroll
    for (int i = 6; i < 9; ++i) wz[i] = ld_nt(rD, to, i * 4608);
#pragma unroll
    for (int i = 0; i < 8; ++i) wy[i] = ld_nt(rO, to, i * 4608);
    SB();
    f32x4 y = gemv<9>(f32x4{0.f, 0.f, 0.f, 0.f}, hx, am, wz);
    asm volatile("" : "+v"(y));
    v4u p0, p1;
    poll2<1024>(rW, lane * 16, OG_OFF + h * 8192 + sw * 2048, T, p0, p1);
    {
      const float e = add32(add16(u2f(p0.x), u2f(p0.z)), add16(u2f(p1.x), u2f(p1.z)));
      auto eb = __builtin_amdgcn_permlane32_swap(f2u(e), f2u(e), false, false);
      unsigned pk = cvtpk(u2f(eb[0]), u2f(eb[1]));
      pk = __builtin_amdgcn_update_dpp(pk, pk, XOR8, 0xc, 0xf, false);
      f32x4 y1 = {0.f, 0.f, 0.f, 0.f};
#define YM(I, ACC) { const unsigned bc = dppu<BCAST + (I)>(pk); ACC = mfma(v4u{__builtin_amdgcn_perm(0u, bc, sl.x), __builtin_amdgcn_perm(0u, bc, sl.y), \
        __builtin_amdgcn_perm(0u, bc, sl.z), __builtin_amdgcn_perm(0u, bc, sl.w)}, wy[I], ACC); }
      YM(0, y) YM(1, y1) YM(2, y) YM(3, y1) YM(4, y) YM(5, y1) YM(6, y) YM(7, y1)
      y += y1;
    }
    *(LAS f32x4*)(Lf + L_YW + sw * 256 + lane * 4) = y;
    LDS_BARRIER();
    if (sw < 2) {
      f32x2 t = *(LAS f32x2*)((LAS char*)L + a_yr);
#pragma unroll
      for (int i = 1; i < 4; ++i) t += *(LAS f32x2*)((LAS char*)L + a_yr + i * 1024);
      st_sc(rW, a_yg, YG_OFF + bid * 1152 + sw * 1024, gran(t.x, t.y, T));
    } else if (sw == 2 && h < 18) {
      poll2<16 * 1152>(rW, (lane >> 2) * 1152 + (lane & 3) * 16, YG_OFF + j * 32 * 1152 + h * 64, T, p0, p1);
      float y0 = u2f(p0.x) + u2f(p1.x), y1 = u2f(p0.z) + u2f(p1.z);
      y0 += dpp<ROR4>(y0); y1 += dpp<ROR4>(y1);
      y0 += dpp<XOR8>(y0); y1 += dpp<XOR8>(y1);
      y0 = sum32x(add16(y0, y1));
      auto yy = __builtin_amdgcn_permlane16_swap(f2u(y0), f2u(y0), false, false);
      const unsigned res = cvtpk(u2f(xres << 16) + u2f(yy[0]), u2f(xres & 0xffff0000u) + u2f(yy[1]));
      __builtin_amdgcn_raw_buffer_store_b32(res, mk(out, 2304), lane < 4 ? (unsigned)oo : OOB, 0, 0);
      if (bid == 0 && lane == 0) __builtin_amdgcn_raw_buffer_store_b32(T, rW, E_OFF, 0, AUX_SC1);
    }
    return;
  }

  const bool isA = wave == 4;
  const int g = lane >> 4, c = lane & 15;
  const int hp = 4 * (h & 7) + (h >> 3);
  const rsrc_t rW = mk(ws, WS_USED);
  v4u w[36];
  unsigned E = 0, ta = 0;
  const int u = (r & 15) + 16 * (xc & 3), qq = (r >> 4) + 2 * (xc >> 2);
  const int gm = j & 1, half = (j >> 1) & 1, rh = j >> 2;
  const unsigned stride = isA ? 8192 : 9216, cofs = isA ? u * 128 : hp * 288 + half * 144;
  int row0 = isA ? 144 * qq : 288 * rh;
  const rsrc_t rG = mk((isA ? wq : gm ? wu : wg) + cofs, 576 * stride - cofs);
  const unsigned gvo = (c < (isA ? 8 : 9) ? (unsigned)(c * 16) : OOB) + g * stride;
  if (isA) {
    __builtin_amdgcn_s_setprio(3);
    ta = now();
    E = __builtin_amdgcn_raw_buffer_load_b32(rW, E_OFF, 0, AUX_SC1);
  } else {
    __builtin_amdgcn_s_setprio(2);
    v3u xr[3];
    const rsrc_t rX = mk(xg, 2304);
#pragma unroll
    for (int k = 0; k < 3; ++k) xr[k] = __builtin_amdgcn_raw_buffer_load_b96(rX, (lane >> 5) * 1152 + (lane & 31) * 12, k * 384, 0);
    float xv[18];
#pragma unroll
    for (int k = 0; k < 3; ++k)
#pragma unroll
      for (int e = 0; e < 3; ++e) { xv[k * 6 + 2 * e] = u2f(xr[k][e] << 16); xv[k * 6 + 2 * e + 1] = u2f(xr[k][e] & 0xffff0000u); }
    float sum = 0.f;
#pragma unroll
    for (int i = 0; i < 18; ++i) sum += xv[i];
    const float mean = sum16x(rowsum(sum)) * (1.0f / 576.0f);
    float sq = 0.f;
#pragma unroll
    for (int i = 0; i < 18; ++i) { const float d = xv[i] - mean; sq = fmaf(d, d, sq); }
    const float var = sum16x(rowsum(sq)) * (1.0f / 576.0f);
    const float mb = rbf(mean), sd = rbf(__builtin_sqrtf(rbf(rbf(var) + 1e-5f))), rs = 1.0f / sd;
    LAS v2u* dst = (LAS v2u*)(L + L_XN + (lane >> 5) * XN_S + (lane & 31) * 6);
#pragma unroll
    for (int k = 0; k < 3; ++k)
#pragma unroll
      for (int e = 0; e < 3; ++e) {
        float n[2];
#pragma unroll
        for (int f = 0; f < 2; ++f) { const float d = rbf(xv[k * 6 + 2 * e + f] - mb); const float q0 = d * rs; n[f] = fmaf(fmaf(-q0, sd, d), rs, q0); }
        dst[k * 96 + e] = v2u{cvtpk(n[0], n[0]), cvtpk(n[1], n[1])};
      }
    lds_signal(L + L_FL + F_XN, lane);
    __builtin_amdgcn_s_setprio(0);
  }
  const unsigned hm = (c & 1) ? 0xffff0000u : 0x0000ffffu;
  const int mt = (c & 7) >> 1;
  const v4u am = {mt == 0 ? hm : 0u, mt == 1 ? hm : 0u, mt == 2 ? hm : 0u, mt == 3 ? hm : 0u};
  unsigned T = 0;
  f32x4 ga = {0.f, 0.f, 0.f, 0.f};
#pragma unroll 1
  for (int t = 0; t < (isA ? 1 : 2); ++t, row0 += 144) {
    unsigned so = row0 * stride;
#pragma unroll
    for (int i = 0; i < 36; ++i) { w[i] = ld_nt(rG, gvo, so); so += 4 * stride; }
    SB();
    if (isA) {
      T = __builtin_amdgcn_readfirstlane(E) + 0x9E3779B9u;
      if (lane == 0) L[L_FL + F_T] = T;
      lds_wait(L + L_FL + F_XN);
    }
    asm volatile("" :: "v"(cv));
    unsigned xv[36];
#pragma unroll
    for (int i = 0; i < 36; ++i) xv[i] = L[L_XN + (c >> 3) * XN_S + row0 + g + 4 * i];
    SB();
    ga = gemv<36>(ga, xv, am, w);
  }
  if (isA) {
    const float s2 = dpp<XOR8>(ga[2]), s3 = dpp<XOR8>(ga[3]);
    const unsigned o = QG_OFF + (((u >> 1) * 2 + (g >> 1)) * 4 + qq) * 1024 + ((u & 1) * 64 + (c & 7) * 8 + (g & 1) * 4 + (c >> 3) * 2) * 8;
    st_sc(rW, o, 0, gran(c < 8 ? ga[0] : s2, c < 8 ? ga[1] : s3, T));
    __builtin_amdgcn_s_sleep(32);
    float q0, q1;
#pragma unroll 1
    for (int spin = 0; spin < SPIN_CAP; ++spin) {
      MEMBAR();
#pragma unroll
      for (int i = 0; i < 4; ++i) w[i] = ld_sc(rW, lane * 16, QG_OFF + ((h * 2 + b) * 4 + i) * 1024);
      q0 = (u2f(w[0].x) + u2f(w[1].x)) + (u2f(w[2].x) + u2f(w[3].x));
      q1 = (u2f(w[0].z) + u2f(w[1].z)) + (u2f(w[2].z) + u2f(w[3].z));
      if (none_bad(tagbad(w[0], T) | tagbad(w[1], T) | tagbad(w[2], T) | tagbad(w[3], T))) break;
      __builtin_amdgcn_s_sleep(16);
    }
    L[L_QB + lane] = cvtpk(q0, q1);
    if (lane == 0) L[L_FL + F_TK] = ta + LAG0;
    lds_signal(L + L_FL + F_Q, lane);
    __builtin_amdgcn_s_setprio(0);
#pragma unroll 1
    for (int k = 0; k < 2; ++k) {
      while (__builtin_amdgcn_readfirstlane(lds_ld(L + L_FL + F_PB)) < 1u + 6u * k) __builtin_amdgcn_s_sleep(8);
      MEMBAR();
      const unsigned ps = lds_ld(L + L_FL + F_PS);
      if (bid < 8) st_sc(rW, lane == 0 ? RT_OFF + k * 256 + bid * 16 : OOB, 0, v4u{ps, T, ps, T});
#pragma unroll 1
      for (int t = 0; t < 3; ++t) {
        __builtin_amdgcn_s_sleep(64);
        MEMBAR();
        const v4u v = ld_sc(rW, (lane & 7) * 16, RT_OFF + k * 256);
        if (!none_bad(tagbad(v, T))) continue;
        int d = (int)(v.x - ps);
        d += (int)dppu<XOR1>(d); d += (int)dppu<XOR2>(d); d += (int)dppu<XOR7>(d);
        if (lane == 0) lds_st(L + L_FL + F_TK, ps + min(max(d >> 3, -500), 500) - LAGC);
        break;
      }
    }
  } else {
    st_sc(rW, c < 9 ? (unsigned)((g >> 1) * 288 + c * 32 + (g & 1) * 16) : OOB, GU_OFF + bid * 640, __builtin_bit_cast(v4u, ga));
    lds_wait4(L + L_FL + F_KD, lane);
    T = __builtin_amdgcn_readfirstlane(L[L_FL + F_T]);
    float mw[4], lw[4];
#pragma unroll
    for (int i = 0; i < 4; ++i) { mw[i] = Lf[L_ML + 2 * i]; lw[i] = Lf[L_ML + 2 * i + 1]; }
    const float mg = fmaxf(fmaxf(mw[0], mw[1]), fmaxf(mw[2], mw[3]));
    float lg = 0.f;
#pragma unroll
    for (int i = 0; i < 4; ++i) lg += lw[i] * ex2((mw[i] - mg) * LOG2E);
    VMWAIT(0);
    st_sc(rW, lane == 0 ? ML_OFF + h * 128 + j * 16 : OOB, 0, gran(mg, lg, T));
    v4u v;
#pragma unroll 1
    for (int spin = 0; spin < SPIN_CAP; ++spin) {
      __builtin_amdgcn_s_sleep(80);
      MEMBAR();
      v = ld_sc(rW, ((lane & 7) ^ (b * 4)) * 16, ML_OFF + h * 128);
      if (none_bad(tagbad(v, T))) break;
    }
    MEMBAR();
#pragma unroll
    for (int i = 0; i < 8; ++i) w[i] = ld_sc(rW, lane < 36 ? (unsigned)(lane * 16) : OOB, GU_OFF + (i * 32 + h) * 640);
#pragma unroll
    for (int hf = 0; hf < 2; ++hf) {
      v4u hv;
#pragma unroll
      for (int e = 0; e < 4; ++e) {
        const float gt = rbf(u2f(w[2 * hf][e]) + u2f(w[4 + 2 * hf][e])), up = rbf(u2f(w[1 + 2 * hf][e]) + u2f(w[5 + 2 * hf][e]));
        const float t2 = 1.5957691216057308f * (gt + 0.044715f * gt * gt * gt);
        const float hh = rbf(gt / (1.0f + ex2(-t2 * LOG2E))) * up;
        hv[e] = cvtpk(hh, hh);
      }
      if (lane < 36) *(LAS v4u*)(L + L_HD + (lane >= 18 ? HD_S - 72 : 0) + hf * 72 + lane * 4) = hv;
    }
    const float ms = u2f(v.x);
    float M = fmaxf(ms, dpp<XOR1>(ms)); M = fmaxf(M, dpp<XOR2>(M));
    float Ls = u2f(v.z) * ex2((ms - M) * LOG2E);
    Ls += dpp<XOR1>(Ls); Ls += dpp<XOR2>(Ls);
    const float inv = 1.0f / Ls;
    if (lane == 0) *(LAS f32x4*)(Lf + L_FW) = f32x4{ex2((mw[0] - M) * LOG2E) * inv, ex2((mw[1] - M) * LOG2E) * inv, ex2((mw[2] - M) * LOG2E) * inv, ex2((mw[3] - M) * LOG2E) * inv};
    lds_signal(L + L_FL + F_FW, lane);
    const unsigned o = lane < 8 ? OG_OFF + h * 8192 + (lane >> 1) * 2048 + j * 256 + (lane & 1) * 128 : lane < 17 ? YG_OFF + bid * 1152 + (lane - 8) * 128 : OOB;
    const unsigned t = __builtin_amdgcn_raw_buffer_load_b32(rW, o, 0, AUX_SC1);
    asm volatile("" :: "v"(t));
  }
}

extern "C" void kernel_launch(void* const* d_in, const int* in_sizes, int n_in, void* d_out, int out_size, void* d_ws, size_t ws_size,
                              hipStream_t stream) {
  fused_decode<<<256, 384, 0, stream>>>((const char*)d_in[0], (const char*)d_in[1], (const char*)d_in[2], (const char*)d_in[3],
                                        (const char*)d_in[4], (const char*)d_in[6], (const char*)d_in[7], (const char*)d_in[8],
                                        (char*)d_out, (char*)d_ws);
}
```

```cpp
#include <hip/hip_runtime.h>
#include <hip/hip_bf16.h>

#define GAS __attribute__((address_space(1)))
#define LAS __attribute__((address_space(3)))
typedef unsigned v4u __attribute__((ext_vector_type(4)));
typedef unsigned v3u __attribute__((ext_vector_type(3)));
typedef unsigned v2u __attribute__((ext_vector_type(2)));
typedef float f32x4 __attribute__((ext_vector_type(4)));
typedef float f32x2 __attribute__((ext_vector_type(2)));
typedef __bf16 bf16x8 __attribute__((ext_vector_type(8)));
typedef __bf16 bf16x2 __attribute__((ext_vector_type(2)));
typedef unsigned long long v8q __attribute__((ext_vector_type(8)));
typedef unsigned long long v2q __attribute__((ext_vector_type(2)));
typedef __amdgpu_buffer_rsrc_t rsrc_t;

constexpr int LSTR = 16384;
constexpr int NPRE = 2;
constexpr int NVB = 8 - NPRE;
constexpr int PINFL = 6;
constexpr int TAU = 216;
constexpr int LAG0 = 150, LAGC = 140;

constexpr int E_OFF = 0;
constexpr int RT_OFF = 1024;
constexpr int QG_OFF = 4096;
constexpr int ML_OFF = QG_OFF + 262144;
constexpr int GU_OFF = ML_OFF + 4096;
constexpr int OG_OFF = GU_OFF + 163840;
constexpr int YG_OFF = OG_OFF + 262144;
constexpr int WS_USED = YG_OFF + 294912;

constexpr int L_XN = 0, XN_S = 580;
constexpr int L_HD = 1160, HD_S = 148;
constexpr int L_QB = L_HD + 2 * HD_S;
constexpr int L_OW = L_QB + 64;
constexpr int L_YW = L_OW + 512;
constexpr int L_ML = L_YW + 1056;
constexpr int L_FW = L_ML + 8;
constexpr int L_FL = L_FW + 4;
constexpr int F_KD = 0, F_Q = 4, F_T = 5, F_XN = 7, F_FW = 8, F_TK = 9, F_PB = 10, F_PS = 11;
constexpr int L_END = L_FL + 12;
static_assert(L_FL % 4 == 0 && L_OW % 4 == 0 && L_YW % 4 == 0 && L_QB % 4 == 0 && L_FW % 4 == 0 && L_HD % 4 == 0, "16-byte aligned LDS pieces");

constexpr int AUX_NT = 2, AUX_SC1 = 16;
constexpr unsigned OOB = 0x80000000u;
constexpr unsigned CODE_BYTES = 19200;
constexpr int SPIN_CAP = 1 << 18;
constexpr float LOG2E = 1.4426950408889634f;

__device__ __forceinline__ rsrc_t mk(const void* p, unsigned bytes) { return __builtin_amdgcn_make_buffer_rsrc((void*)p, 0, bytes, 0x00020000); }
__device__ __forceinline__ v4u ld_nt(rsrc_t r, unsigned vo, unsigned so) { return __builtin_amdgcn_raw_buffer_load_b128(r, vo, so, AUX_NT); }
__device__ __forceinline__ v4u ld_sc(rsrc_t r, unsigned vo, unsigned so) { return __builtin_amdgcn_raw_buffer_load_b128(r, vo, so, AUX_SC1); }
__device__ __forceinline__ void st_sc(rsrc_t r, unsigned vo, unsigned so, v4u v) { __builtin_amdgcn_raw_buffer_store_b128(v, r, vo, so, AUX_SC1); }
__device__ __forceinline__ float u2f(unsigned u) { return __builtin_bit_cast(float, u); }
__device__ __forceinline__ unsigned f2u(float f) { return __builtin_bit_cast(unsigned, f); }
__device__ __forceinline__ unsigned cvtpk(float lo, float hi) {
  bf16x2 r = {(__bf16)lo, (__bf16)hi};
  return __builtin_bit_cast(unsigned, r);
}
__device__ __forceinline__ float rbf(float f) { return (float)(__bf16)f; }
__device__ __forceinline__ float ex2(float f) { return __builtin_amdgcn_exp2f(f); }
__device__ __forceinline__ float dot2(unsigned a, unsigned b, float c) {
  return __builtin_amdgcn_fdot2_f32_bf16(__builtin_bit_cast(bf16x2, a), __builtin_bit_cast(bf16x2, b), c, false);
}
__device__ __forceinline__ f32x4 mfma(v4u a, v4u b, f32x4 c) {
  return __builtin_amdgcn_mfma_f32_16x16x32_bf16(__builtin_bit_cast(bf16x8, a), __builtin_bit_cast(bf16x8, b), c, 0, 0, 0);
}
template <int CTRL> __device__ __forceinline__ float dpp(float x) { return u2f(__builtin_amdgcn_mov_dpp(f2u(x), CTRL, 0xf, 0xf, true)); }
template <int CTRL> __device__ __forceinline__ unsigned dppu(unsigned x) { return __builtin_amdgcn_mov_dpp(x, CTRL, 0xf, 0xf, true); }
constexpr int XOR1 = 0xB1, XOR2 = 0x4E, XOR7 = 0x141, XOR8 = 0x128, ROR4 = 0x124, BCAST = 0x150;
__device__ __forceinline__ float add16(float a, float b) {
  auto s = __builtin_amdgcn_permlane16_swap(f2u(a), f2u(b), false, false); return u2f(s[0]) + u2f(s[1]); }
__device__ __forceinline__ float add32(float a, float b) {
  auto s = __builtin_amdgcn_permlane32_swap(f2u(a), f2u(b), false, false); return u2f(s[0]) + u2f(s[1]); }
__device__ __forceinline__ float sum16x(float x) { return add16(x, x); }
__device__ __forceinline__ float sum32x(float x) { return add32(x, x); }
__device__ __forceinline__ float max16x(float x) { auto s = __builtin_amdgcn_permlane16_swap(f2u(x), f2u(x), false, false); return fmaxf(u2f(s[0]), u2f(s[1])); }
__device__ __forceinline__ float max32x(float x) { auto s = __builtin_amdgcn_permlane32_swap(f2u(x), f2u(x), false, false); return fmaxf(u2f(s[0]), u2f(s[1])); }
__device__ __forceinline__ float rowsum(float x) { x += dpp<XOR1>(x); x += dpp<XOR2>(x); x += dpp<XOR7>(x); x += dpp<XOR8>(x); return x; }
__device__ __forceinline__ float rowmax(float x) { x = fmaxf(x, dpp<XOR1>(x)); x = fmaxf(x, dpp<XOR2>(x)); x = fmaxf(x, dpp<XOR7>(x)); x = fmaxf(x, dpp<XOR8>(x)); return x; }
#define SB() __builtin_amdgcn_sched_barrier(0)
#define MEMBAR() asm volatile("" ::: "memory")
#define LDS_FENCE() asm volatile("s_waitcnt lgkmcnt(0)" ::: "memory")
#define LDS_BARRIER() do { SB(); asm volatile("s_waitcnt lgkmcnt(0)\n\ts_barrier" ::: "memory"); SB(); } while (0)
#define VMWAIT(n) asm volatile("s_waitcnt vmcnt(%0)" :: "i"(n) : "memory")
__device__ __forceinline__ unsigned now() { return (unsigned)__builtin_amdgcn_s_memrealtime(); }
__device__ __forceinline__ unsigned lds_ld(LAS unsigned* p) { return __hip_atomic_load(p, __ATOMIC_RELAXED, __HIP_MEMORY_SCOPE_WORKGROUP); }
__device__ __forceinline__ void lds_st(LAS unsigned* p, unsigned v) { __hip_atomic_store(p, v, __ATOMIC_RELAXED, __HIP_MEMORY_SCOPE_WORKGROUP); }
__device__ __forceinline__ void lds_wait(LAS unsigned* p) {
  while (__builtin_amdgcn_readfirstlane(lds_ld(p)) == 0) __builtin_amdgcn_s_sleep(1);
  MEMBAR();
}
__device__ __forceinline__ void lds_wait4(LAS unsigned* p, int lane) {
  while (__builtin_amdgcn_ballot_w64(lds_ld(p + (lane & 3)) == 0) != 0ull) __builtin_amdgcn_s_sleep(4);
  MEMBAR();
}
__device__ __forceinline__ void lds_signal(LAS unsigned* p, int lane) {
  LDS_FENCE();
  if (lane == 0) lds_st(p, 1u);
}
__device__ __forceinline__ bool none_bad(unsigned bad) { return __builtin_amdgcn_ballot_w64(bad != 0u) == 0ull; }
__device__ __forceinline__ unsigned tagbad(const v4u& v, unsigned T) { return (v.y ^ T) | (v.w ^ T); }
__device__ __forceinline__ v4u gran(float a, float b, unsigned T) { return v4u{f2u(a), T, f2u(b), T}; }
__device__ __forceinline__ v4u afrag(unsigned dup, const v4u& m) { return v4u{dup & m.x, dup & m.y, dup & m.z, dup & m.w}; }
template <int D> __device__ __forceinline__ void poll2(rsrc_t rW, unsigned vo, unsigned so, unsigned T, v4u& r0, v4u& r1) {
#pragma unroll 1
  for (int spin = 0; spin < SPIN_CAP; ++spin) {
    MEMBAR();
    r0 = ld_sc(rW, vo, so); r1 = ld_sc(rW, vo, so + D);
    if (none_bad(tagbad(r0, T) | tagbad(r1, T))) break;
  }
}

#define PACE(n) do { const unsigned need_ = tk0 + (((unsigned)(n) * TAU) >> 4); \
    while ((lag = (int)(now() - need_)) < 0) __builtin_amdgcn_s_sleep(1); \
    const int cls_ = (int)(((unsigned)lag * (65536u / 25)) >> 16);     \
    asm volatile("s_cmp_ge_i32 %0, 18\n\ts_cbranch_scc1 1f\n\ts_waitcnt vmcnt(8)\n\ts_cmp_ge_i32 %0, 13\n\ts_cbranch_scc1 1f\n\ts_waitcnt vmcnt(6)\n\t" \
                 "s_cmp_ge_i32 %0, 9\n\ts_cbranch_scc1 1f\n\ts_waitcnt vmcnt(5)\n\ts_cmp_ge_i32 %0, 6\n\ts_cbranch_scc1 1f\n\ts_waitcnt vmcnt(4)\n\t" \
                 "s_cmp_ge_i32 %0, 4\n\ts_cbranch_scc1 1f\n\ts_waitcnt vmcnt(3)\n\ts_cmp_ge_i32 %0, 2\n\ts_cbranch_scc1 1f\n\ts_waitcnt vmcnt(2)\n1:" \
                 :: "s"(cls_) : "scc", "memory"); } while (0)
#define BLOCK_END(n) do { if (sw == 0 && lane == 0) { L[L_FL + F_PS] = tk0 + lag; MEMBAR(); lds_st(L + L_FL + F_PB, (unsigned)(n)); } \
    tk0 = __builtin_amdgcn_readfirstlane(lds_ld(L + L_FL + F_TK)); } while (0)

template <int N, int M> __device__ __forceinline__ f32x4 gemv(f32x4 a0, const unsigned (&xv)[N], const v4u& am, const v4u (&w)[M]) {
  f32x4 a1 = {0.f, 0.f, 0.f, 0.f};
#pragma unroll
  for (int i = 0; i < N; ++i) { if (i & 1) a1 = mfma(afrag(xv[i], am), w[i], a1); else a0 = mfma(afrag(xv[i], am), w[i], a0); }
  return a0 + a1;
}

__global__ void __launch_bounds__(384, 2)
fused_decode(const char* xg_, const char* kc_, const char* vc_, const char* wq_, const char* wo_, const char* wg_, const char* wu_, const char* wd_,
             char* out_, char* ws_) {
  v8q a8; v2q a2;
  asm volatile("s_load_dwordx16 %0, %2, 0x0\n\ts_load_dwordx4 %1, %2, 0x40" : "=&s"(a8), "=&s"(a2) : "s"(__builtin_amdgcn_kernarg_segment_ptr()));
  unsigned long long pc0;
  asm volatile("s_getpc_b64 %0" : "=s"(pc0));
  SB();
  __shared__ __attribute__((aligned(16))) unsigned smem[L_END];
  LAS unsigned* L = (LAS unsigned*)smem;
  LAS float* Lf = (LAS float*)smem;
  const int bid = blockIdx.x, xc = bid & 7, r = bid >> 3;
  int lane = threadIdx.x & 63;
  const int wave = __builtin_amdgcn_readfirstlane(threadIdx.x >> 6);
  const unsigned cv = __builtin_amdgcn_raw_buffer_load_b32(mk((const void*)(pc0 & ~255ull), CODE_BYTES), 0, min((unsigned)(r * 6 + wave) * 128u, CODE_BYTES - 128u), 0);
  const int h = bid & 31, j = bid >> 5, s = j & 3, b = j >> 2;
  unsigned cb = (b * 32 + h) * 2097152u + s * 1024u;
  unsigned vo = wave * 4096 + lane * 16;
  if (lane < 12) L[L_FL + lane] = 0u;
  asm volatile("" : "+v"(vo));
  SB();
  asm volatile("s_waitcnt lgkmcnt(0)\n\ts_barrier" : "+s"(a8), "+s"(a2), "+s"(cb) :: "memory");
  SB();
  const char* xg = (const char*)a8[0]; const char* kc = (const char*)a8[1]; const char* vc = (const char*)a8[2]; const char* wq = (const char*)a8[3];
  const char* wo = (const char*)a8[4]; const char* wg = (const char*)a8[5]; const char* wu = (const char*)a8[6]; const char* wd = (const char*)a8[7];
  char* out = (char*)a2[0]; char* ws = (char*)a2[1];

  if (__builtin_expect(wave < 4, 1)) {
    const int sw = wave;
    const char* kb = kc + cb;
    const char* vb = vc + cb;
    const unsigned span = 2097152u - s * 1024u;
    const rsrc_t rK = mk(kb, span), rV = mk(vb, span);
    v4u a[16], pre[NPRE * 16];
#pragma unroll
    for (int i = 0; i < PINFL / 2; ++i) { a[i] = ld_nt(rK, vo, i * LSTR); pre[i] = ld_nt(rV, vo, (NVB * 16 + i) * LSTR); }
    SB();
    asm volatile("" : "+v"(lane));
#pragma unroll
    for (int i = PINFL / 2; i < NPRE * 16; ++i) { VMWAIT(PINFL - 1); pre[i] = ld_nt(rV, vo, (NVB * 16 + i) * LSTR); SB(); }
#pragma unroll
    for (int i = PINFL / 2; i < 16; ++i) { VMWAIT(PINFL - 1); a[i] = ld_nt(rK, vo, i * LSTR); SB(); }
    const int g = lane >> 4, c = lane & 15;
    lds_wait(L + L_FL + F_Q);
    asm volatile("" :: "v"(cv));
    const v4u q = *(LAS v4u*)(L + L_QB + c * 4);
    unsigned tk0 = __builtin_amdgcn_readfirstlane(L[L_FL + F_TK]);
    int lag = 0;
    float sc[8];
#pragma unroll
    for (int i = 0; i < 8; ++i) sc[i] = 0.f;
#pragma unroll 1
    for (int blk = 0; blk < 8; ++blk) {
      const rsrc_t rN = mk(blk < 7 ? kb : vb, span);
      const unsigned sN = blk < 7 ? (blk + 1) * 16 * LSTR : 0;
      float x[16];
#pragma unroll
      for (int i = 0; i < 16; ++i) {
        const v4u k = a[i];
        float d = dot2(k.x, q.x, 0.f); d = dot2(k.y, q.y, d); d = dot2(k.z, q.z, d); d = dot2(k.w, q.w, d);
        x[i] = d;
        asm volatile("" : "+v"(x[i]));
        SB();
        if (i & 1) {
          PACE(NPRE * 16 + 7 + blk * 16 + i);
          a[i - 1] = ld_nt(rN, vo, sN + (i - 1) * LSTR);
          a[i] = ld_nt(rN, vo, sN + i * LSTR);
          SB();
#define TRC(LO, HI, CTRL, BIT) { const float t1_ = (LO) + dpp<CTRL>(LO), t2_ = (HI) + dpp<CTRL>(HI); LO = (c & (BIT)) ? t2_ : t1_; }
          TRC(x[i - 1], x[i], XOR8, 8)
          if ((i & 3) == 3) TRC(x[(i - 3) & 15], x[i - 1], XOR7, 4)
          if ((i & 7) == 7) TRC(x[(i - 7) & 15], x[(i - 3) & 15], XOR2, 2)
          if (i == 15) TRC(x[0], x[8], XOR1, 1)
          asm volatile("" : "+v"(x[(i & 7) == 7 ? (i == 15 ? 0 : (i - 7) & 15) : (i & 3) == 3 ? (i - 3) & 15 : i - 1]));
          SB();
        }
      }
#pragma unroll
      for (int i = 0; i < 7; ++i) sc[i] = sc[i + 1];
      sc[7] = rbf(rbf(x[0]) * 0.08838834764831845f);
      BLOCK_END(blk + 1);
    }
    float m = sc[0];
#pragma unroll
    for (int i = 1; i < 8; ++i) m = fmaxf(m, sc[i]);
    m = max32x(max16x(rowmax(m)));
    float l = 0.f;
    unsigned pp[8];
#pragma unroll
    for (int i = 0; i < 8; ++i) { const float p = ex2((sc[i] - m) * LOG2E); l += p; pp[i] = cvtpk(p, p); }
    const unsigned hm = (c & 1) ? 0xffff0000u : 0x0000ffffu;
    const int mt = (c & 7) >> 1;
    const v4u am = {mt == 0 ? hm : 0u, mt == 1 ? hm : 0u, mt == 2 ? hm : 0u, mt == 3 ? hm : 0u};
    const v4u pm = {c < 8 ? am.x : 0u, c < 8 ? am.y : 0u, c < 8 ? am.z : 0u, c < 8 ? am.w : 0u};
    f32x4 acc = {0.f, 0.f, 0.f, 0.f};
#define REV4(I) ((((I) & 1) << 3) | (((I) & 2) << 1) | (((I) & 4) >> 1) | (((I) & 8) >> 3))
#define PVM(I, P, V) { acc = mfma(afrag(dppu<BCAST + REV4(I)>(P), pm), (V), acc); asm volatile("" : "+v"(acc)); SB(); }
#define PV2(I) { PVM(I, pp[0], a[I]) PVM((I) + 1, pp[0], a[(I) + 1]) PACE(NPRE * 16 + 136 + blk * 16 + (I)); \
      a[I] = ld_nt(rV, vo, sN + (I) * LSTR); a[(I) + 1] = ld_nt(rV, vo, sN + ((I) + 1) * LSTR); SB(); \
      if (blk == 0) { PVM(I, pq0, pre[I]) PVM((I) + 1, pq0, pre[(I) + 1]) } else if (blk == 1) { PVM(I, pq1, pre[16 + (I)]) PVM((I) + 1, pq1, pre[17 + (I)]) } }
    const unsigned pq0 = pp[NVB], pq1 = pp[NVB + 1];
#pragma unroll 1
    for (int blk = 0; blk < NVB - 1; ++blk) {
      const unsigned sN = (blk + 1) * 16 * LSTR;
      PV2(0)
      if (blk == 0) {
        l = sum32x(sum16x(rowsum(l)));
        if (lane == 0) { Lf[L_ML + 2 * sw] = m; Lf[L_ML + 2 * sw + 1] = l; }
        lds_signal(L + L_FL + F_KD + sw, lane);
        SB();
      }
      PV2(2) PV2(4) PV2(6) PV2(8) PV2(10) PV2(12) PV2(14)
#pragma unroll
      for (int i = 0; i < NVB - 1; ++i) pp[i] = pp[i + 1];
      BLOCK_END(blk + 9);
    }
    const int hp = 4 * (h & 7) + (h >> 3);
    v4u wz[9], wy[8];
    const rsrc_t rD = mk(wd + (size_t)(144 * hp + 36 * sw) * 1152 + j * 144, 36 * 1152 - j * 144);
    const rsrc_t rO = mk(wo + (size_t)(128 * h + 32 * sw) * 1152 + j * 144, 32 * 1152 - j * 144);
    const unsigned to = (c < 9 ? (unsigned)(c * 16) : OOB) + g * 1152;
    const rsrc_t rW = mk(ws, WS_USED);
    lds_wait(L + L_FL + F_FW);
    const unsigned T = __builtin_amdgcn_readfirstlane(L[L_FL + F_T]);
    float fw = Lf[L_FW + sw];
    unsigned hx[9];
#pragma unroll
    for (int i = 0; i < 9; ++i) hx[i] = L[L_HD + (c >> 3) * HD_S + 36 * sw + g + 4 * i];
    const unsigned src = (c >> 3) ? 0x0302u : 0x0100u, sel = (c & 1) ? (src << 16) | 0x0c0cu : 0x0c0c0000u | src;
    v4u sl = {mt == 0 ? sel : 0x0c0c0c0cu, mt == 1 ? sel : 0x0c0c0c0cu, mt == 2 ? sel : 0x0c0c0c0cu, mt == 3 ? sel : 0x0c0c0c0cu};
    unsigned a_ow = (L_OW + sw * 128 + c * 8 + g * 4) * 4, a_or = (L_OW + sw * 32 + (c & 7) * 4 + (c >> 3) * 2) * 4, a_og = g == 0 ? c * 16 : OOB;
    const int yk = (sw & 1) * 16 + (lane >> 2), ykb = yk >= 9 ? 1 : 0;
    unsigned a_yr = (L_YW + ((ykb * 2 + ((lane & 3) >> 1)) * 16 + yk - ykb * 9) * 4 + (lane & 1) * 2) * 4, a_yg = (sw == 0 || lane < 8) ? lane * 16 : OOB;
    const int oo = (h >= 9 ? 1152 - 144 : 0) + 144 * j + 16 * h + (lane & 3) * 4;
    unsigned xres = 0;
    if (sw == 2 && h < 18) xres = *(const GAS unsigned*)(xg + oo);
    asm volatile("" : "+v"(fw), "+v"(sl), "+v"(a_ow), "+v"(a_or), "+v"(a_og), "+v"(a_yr), "+v"(a_yg));
    asm volatile("" : "+v"(hx[0]), "+v"(hx[1]), "+v"(hx[2]), "+v"(hx[3]), "+v"(hx[4]), "+v"(hx[5]), "+v"(hx[6]), "+v"(hx[7]), "+v"(hx[8]));
    SB();
    PVM(0, pp[0], a[0]) PVM(1, pp[0], a[1]) PVM(2, pp[0], a[2]) PVM(3, pp[0], a[3]) PVM(4, pp[0], a[4]) PVM(5, pp[0], a[5]) PVM(6, pp[0], a[6])
    PVM(7, pp[0], a[7]) PVM(8, pp[0], a[8]) PVM(9, pp[0], a[9]) PVM(10, pp[0], a[10]) PVM(11, pp[0], a[11]) PVM(12, pp[0], a[12]) PVM(13, pp[0], a[13])
#pragma unroll
    for (int i = 0; i < 3; ++i) wz[i] = ld_nt(rD, to, i * 4608);
    SB();
    PVM(14, pp[0], a[14])
#pragma unroll
    for (int i = 3; i < 6; ++i) wz[i] = ld_nt(rD, to, i * 4608);
    SB();
    PVM(15, pp[0], a[15])
    if (lane < 32) *(LAS f32x4*)((LAS char*)L + a_ow) = acc * fw;
    LDS_BARRIER();
    {
      f32x2 t = *(LAS f32x2*)((LAS char*)L + a_or);
#pragma unroll
      for (int i = 1; i < 4; ++i) t += *(LAS f32x2*)((LAS char*)L + a_or + i * 512);
      st_sc(rW, a_og, OG_OFF + h * 8192 + sw * 2048 + j * 256, gran(t.x, t.y, T));
      SB();
    }
#pragma unroll
    for (int i = 6; i < 9; ++i) wz[i] = ld_nt(rD, to, i * 4608);
#pragma unroll
    for (int i = 0; i < 8; ++i) wy[i] = ld_nt(rO, to, i * 4608);
    SB();
    f32x4 y = gemv<9>(f32x4{0.f, 0.f, 0.f, 0.f}, hx, am, wz);
    asm volatile("" : "+v"(y));
    v4u p0, p1;
    poll2<1024>(rW, lane * 16, OG_OFF + h * 8192 + sw * 2048, T, p0, p1);
    {
      const float e = add32(add16(u2f(p0.x), u2f(p0.z)), add16(u2f(p1.x), u2f(p1.z)));
      auto eb = __builtin_amdgcn_permlane32_swap(f2u(e), f2u(e), false, false);
      unsigned pk = cvtpk(u2f(eb[0]), u2f(eb[1]));
      pk = __builtin_amdgcn_update_dpp(pk, pk, XOR8, 0xc, 0xf, false);
      f32x4 y1 = {0.f, 0.f, 0.f, 0.f};
#define YM(I, ACC) { const unsigned bc = dppu<BCAST + (I)>(pk); ACC = mfma(v4u{__builtin_amdgcn_perm(0u, bc, sl.x), __builtin_amdgcn_perm(0u, bc, sl.y), \
        __builtin_amdgcn_perm(0u, bc, sl.z), __builtin_amdgcn_perm(0u, bc, sl.w)}, wy[I], ACC); }
      YM(0, y) YM(1, y1) YM(2, y) YM(3, y1) YM(4, y) YM(5, y1) YM(6, y) YM(7, y1)
      y += y1;
    }
    *(LAS f32x4*)(Lf + L_YW + sw * 256 + lane * 4) = y;
    LDS_BARRIER();
    if (sw < 2) {
      f32x2 t = *(LAS f32x2*)((LAS char*)L + a_yr);
#pragma unroll
      for (int i = 1; i < 4; ++i) t += *(LAS f32x2*)((LAS char*)L + a_yr + i * 1024);
      st_sc(rW, a_yg, YG_OFF + bid * 1152 + sw * 1024, gran(t.x, t.y, T));
    } else if (sw == 2 && h < 18) {
      poll2<16 * 1152>(rW, (lane >> 2) * 1152 + (lane & 3) * 16, YG_OFF + j * 32 * 1152 + h * 64, T, p0, p1);
      float y0 = u2f(p0.x) + u2f(p1.x), y1 = u2f(p0.z) + u2f(p1.z);
      y0 += dpp<ROR4>(y0); y1 += dpp<ROR4>(y1);
      y0 += dpp<XOR8>(y0); y1 += dpp<XOR8>(y1);
      y0 = sum32x(add16(y0, y1));
      auto yy = __builtin_amdgcn_permlane16_swap(f2u(y0), f2u(y0), false, false);
      const unsigned res = cvtpk(u2f(xres << 16) + u2f(yy[0]), u2f(xres & 0xffff0000u) + u2f(yy[1]));
      __builtin_amdgcn_raw_buffer_store_b32(res, mk(out, 2304), lane < 4 ? (unsigned)oo : OOB, 0, 0);
      if (bid == 0 && lane == 0) __builtin_amdgcn_raw_buffer_store_b32(T, rW, E_OFF, 0, AUX_SC1);
    }
    return;
  }

  const bool isA = wave == 4;
  const int g = lane >> 4, c = lane & 15;
  const int hp = 4 * (h & 7) + (h >> 3);
  const rsrc_t rW = mk(ws, WS_USED);
  v4u w[36];
  unsigned E = 0, ta = 0;
  const int u = (r & 15) + 16 * (xc & 3), qq = (r >> 4) + 2 * (xc >> 2);
  const int gm = j & 1, half = (j >> 1) & 1, rh = j >> 2;
  const unsigned stride = isA ? 8192 : 9216, cofs = isA ? u * 128 : hp * 288 + half * 144;
  int row0 = isA ? 144 * qq : 288 * rh;
  const rsrc_t rG = mk((isA ? wq : gm ? wu : wg) + cofs, 576 * stride - cofs);
  const unsigned gvo = (c < (isA ? 8 : 9) ? (unsigned)(c * 16) : OOB) + g * stride;
  if (isA) {
    __builtin_amdgcn_s_setprio(3);
    ta = now();
    E = __builtin_amdgcn_raw_buffer_load_b32(rW, E_OFF, 0, AUX_SC1);
  } else {
    __builtin_amdgcn_s_setprio(2);
    v3u xr[3];
    const rsrc_t rX = mk(xg, 2304);
#pragma unroll
    for (int k = 0; k < 3; ++k) xr[k] = __builtin_amdgcn_raw_buffer_load_b96(rX, (lane >> 5) * 1152 + (lane & 31) * 12, k * 384, 0);
    float xv[18];
#pragma unroll
    for (int k = 0; k < 3; ++k)
#pragma unroll
      for (int e = 0; e < 3; ++e) { xv[k * 6 + 2 * e] = u2f(xr[k][e] << 16); xv[k * 6 + 2 * e + 1] = u2f(xr[k][e] & 0xffff0000u); }
    float sum = 0.f;
#pragma unroll
    for (int i = 0; i < 18; ++i) sum += xv[i];
    const float mean = sum16x(rowsum(sum)) * (1.0f / 576.0f);
    float sq = 0.f;
#pragma unroll
    for (int i = 0; i < 18; ++i) { const float d = xv[i] - mean; sq = fmaf(d, d, sq); }
    const float var = sum16x(rowsum(sq)) * (1.0f / 576.0f);
    const float mb = rbf(mean), sd = rbf(__builtin_sqrtf(rbf(rbf(var) + 1e-5f))), rs = 1.0f / sd;
    LAS v2u* dst = (LAS v2u*)(L + L_XN + (lane >> 5) * XN_S + (lane & 31) * 6);
#pragma unroll
    for (int k = 0; k < 3; ++k)
#pragma unroll
      for (int e = 0; e < 3; ++e) {
        float n[2];
#pragma unroll
        for (int f = 0; f < 2; ++f) { const float d = rbf(xv[k * 6 + 2 * e + f] - mb); const float q0 = d * rs; n[f] = fmaf(fmaf(-q0, sd, d), rs, q0); }
        dst[k * 96 + e] = v2u{cvtpk(n[0], n[0]), cvtpk(n[1], n[1])};
      }
    lds_signal(L + L_FL + F_XN, lane);
    __builtin_amdgcn_s_setprio(0);
  }
  const unsigned hm = (c & 1) ? 0xffff0000u : 0x0000ffffu;
  const int mt = (c & 7) >> 1;
  const v4u am = {mt == 0 ? hm : 0u, mt == 1 ? hm : 0u, mt == 2 ? hm : 0u, mt == 3 ? hm : 0u};
  unsigned T = 0;
  f32x4 ga = {0.f, 0.f, 0.f, 0.f};
#pragma unroll 1
  for (int t = 0; t < (isA ? 1 : 2); ++t, row0 += 144) {
    unsigned so = row0 * stride;
#pragma unroll
    for (int i = 0; i < 36; ++i) { w[i] = ld_nt(rG, gvo, so); so += 4 * stride; }
    SB();
    if (isA) {
      T = __builtin_amdgcn_readfirstlane(E) + 0x9E3779B9u;
      if (lane == 0) L[L_FL + F_T] = T;
      lds_wait(L + L_FL + F_XN);
    }
    asm volatile("" :: "v"(cv));
    unsigned xv[36];
#pragma unroll
    for (int i = 0; i < 36; ++i) xv[i] = L[L_XN + (c >> 3) * XN_S + row0 + g + 4 * i];
    SB();
    ga = gemv<36>(ga, xv, am, w);
  }
  if (isA) {
    const float s2 = dpp<XOR8>(ga[2]), s3 = dpp<XOR8>(ga[3]);
    const unsigned o = QG_OFF + (((u >> 1) * 2 + (g >> 1)) * 4 + qq) * 1024 + ((u & 1) * 64 + (c & 7) * 8 + (g & 1) * 4 + (c >> 3) * 2) * 8;
    st_sc(rW, o, 0, gran(c < 8 ? ga[0] : s2, c < 8 ? ga[1] : s3, T));
    __builtin_amdgcn_s_sleep(32);
    float q0, q1;
#pragma unroll 1
    for (int spin = 0; spin < SPIN_CAP; ++spin) {
      MEMBAR();
#pragma unroll
      for (int i = 0; i < 4; ++i) w[i] = ld_sc(rW, lane * 16, QG_OFF + ((h * 2 + b) * 4 + i) * 1024);
      q0 = (u2f(w[0].x) + u2f(w[1].x)) + (u2f(w[2].x) + u2f(w[3].x));
      q1 = (u2f(w[0].z) + u2f(w[1].z)) + (u2f(w[2].z) + u2f(w[3].z));
      if (none_bad(tagbad(w[0], T) | tagbad(w[1], T) | tagbad(w[2], T) | tagbad(w[3], T))) break;
      __builtin_amdgcn_s_sleep(16);
    }
    L[L_QB + lane] = cvtpk(q0, q1);
    if (lane == 0) L[L_FL + F_TK] = ta + LAG0;
    lds_signal(L + L_FL + F_Q, lane);
    __builtin_amdgcn_s_setprio(0);
#pragma unroll 1
    for (int k = 0; k < 2; ++k) {
      while (__builtin_amdgcn_readfirstlane(lds_ld(L + L_FL + F_PB)) < 1u + 6u * k) __builtin_amdgcn_s_sleep(8);
      MEMBAR();
      const unsigned ps = lds_ld(L + L_FL + F_PS);
      if (bid < 8) st_sc(rW, lane == 0 ? RT_OFF + k * 256 + bid * 16 : OOB, 0, v4u{ps, T, ps, T});
#pragma unroll 1
      for (int t = 0; t < 3; ++t) {
        __builtin_amdgcn_s_sleep(64);
        MEMBAR();
        const v4u v = ld_sc(rW, (lane & 7) * 16, RT_OFF + k * 256);
        if (!none_bad(tagbad(v, T))) continue;
        int d = (int)(v.x - ps);
        d += (int)dppu<XOR1>(d); d += (int)dppu<XOR2>(d); d += (int)dppu<XOR7>(d);
        if (lane == 0) lds_st(L + L_FL + F_TK, ps + min(max(d >> 3, -500), 500) - LAGC);
        break;
      }
    }
  } else {
    st_sc(rW, c < 9 ? (unsigned)((g >> 1) * 288 + c * 32 + (g & 1) * 16) : OOB, GU_OFF + bid * 640, __builtin_bit_cast(v4u, ga));
    lds_wait4(L + L_FL + F_KD, lane);
    T = __builtin_amdgcn_readfirstlane(L[L_FL + F_T]);
    float mw[4], lw[4];
#pragma unroll
    for (int i = 0; i < 4; ++i) { mw[i] = Lf[L_ML + 2 * i]; lw[i] = Lf[L_ML + 2 * i + 1]; }
    const float mg = fmaxf(fmaxf(mw[0], mw[1]), fmaxf(mw[2], mw[3]));
    float lg = 0.f;
#pragma unroll
    for (int i = 0; i < 4; ++i) lg += lw[i] * ex2((mw[i] - mg) * LOG2E);
    VMWAIT(0);
    st_sc(rW, lane == 0 ? ML_OFF + h * 128 + j * 16 : OOB, 0, gran(mg, lg, T));
    v4u v;
#pragma unroll 1
    for (int spin = 0; spin < SPIN_CAP; ++spin) {
      __builtin_amdgcn_s_sleep(80);
      MEMBAR();
      v = ld_sc(rW, ((lane & 7) ^ (b * 4)) * 16, ML_OFF + h * 128);
      if (none_bad(tagbad(v, T))) break;
    }
    MEMBAR();
#pragma unroll
    for (int i = 0; i < 8; ++i) w[i] = ld_sc(rW, lane < 36 ? (unsigned)(lane * 16) : OOB, GU_OFF + (i * 32 + h) * 640);
#pragma unroll
    for (int hf = 0; hf < 2; ++hf) {
      v4u hv;
#pragma unroll
      for (int e = 0; e < 4; ++e) {
        const float gt = rbf(u2f(w[2 * hf][e]) + u2f(w[4 + 2 * hf][e])), up = rbf(u2f(w[1 + 2 * hf][e]) + u2f(w[5 + 2 * hf][e]));
        const float t2 = 1.5957691216057308f * (gt + 0.044715f * gt * gt * gt);
        const float hh = rbf(gt / (1.0f + ex2(-t2 * LOG2E))) * up;
        hv[e] = cvtpk(hh, hh);
      }
      if (lane < 36) *(LAS v4u*)(L + L_HD + (lane >= 18 ? HD_S - 72 : 0) + hf * 72 + lane * 4) = hv;
    }
    const float ms = u2f(v.x);
    float M = fmaxf(ms, dpp<XOR1>(ms)); M = fmaxf(M, dpp<XOR2>(M));
    float Ls = u2f(v.z) * ex2((ms - M) * LOG2E);
    Ls += dpp<XOR1>(Ls); Ls += dpp<XOR2>(Ls);
    const float inv = 1.0f / Ls;
    if (lane == 0) *(LAS f32x4*)(Lf + L_FW) = f32x4{ex2((mw[0] - M) * LOG2E) * inv, ex2((mw[1] - M) * LOG2E) * inv, ex2((mw[2] - M) * LOG2E) * inv, ex2((mw[3] - M) * LOG2E) * inv};
    lds_signal(L + L_FL + F_FW, lane);
    const unsigned o = lane < 8 ? OG_OFF + h * 8192 + (lane >> 1) * 2048 + j * 256 + (lane & 1) * 128 : lane < 17 ? YG_OFF + bid * 1152 + (lane - 8) * 128 : OOB;
    const unsigned t = __builtin_amdgcn_raw_buffer_load_b32(rW, o, 0, AUX_SC1);
    asm volatile("" :: "v"(t));
  }
}

extern "C" void kernel_launch(void* const* d_in, const int* in_sizes, int n_in, void* d_out, int out_size, void* d_ws, size_t ws_size,
                              hipStream_t stream) {
  fused_decode<<<256, 384, 0, stream>>>((const char*)d_in[0], (const char*)d_in[1], (const char*)d_in[2], (const char*)d_in[3],
                                        (const char*)d_in[4], (const char*)d_in[6], (const char*)d_in[7], (const char*)d_in[8],
                                        (char*)d_out, (char*)d_ws);
}
```

```cpp
#define PH 1
#include <hip/hip_runtime.h>
#include <hip/hip_bf16.h>

#define GAS __attribute__((address_space(1)))
#define LAS __attribute__((address_space(3)))
typedef unsigned v4u __attribute__((ext_vector_type(4)));
typedef unsigned v3u __attribute__((ext_vector_type(3)));
typedef unsigned v2u __attribute__((ext_vector_type(2)));
typedef float f32x4 __attribute__((ext_vector_type(4)));
typedef float f32x2 __attribute__((ext_vector_type(2)));
typedef __bf16 bf16x8 __attribute__((ext_vector_type(8)));
typedef __bf16 bf16x2 __attribute__((ext_vector_type(2)));
typedef unsigned long long v8q __attribute__((ext_vector_type(8)));
typedef unsigned long long v2q __attribute__((ext_vector_type(2)));
typedef __amdgpu_buffer_rsrc_t rsrc_t;

constexpr int LSTR = 16384;
constexpr int NPRE = 2;
constexpr int NVB = 8 - NPRE;
constexpr int PINFL = 6;
constexpr int TAU = 216;
constexpr int LAG0 = 150, LAGC = 140;

constexpr int E_OFF = 0;
constexpr int RT_OFF = 1024;
constexpr int QG_OFF = 4096;
constexpr int ML_OFF = QG_OFF + 262144;
constexpr int GU_OFF = ML_OFF + 4096;
constexpr int OG_OFF = GU_OFF + 163840;
constexpr int YG_OFF = OG_OFF + 262144;
constexpr int WS_USED = YG_OFF + 294912;

constexpr int L_XN = 0, XN_S = 580;
constexpr int L_HD = 1160, HD_S = 148;
constexpr int L_QB = L_HD + 2 * HD_S;
constexpr int L_OW = L_QB + 64;
constexpr int L_YW = L_OW + 512;
constexpr int L_ML = L_YW + 1056;
constexpr int L_FW = L_ML + 8;
constexpr int L_FL = L_FW + 4;
constexpr int F_KD = 0, F_Q = 4, F_T = 5, F_XN = 7, F_FW = 8, F_TK = 9, F_PB = 10, F_PS = 11;
constexpr int L_END = L_FL + 12;
static_assert(L_FL % 4 == 0 && L_OW % 4 == 0 && L_YW % 4 == 0 && L_QB % 4 == 0 && L_FW % 4 == 0 && L_HD % 4 == 0, "16-byte aligned LDS pieces");

constexpr int AUX_NT = 2, AUX_SC1 = 16;
constexpr unsigned OOB = 0x80000000u;
constexpr unsigned CODE_BYTES = 19200;
constexpr int SPIN_CAP = 1 << 18;
constexpr float LOG2E = 1.4426950408889634f;

__device__ __forceinline__ rsrc_t mk(const void* p, unsigned bytes) { return __builtin_amdgcn_make_buffer_rsrc((void*)p, 0, bytes, 0x00020000); }
__device__ __forceinline__ v4u ld_nt(rsrc_t r, unsigned vo, unsigned so) { return __builtin_amdgcn_raw_buffer_load_b128(r, vo, so, AUX_NT); }
__device__ __forceinline__ v4u ld_sc(rsrc_t r, unsigned vo, unsigned so) { return __builtin_amdgcn_raw_buffer_load_b128(r, vo, so, AUX_SC1); }
__device__ __forceinline__ void st_sc(rsrc_t r, unsigned vo, unsigned so, v4u v) { __builtin_amdgcn_raw_buffer_store_b128(v, r, vo, so, AUX_SC1); }
__device__ __forceinline__ float u2f(unsigned u) { return __builtin_bit_cast(float, u); }
__device__ __forceinline__ unsigned f2u(float f) { return __builtin_bit_cast(unsigned, f); }
__device__ __forceinline__ unsigned cvtpk(float lo, float hi) {
  bf16x2 r = {(__bf16)lo, (__bf16)hi};
  return __builtin_bit_cast(unsigned, r);
}
__device__ __forceinline__ float rbf(float f) { return (float)(__bf16)f; }
__device__ __forceinline__ float ex2(float f) { return __builtin_amdgcn_exp2f(f); }
__device__ __forceinline__ float dot2(unsigned a, unsigned b, float c) {
  return __builtin_amdgcn_fdot2_f32_bf16(__builtin_bit_cast(bf16x2, a), __builtin_bit_cast(bf16x2, b), c, false);
}
__device__ __forceinline__ f32x4 mfma(v4u a, v4u b, f32x4 c) {
  return __builtin_amdgcn_mfma_f32_16x16x32_bf16(__builtin_bit_cast(bf16x8, a), __builtin_bit_cast(bf16x8, b), c, 0, 0, 0);
}
template <int CTRL> __device__ __forceinline__ float dpp(float x) { return u2f(__builtin_amdgcn_mov_dpp(f2u(x), CTRL, 0xf, 0xf, true)); }
template <int CTRL> __device__ __forceinline__ unsigned dppu(unsigned x) { return __builtin_amdgcn_mov_dpp(x, CTRL, 0xf, 0xf, true); }
constexpr int XOR1 = 0xB1, XOR2 = 0x4E, XOR7 = 0x141, XOR8 = 0x128, ROR4 = 0x124, BCAST = 0x150;
__device__ __forceinline__ float add16(float a, float b) {
  auto s = __builtin_amdgcn_permlane16_swap(f2u(a), f2u(b), false, false); return u2f(s[0]) + u2f(s[1]); }
__device__ __forceinline__ float add32(float a, float b) {
  auto s = __builtin_amdgcn_permlane32_swap(f2u(a), f2u(b), false, false); return u2f(s[0]) + u2f(s[1]); }
__device__ __forceinline__ float sum16x(float x) { return add16(x, x); }
__device__ __forceinline__ float sum32x(float x) { return add32(x, x); }
__device__ __forceinline__ float max16x(float x) { auto s = __builtin_amdgcn_permlane16_swap(f2u(x), f2u(x), false, false); return fmaxf(u2f(s[0]), u2f(s[1])); }
__device__ __forceinline__ float max32x(float x) { auto s = __builtin_amdgcn_permlane32_swap(f2u(x), f2u(x), false, false); return fmaxf(u2f(s[0]), u2f(s[1])); }
__device__ __forceinline__ float rowsum(float x) { x += dpp<XOR1>(x); x += dpp<XOR2>(x); x += dpp<XOR7>(x); x += dpp<XOR8>(x); return x; }
__device__ __forceinline__ float rowmax(float x) { x = fmaxf(x, dpp<XOR1>(x)); x = fmaxf(x, dpp<XOR2>(x)); x = fmaxf(x, dpp<XOR7>(x)); x = fmaxf(x, dpp<XOR8>(x)); return x; }
#define SB() __builtin_amdgcn_sched_barrier(0)
#define MEMBAR() asm volatile("" ::: "memory")
#define LDS_FENCE() asm volatile("s_waitcnt lgkmcnt(0)" ::: "memory")
#define LDS_BARRIER() do { SB(); asm volatile("s_waitcnt lgkmcnt(0)\n\ts_barrier" ::: "memory"); SB(); } while (0)
#define VMWAIT(n) asm volatile("s_waitcnt vmcnt(%0)" :: "i"(n) : "memory")
__device__ __forceinline__ unsigned now() { return (unsigned)__builtin_amdgcn_s_memrealtime(); }
__device__ __forceinline__ unsigned lds_ld(LAS unsigned* p) { return __hip_atomic_load(p, __ATOMIC_RELAXED, __HIP_MEMORY_SCOPE_WORKGROUP); }
__device__ __forceinline__ void lds_st(LAS unsigned* p, unsigned v) { __hip_atomic_store(p, v, __ATOMIC_RELAXED, __HIP_MEMORY_SCOPE_WORKGROUP); }
__device__ __forceinline__ void lds_wait(LAS unsigned* p) {
  while (__builtin_amdgcn_readfirstlane(lds_ld(p)) == 0) __builtin_amdgcn_s_sleep(1);
  MEMBAR();
}
__device__ __forceinline__ void lds_wait4(LAS unsigned* p, int lane) {
  while (__builtin_amdgcn_ballot_w64(lds_ld(p + (lane & 3)) == 0) != 0ull) __builtin_amdgcn_s_sleep(4);
  MEMBAR();
}
__device__ __forceinline__ void lds_signal(LAS unsigned* p, int lane) {
  LDS_FENCE();
  if (lane == 0) lds_st(p, 1u);
}
__device__ __forceinline__ bool none_bad(unsigned bad) { return __builtin_amdgcn_ballot_w64(bad != 0u) == 0ull; }
__device__ __forceinline__ unsigned tagbad(const v4u& v, unsigned T) { return (v.y ^ T) | (v.w ^ T); }
__device__ __forceinline__ v4u gran(float a, float b, unsigned T) { return v4u{f2u(a), T, f2u(b), T}; }
__device__ __forceinline__ v4u afrag(unsigned dup, const v4u& m) { return v4u{dup & m.x, dup & m.y, dup & m.z, dup & m.w}; }
template <int D> __device__ __forceinline__ void poll2(rsrc_t rW, unsigned vo, unsigned so, unsigned T, v4u& r0, v4u& r1) {
#pragma unroll 1
  for (int spin = 0; spin < SPIN_CAP; ++spin) {
    MEMBAR();
    r0 = ld_sc(rW, vo, so); r1 = ld_sc(rW, vo, so + D);
    if (none_bad(tagbad(r0, T) | tagbad(r1, T))) break;
  }
}

#define PACE(n) do { const unsigned need_ = tk0 + (((unsigned)(n) * TAU) >> 4); \
    while ((lag = (int)(now() - need_)) < 0) __builtin_amdgcn_s_sleep(1); \
    const int cls_ = (int)(((unsigned)lag * (65536u / 25)) >> 16);     \
    asm volatile("s_cmp_ge_i32 %0, 18\n\ts_cbranch_scc1 1f\n\ts_waitcnt vmcnt(8)\n\ts_cmp_ge_i32 %0, 13\n\ts_cbranch_scc1 1f\n\ts_waitcnt vmcnt(6)\n\t" \
                 "s_cmp_ge_i32 %0, 9\n\ts_cbranch_scc1 1f\n\ts_waitcnt vmcnt(5)\n\ts_cmp_ge_i32 %0, 6\n\ts_cbranch_scc1 1f\n\ts_waitcnt vmcnt(4)\n\t" \
                 "s_cmp_ge_i32 %0, 4\n\ts_cbranch_scc1 1f\n\ts_waitcnt vmcnt(3)\n\ts_cmp_ge_i32 %0, 2\n\ts_cbranch_scc1 1f\n\ts_waitcnt vmcnt(2)\n1:" \
                 :: "s"(cls_) : "scc", "memory"); } while (0)
#define BLOCK_END(n) do { if (sw == 0 && lane == 0) { L[L_FL + F_PS] = tk0 + lag; MEMBAR(); lds_st(L + L_FL + F_PB, (unsigned)(n)); } \
    tk0 = __builtin_amdgcn_readfirstlane(lds_ld(L + L_FL + F_TK)); } while (0)

template <int N, int M> __device__ __forceinline__ f32x4 gemv(f32x4 a0, const unsigned (&xv)[N], const v4u& am, const v4u (&w)[M]) {
  f32x4 a1 = {0.f, 0.f, 0.f, 0.f};
#pragma unroll
  for (int i = 0; i < N; ++i) { if (i & 1) a1 = mfma(afrag(xv[i], am), w[i], a1); else a0 = mfma(afrag(xv[i], am), w[i], a0); }
  return a0 + a1;
}

__global__ void __launch_bounds__(384, 2)
fused_decode(const char* xg_, const char* kc_, const char* vc_, const char* wq_, const char* wo_, const char* wg_, const char* wu_, const char* wd_,
             char* out_, char* ws_) {
  v8q a8; v2q a2;
  asm volatile("s_load_dwordx16 %0, %2, 0x0\n\ts_load_dwordx4 %1, %2, 0x40" : "=&s"(a8), "=&s"(a2) : "s"(__builtin_amdgcn_kernarg_segment_ptr()));
  unsigned long long pc0;
  asm volatile("s_getpc_b64 %0" : "=s"(pc0));
  unsigned long long te_; asm volatile("s_memrealtime %0" : "=s"(te_));
  SB();
  __shared__ __attribute__((aligned(16))) unsigned smem[L_END];
  LAS unsigned* L = (LAS unsigned*)smem;
  LAS float* Lf = (LAS float*)smem;
  const int bid = blockIdx.x, xc = bid & 7, r = bid >> 3;
  int lane = threadIdx.x & 63;
  const int wave = __builtin_amdgcn_readfirstlane(threadIdx.x >> 6);
  const unsigned cv = __builtin_amdgcn_raw_buffer_load_b32(mk((const void*)(pc0 & ~255ull), CODE_BYTES), 0, min((unsigned)(r * 6 + wave) * 128u, CODE_BYTES - 128u), 0);
  const int h = bid & 31, j = bid >> 5, s = j & 3, b = j >> 2;
  unsigned cb = (b * 32 + h) * 2097152u + s * 1024u;
  unsigned vo = wave * 4096 + lane * 16;
  if (lane < 12) L[L_FL + lane] = 0u;
  asm volatile("" : "+v"(vo));
  SB();
  asm volatile("s_waitcnt lgkmcnt(0)\n\ts_barrier" : "+s"(a8), "+s"(a2), "+s"(cb), "+s"(te_) :: "memory");
  SB();
  const char* xg = (const char*)a8[0]; const char* kc = (const char*)a8[1]; const char* vc = (const char*)a8[2]; const char* wq = (const char*)a8[3];
  const char* wo = (const char*)a8[4]; const char* wg = (const char*)a8[5]; const char* wu = (const char*)a8[6]; const char* wd = (const char*)a8[7];
  char* out = (char*)a2[0]; char* ws = (char*)a2[1];

  if (__builtin_expect(wave < 4, 1)) {
    const int sw = wave;
    unsigned tph = (unsigned)te_;
    if (PH == 1) tph = now();
    const char* kb = kc + cb;
    const char* vb = vc + cb;
    const unsigned span = 2097152u - s * 1024u;
    const rsrc_t rK = mk(kb, span), rV = mk(vb, span);
    v4u a[16], pre[NPRE * 16];
#pragma unroll
    for (int i = 0; i < PINFL / 2; ++i) { a[i] = ld_nt(rK, vo, i * LSTR); pre[i] = ld_nt(rV, vo, (NVB * 16 + i) * LSTR); }
    SB();
    asm volatile("" : "+v"(lane));
#pragma unroll
    for (int i = PINFL / 2; i < NPRE * 16; ++i) { VMWAIT(PINFL - 1); if (PH == 2 && i == PINFL / 2) tph = now(); if (PH == 3 && i == PINFL / 2 + 5) tph = now(); pre[i] = ld_nt(rV, vo, (NVB * 16 + i) * LSTR); SB(); }
#pragma unroll
    for (int i = PINFL / 2; i < 16; ++i) { VMWAIT(PINFL - 1); a[i] = ld_nt(rK, vo, i * LSTR); SB(); }
    const int g = lane >> 4, c = lane & 15;
    lds_wait(L + L_FL + F_Q);
    asm volatile("" :: "v"(cv));
    const v4u q = *(LAS v4u*)(L + L_QB + c * 4);
    unsigned tk0 = __builtin_amdgcn_readfirstlane(L[L_FL + F_TK]);
    int lag = 0;
    float sc[8];
#pragma unroll
    for (int i = 0; i < 8; ++i) sc[i] = 0.f;
#pragma unroll 1
    for (int blk = 0; blk < 8; ++blk) {
      const rsrc_t rN = mk(blk < 7 ? kb : vb, span);
      const unsigned sN = blk < 7 ? (blk + 1) * 16 * LSTR : 0;
      float x[16];
#pragma unroll
      for (int i = 0; i < 16; ++i) {
        const v4u k = a[i];
        float d = dot2(k.x, q.x, 0.f); d = dot2(k.y, q.y, d); d = dot2(k.z, q.z, d); d = dot2(k.w, q.w, d);
        x[i] = d;
        asm volatile("" : "+v"(x[i]));
        SB();
        if (i & 1) {
          PACE(NPRE * 16 + 7 + blk * 16 + i);
          a[i - 1] = ld_nt(rN, vo, sN + (i - 1) * LSTR);
          a[i] = ld_nt(rN, vo, sN + i * LSTR);
          SB();
#define TRC(LO, HI, CTRL, BIT) { const float t1_ = (LO) + dpp<CTRL>(LO), t2_ = (HI) + dpp<CTRL>(HI); LO = (c & (BIT)) ? t2_ : t1_; }
          TRC(x[i - 1], x[i], XOR8, 8)
          if ((i & 3) == 3) TRC(x[(i - 3) & 15], x[i - 1], XOR7, 4)
          if ((i & 7) == 7) TRC(x[(i - 7) & 15], x[(i - 3) & 15], XOR2, 2)
          if (i == 15) TRC(x[0], x[8], XOR1, 1)
          asm volatile("" : "+v"(x[(i & 7) == 7 ? (i == 15 ? 0 : (i - 7) & 15) : (i & 3) == 3 ? (i - 3) & 15 : i - 1]));
          SB();
        }
      }
#pragma unroll
      for (int i = 0; i < 7; ++i) sc[i] = sc[i + 1];
      sc[7] = rbf(rbf(x[0]) * 0.08838834764831845f);
      BLOCK_END(blk + 1);
    }
    float m = sc[0];
#pragma unroll
    for (int i = 1; i < 8; ++i) m = fmaxf(m, sc[i]);
    m = max32x(max16x(rowmax(m)));
    float l = 0.f;
    unsigned pp[8];
#pragma unroll
    for (int i = 0; i < 8; ++i) { const float p = ex2((sc[i] - m) * LOG2E); l += p; pp[i] = cvtpk(p, p); }
    const unsigned hm = (c & 1) ? 0xffff0000u : 0x0000ffffu;
    const int mt = (c & 7) >> 1;
    const v4u am = {mt == 0 ? hm : 0u, mt == 1 ? hm : 0u, mt == 2 ? hm : 0u, mt == 3 ? hm : 0u};
    const v4u pm = {c < 8 ? am.x : 0u, c < 8 ? am.y : 0u, c < 8 ? am.z : 0u, c < 8 ? am.w : 0u};
    f32x4 acc = {0.f, 0.f, 0.f, 0.f};
#define REV4(I) ((((I) & 1) << 3) | (((I) & 2) << 1) | (((I) & 4) >> 1) | (((I) & 8) >> 3))
#define PVM(I, P, V) { acc = mfma(afrag(dppu<BCAST + REV4(I)>(P), pm), (V), acc); asm volatile("" : "+v"(acc)); SB(); }
#define PV2(I) { PVM(I, pp[0], a[I]) PVM((I) + 1, pp[0], a[(I) + 1]) PACE(NPRE * 16 + 136 + blk * 16 + (I)); \
      a[I] = ld_nt(rV, vo, sN + (I) * LSTR); a[(I) + 1] = ld_nt(rV, vo, sN + ((I) + 1) * LSTR); SB(); \
      if (blk == 0) { PVM(I, pq0, pre[I]) PVM((I) + 1, pq0, pre[(I) + 1]) } else if (blk == 1) { PVM(I, pq1, pre[16 + (I)]) PVM((I) + 1, pq1, pre[17 + (I)]) } }
    const unsigned pq0 = pp[NVB], pq1 = pp[NVB + 1];
#pragma unroll 1
    for (int blk = 0; blk < NVB - 1; ++blk) {
      const unsigned sN = (blk + 1) * 16 * LSTR;
      PV2(0)
      if (blk == 0) {
        l = sum32x(sum16x(rowsum(l)));
        if (lane == 0) { Lf[L_ML + 2 * sw] = m; Lf[L_ML + 2 * sw + 1] = l; }
        lds_signal(L + L_FL + F_KD + sw, lane);
        SB();
      }
      PV2(2) PV2(4) PV2(6) PV2(8) PV2(10) PV2(12) PV2(14)
#pragma unroll
      for (int i = 0; i < NVB - 1; ++i) pp[i] = pp[i + 1];
      BLOCK_END(blk + 9);
    }
    const int hp = 4 * (h & 7) + (h >> 3);
    v4u wz[9], wy[8];
    const rsrc_t rD = mk(wd + (size_t)(144 * hp + 36 * sw) * 1152 + j * 144, 36 * 1152 - j * 144);
    const rsrc_t rO = mk(wo + (size_t)(128 * h + 32 * sw) * 1152 + j * 144, 32 * 1152 - j * 144);
    const unsigned to = (c < 9 ? (unsigned)(c * 16) : OOB) + g * 1152;
    const rsrc_t rW = mk(ws, WS_USED);
    lds_wait(L + L_FL + F_FW);
    const unsigned T = __builtin_amdgcn_readfirstlane(L[L_FL + F_T]);
    float fw = Lf[L_FW + sw];
    unsigned hx[9];
#pragma unroll
    for (int i = 0; i < 9; ++i) hx[i] = L[L_HD + (c >> 3) * HD_S + 36 * sw + g + 4 * i];
    const unsigned src = (c >> 3) ? 0x0302u : 0x0100u, sel = (c & 1) ? (src << 16) | 0x0c0cu : 0x0c0c0000u | src;
    v4u sl = {mt == 0 ? sel : 0x0c0c0c0cu, mt == 1 ? sel : 0x0c0c0c0cu, mt == 2 ? sel : 0x0c0c0c0cu, mt == 3 ? sel : 0x0c0c0c0cu};
    unsigned a_ow = (L_OW + sw * 128 + c * 8 + g * 4) * 4, a_or = (L_OW + sw * 32 + (c & 7) * 4 + (c >> 3) * 2) * 4, a_og = g == 0 ? c * 16 : OOB;
    const int yk = (sw & 1) * 16 + (lane >> 2), ykb = yk >= 9 ? 1 : 0;
    unsigned a_yr = (L_YW + ((ykb * 2 + ((lane & 3) >> 1)) * 16 + yk - ykb * 9) * 4 + (lane & 1) * 2) * 4, a_yg = (sw == 0 || lane < 8) ? lane * 16 : OOB;
    const int oo = (h >= 9 ? 1152 - 144 : 0) + 144 * j + 16 * h + (lane & 3) * 4;
    unsigned xres = 0;
    if (sw == 2 && h < 18) xres = *(const GAS unsigned*)(xg + oo);
    asm volatile("" : "+v"(fw), "+v"(sl), "+v"(a_ow), "+v"(a_or), "+v"(a_og), "+v"(a_yr), "+v"(a_yg));
    asm volatile("" : "+v"(hx[0]), "+v"(hx[1]), "+v"(hx[2]), "+v"(hx[3]), "+v"(hx[4]), "+v"(hx[5]), "+v"(hx[6]), "+v"(hx[7]), "+v"(hx[8]));
    SB();
    PVM(0, pp[0], a[0]) PVM(1, pp[0], a[1]) PVM(2, pp[0], a[2]) PVM(3, pp[0], a[3]) PVM(4, pp[0], a[4]) PVM(5, pp[0], a[5]) PVM(6, pp[0], a[6])
    PVM(7, pp[0], a[7]) PVM(8, pp[0], a[8]) PVM(9, pp[0], a[9]) PVM(10, pp[0], a[10]) PVM(11, pp[0], a[11]) PVM(12, pp[0], a[12]) PVM(13, pp[0], a[13])
#pragma unroll
    for (int i = 0; i < 3; ++i) wz[i] = ld_nt(rD, to, i * 4608);
    SB();
    PVM(14, pp[0], a[14])
#pragma unroll
    for (int i = 3; i < 6; ++i) wz[i] = ld_nt(rD, to, i * 4608);
    SB();
    PVM(15, pp[0], a[15])
    if (lane < 32) *(LAS f32x4*)((LAS char*)L + a_ow) = acc * fw;
    LDS_BARRIER();
    {
      f32x2 t = *(LAS f32x2*)((LAS char*)L + a_or);
#pragma unroll
      for (int i = 1; i < 4; ++i) t += *(LAS f32x2*)((LAS char*)L + a_or + i * 512);
      st_sc(rW, a_og, OG_OFF + h * 8192 + sw * 2048 + j * 256, gran(t.x, t.y, T));
      SB();
    }
#pragma unroll
    for (int i = 6; i < 9; ++i) wz[i] = ld_nt(rD, to, i * 4608);
#pragma unroll
    for (int i = 0; i < 8; ++i) wy[i] = ld_nt(rO, to, i * 4608);
    SB();
    f32x4 y = gemv<9>(f32x4{0.f, 0.f, 0.f, 0.f}, hx, am, wz);
    asm volatile("" : "+v"(y));
    v4u p0, p1;
    poll2<1024>(rW, lane * 16, OG_OFF + h * 8192 + sw * 2048, T, p0, p1);
    {
      const float e = add32(add16(u2f(p0.x), u2f(p0.z)), add16(u2f(p1.x), u2f(p1.z)));
      auto eb = __builtin_amdgcn_permlane32_swap(f2u(e), f2u(e), false, false);
      unsigned pk = cvtpk(u2f(eb[0]), u2f(eb[1]));
      pk = __builtin_amdgcn_update_dpp(pk, pk, XOR8, 0xc, 0xf, false);
      f32x4 y1 = {0.f, 0.f, 0.f, 0.f};
#define YM(I, ACC) { const unsigned bc = dppu<BCAST + (I)>(pk); ACC = mfma(v4u{__builtin_amdgcn_perm(0u, bc, sl.x), __builtin_amdgcn_perm(0u, bc, sl.y), \
        __builtin_amdgcn_perm(0u, bc, sl.z), __builtin_amdgcn_perm(0u, bc, sl.w)}, wy[I], ACC); }
      YM(0, y) YM(1, y1) YM(2, y) YM(3, y1) YM(4, y) YM(5, y1) YM(6, y) YM(7, y1)
      y += y1;
    }
    *(LAS f32x4*)(Lf + L_YW + sw * 256 + lane * 4) = y;
    LDS_BARRIER();
    if (sw < 2) {
      f32x2 t = *(LAS f32x2*)((LAS char*)L + a_yr);
#pragma unroll
      for (int i = 1; i < 4; ++i) t += *(LAS f32x2*)((LAS char*)L + a_yr + i * 1024);
      st_sc(rW, a_yg, YG_OFF + bid * 1152 + sw * 1024, gran(t.x, t.y, T));
    } else if (sw == 2 && h < 18) {
      poll2<16 * 1152>(rW, (lane >> 2) * 1152 + (lane & 3) * 16, YG_OFF + j * 32 * 1152 + h * 64, T, p0, p1);
      float y0 = u2f(p0.x) + u2f(p1.x), y1 = u2f(p0.z) + u2f(p1.z);
      y0 += dpp<ROR4>(y0); y1 += dpp<ROR4>(y1);
      y0 += dpp<XOR8>(y0); y1 += dpp<XOR8>(y1);
      y0 = sum32x(add16(y0, y1));
      auto yy = __builtin_amdgcn_permlane16_swap(f2u(y0), f2u(y0), false, false);
      const unsigned res = cvtpk(u2f(xres << 16) + u2f(yy[0]), u2f(xres & 0xffff0000u) + u2f(yy[1]));
      __builtin_amdgcn_raw_buffer_store_b32(res, mk(out, 2304), lane < 4 ? (unsigned)oo : OOB, 0, 0);
      if (bid == 0 && lane == 0) __builtin_amdgcn_raw_buffer_store_b32(T, rW, E_OFF, 0, AUX_SC1);
    }
    while (now() - tph < 8000u) __builtin_amdgcn_s_sleep(2);
    return;
  }

  const bool isA = wave == 4;
  const int g = lane >> 4, c = lane & 15;
  const int hp = 4 * (h & 7) + (h >> 3);
  const rsrc_t rW = mk(ws, WS_USED);
  v4u w[36];
  unsigned E = 0, ta = 0;
  const int u = (r & 15) + 16 * (xc & 3), qq = (r >> 4) + 2 * (xc >> 2);
  const int gm = j & 1, half = (j >> 1) & 1, rh = j >> 2;
  const unsigned stride = isA ? 8192 : 9216, cofs = isA ? u * 128 : hp * 288 + half * 144;
  int row0 = isA ? 144 * qq : 288 * rh;
  const rsrc_t rG = mk((isA ? wq : gm ? wu : wg) + cofs, 576 * stride - cofs);
  const unsigned gvo = (c < (isA ? 8 : 9) ? (unsigned)(c * 16) : OOB) + g * stride;
  if (isA) {
    __builtin_amdgcn_s_setprio(3);
    ta = now();
    E = __builtin_amdgcn_raw_buffer_load_b32(rW, E_OFF, 0, AUX_SC1);
  } else {
    __builtin_amdgcn_s_setprio(2);
    v3u xr[3];
    const rsrc_t rX = mk(xg, 2304);
#pragma unroll
    for (int k = 0; k < 3; ++k) xr[k] = __builtin_amdgcn_raw_buffer_load_b96(rX, (lane >> 5) * 1152 + (lane & 31) * 12, k * 384, 0);
    float xv[18];
#pragma unroll
    for (int k = 0; k < 3; ++k)
#pragma unroll
      for (int e = 0; e < 3; ++e) { xv[k * 6 + 2 * e] = u2f(xr[k][e] << 16); xv[k * 6 + 2 * e + 1] = u2f(xr[k][e] & 0xffff0000u); }
    float sum = 0.f;
#pragma unroll
    for (int i = 0; i < 18; ++i) sum += xv[i];
    const float mean = sum16x(rowsum(sum)) * (1.0f / 576.0f);
    float sq = 0.f;
#pragma unroll
    for (int i = 0; i < 18; ++i) { const float d = xv[i] - mean; sq = fmaf(d, d, sq); }
    const float var = sum16x(rowsum(sq)) * (1.0f / 576.0f);
    const float mb = rbf(mean), sd = rbf(__builtin_sqrtf(rbf(rbf(var) + 1e-5f))), rs = 1.0f / sd;
    LAS v2u* dst = (LAS v2u*)(L + L_XN + (lane >> 5) * XN_S + (lane & 31) * 6);
#pragma unroll
    for (int k = 0; k < 3; ++k)
#pragma unroll
      for (int e = 0; e < 3; ++e) {
        float n[2];
#pragma unroll
        for (int f = 0; f < 2; ++f) { const float d = rbf(xv[k * 6 + 2 * e + f] - mb); const float q0 = d * rs; n[f] = fmaf(fmaf(-q0, sd, d), rs, q0); }
        dst[k * 96 + e] = v2u{cvtpk(n[0], n[0]), cvtpk(n[1], n[1])};
      }
    lds_signal(L + L_FL + F_XN, lane);
    __builtin_amdgcn_s_setprio(0);
  }
  const unsigned hm = (c & 1) ? 0xffff0000u : 0x0000ffffu;
  const int mt = (c & 7) >> 1;
  const v4u am = {mt == 0 ? hm : 0u, mt == 1 ? hm : 0u, mt == 2 ? hm : 0u, mt == 3 ? hm : 0u};
  unsigned T = 0;
  f32x4 ga = {0.f, 0.f, 0.f, 0.f};
#pragma unroll 1
  for (int t = 0; t < (isA ? 1 : 2); ++t, row0 += 144) {
    unsigned so = row0 * stride;
#pragma unroll
    for (int i = 0; i < 36; ++i) { w[i] = ld_nt(rG, gvo, so); so += 4 * stride; }
    SB();
    if (isA) {
      T = __builtin_amdgcn_readfirstlane(E) + 0x9E3779B9u;
      if (lane == 0) L[L_FL + F_T] = T;
      lds_wait(L + L_FL + F_XN);
    }
    asm volatile("" :: "v"(cv));
    unsigned xv[36];
#pragma unroll
    for (int i = 0; i < 36; ++i) xv[i] = L[L_XN + (c >> 3) * XN_S + row0 + g + 4 * i];
    SB();
    ga = gemv<36>(ga, xv, am, w);
  }
  if (isA) {
    const float s2 = dpp<XOR8>(ga[2]), s3 = dpp<XOR8>(ga[3]);
    const unsigned o = QG_OFF + (((u >> 1) * 2 + (g >> 1)) * 4 + qq) * 1024 + ((u & 1) * 64 + (c & 7) * 8 + (g & 1) * 4 + (c >> 3) * 2) * 8;
    st_sc(rW, o, 0, gran(c < 8 ? ga[0] : s2, c < 8 ? ga[1] : s3, T));
    __builtin_amdgcn_s_sleep(32);
    float q0, q1;
#pragma unroll 1
    for (int spin = 0; spin < SPIN_CAP; ++spin) {
      MEMBAR();
#pragma unroll
      for (int i = 0; i < 4; ++i) w[i] = ld_sc(rW, lane * 16, QG_OFF + ((h * 2 + b) * 4 + i) * 1024);
      q0 = (u2f(w[0].x) + u2f(w[1].x)) + (u2f(w[2].x) + u2f(w[3].x));
      q1 = (u2f(w[0].z) + u2f(w[1].z)) + (u2f(w[2].z) + u2f(w[3].z));
      if (none_bad(tagbad(w[0], T) | tagbad(w[1], T) | tagbad(w[2], T) | tagbad(w[3], T))) break;
      __builtin_amdgcn_s_sleep(16);
    }
    L[L_QB + lane] = cvtpk(q0, q1);
    if (lane == 0) L[L_FL + F_TK] = ta + LAG0;
    lds_signal(L + L_FL + F_Q, lane);
    __builtin_amdgcn_s_setprio(0);
#pragma unroll 1
    for (int k = 0; k < 2; ++k) {
      while (__builtin_amdgcn_readfirstlane(lds_ld(L + L_FL + F_PB)) < 1u + 6u * k) __builtin_amdgcn_s_sleep(8);
      MEMBAR();
      const unsigned ps = lds_ld(L + L_FL + F_PS);
      if (bid < 8) st_sc(rW, lane == 0 ? RT_OFF + k * 256 + bid * 16 : OOB, 0, v4u{ps, T, ps, T});
#pragma unroll 1
      for (int t = 0; t < 3; ++t) {
        __builtin_amdgcn_s_sleep(64);
        MEMBAR();
        const v4u v = ld_sc(rW, (lane & 7) * 16, RT_OFF + k * 256);
        if (!none_bad(tagbad(v, T))) continue;
        int d = (int)(v.x - ps);
        d += (int)dppu<XOR1>(d); d += (int)dppu<XOR2>(d); d += (int)dppu<XOR7>(d);
        if (lane == 0) lds_st(L + L_FL + F_TK, ps + min(max(d >> 3, -500), 500) - LAGC);
        break;
      }
    }
  } else {
    st_sc(rW, c < 9 ? (unsigned)((g >> 1) * 288 + c * 32 + (g & 1) * 16) : OOB, GU_OFF + bid * 640, __builtin_bit_cast(v4u, ga));
    lds_wait4(L + L_FL + F_KD, lane);
    T = __builtin_amdgcn_readfirstlane(L[L_FL + F_T]);
    float mw[4], lw[4];
#pragma unroll
    for (int i = 0; i < 4; ++i) { mw[i] = Lf[L_ML + 2 * i]; lw[i] = Lf[L_ML + 2 * i + 1]; }
    const float mg = fmaxf(fmaxf(mw[0], mw[1]), fmaxf(mw[2], mw[3]));
    float lg = 0.f;
#pragma unroll
    for (int i = 0; i < 4; ++i) lg += lw[i] * ex2((mw[i] - mg) * LOG2E);
    VMWAIT(0);
    st_sc(rW, lane == 0 ? ML_OFF + h * 128 + j * 16 : OOB, 0, gran(mg, lg, T));
    v4u v;
#pragma unroll 1
    for (int spin = 0; spin < SPIN_CAP; ++spin) {
      __builtin_amdgcn_s_sleep(80);
      MEMBAR();
      v = ld_sc(rW, ((lane & 7) ^ (b * 4)) * 16, ML_OFF + h * 128);
      if (none_bad(tagbad(v, T))) break;
    }
    MEMBAR();
#pragma unroll
    for (int i = 0; i < 8; ++i) w[i] = ld_sc(rW, lane < 36 ? (unsigned)(lane * 16) : OOB, GU_OFF + (i * 32 + h) * 640);
#pragma unroll
    for (int hf = 0; hf < 2; ++hf) {
      v4u hv;
#pragma unroll
      for (int e = 0; e < 4; ++e) {
        const float gt = rbf(u2f(w[2 * hf][e]) + u2f(w[4 + 2 * hf][e])), up = rbf(u2f(w[1 + 2 * hf][e]) + u2f(w[5 + 2 * hf][e]));
        const float t2 = 1.5957691216057308f * (gt + 0.044715f * gt * gt * gt);
        const float hh = rbf(gt / (1.0f + ex2(-t2 * LOG2E))) * up;
        hv[e] = cvtpk(hh, hh);
      }
      if (lane < 36) *(LAS v4u*)(L + L_HD + (lane >= 18 ? HD_S - 72 : 0) + hf * 72 + lane * 4) = hv;
    }
    const float ms = u2f(v.x);
    float M = fmaxf(ms, dpp<XOR1>(ms)); M = fmaxf(M, dpp<XOR2>(M));
    float Ls = u2f(v.z) * ex2((ms - M) * LOG2E);
    Ls += dpp<XOR1>(Ls); Ls += dpp<XOR2>(Ls);
    const float inv = 1.0f / Ls;
    if (lane == 0) *(LAS f32x4*)(Lf + L_FW) = f32x4{ex2((mw[0] - M) * LOG2E) * inv, ex2((mw[1] - M) * LOG2E) * inv, ex2((mw[2] - M) * LOG2E) * inv, ex2((mw[3] - M) * LOG2E) * inv};
    lds_signal(L + L_FL + F_FW, lane);
    const unsigned o = lane < 8 ? OG_OFF + h * 8192 + (lane >> 1) * 2048 + j * 256 + (lane & 1) * 128 : lane < 17 ? YG_OFF + bid * 1152 + (lane - 8) * 128 : OOB;
    const unsigned t = __builtin_amdgcn_raw_buffer_load_b32(rW, o, 0, AUX_SC1);
    asm volatile("" :: "v"(t));
  }
}

extern "C" void kernel_launch(void* const* d_in, const int* in_sizes, int n_in, void* d_out, int out_size, void* d_ws, size_t ws_size,
                              hipStream_t stream) {
  fused_decode<<<256, 384, 0, stream>>>((const char*)d_in[0], (const char*)d_in[1], (const char*)d_in[2], (const char*)d_in[3],
                                        (const char*)d_in[4], (const char*)d_in[6], (const char*)d_in[7], (const char*)d_in[8],
                                        (char*)d_out, (char*)d_ws);
}
```

```cpp
#define PB 73
#include <hip/hip_runtime.h>
#include <hip/hip_bf16.h>

#define GAS __attribute__((address_space(1)))
#define LAS __attribute__((address_space(3)))
typedef unsigned v4u __attribute__((ext_vector_type(4)));
typedef unsigned v3u __attribute__((ext_vector_type(3)));
typedef unsigned v2u __attribute__((ext_vector_type(2)));
typedef float f32x4 __attribute__((ext_vector_type(4)));
typedef float f32x2 __attribute__((ext_vector_type(2)));
typedef __bf16 bf16x8 __attribute__((ext_vector_type(8)));
typedef __bf16 bf16x2 __attribute__((ext_vector_type(2)));
typedef unsigned long long v8q __attribute__((ext_vector_type(8)));
typedef unsigned long long v2q __attribute__((ext_vector_type(2)));
typedef __amdgpu_buffer_rsrc_t rsrc_t;

constexpr int LSTR = 16384;
constexpr int NPRE = 2;
constexpr int NVB = 8 - NPRE;
constexpr int PINFL = 6;
constexpr int TAU = 217;
constexpr int LAG0 = 150, LAGC = 140;

constexpr int E_OFF = 0;
constexpr int RT_OFF = 1024;
constexpr int QG_OFF = 4096;
constexpr int ML_OFF = QG_OFF + 262144;
constexpr int GU_OFF = ML_OFF + 4096;
constexpr int OG_OFF = GU_OFF + 163840;
constexpr int YG_OFF = OG_OFF + 262144;
constexpr int WS_USED = YG_OFF + 294912;

constexpr int L_XN = 0, XN_S = 580;
constexpr int L_HD = 1160, HD_S = 148;
constexpr int L_QB = L_HD + 2 * HD_S;
constexpr int L_OW = L_QB + 64;
constexpr int L_YW = L_OW + 512;
constexpr int L_ML = L_YW + 1056;
constexpr int L_FW = L_ML + 8;
constexpr int L_FL = L_FW + 4;
constexpr int F_KD = 0, F_Q = 4, F_T = 5, F_XN = 7, F_FW = 8, F_TK = 9, F_PB = 10, F_PS = 11;
constexpr int L_END = L_FL + 12;
static_assert(L_FL % 4 == 0 && L_OW % 4 == 0 && L_YW % 4 == 0 && L_QB % 4 == 0 && L_FW % 4 == 0 && L_HD % 4 == 0, "16-byte aligned LDS pieces");

constexpr int AUX_NT = 2, AUX_SC1 = 16;
constexpr unsigned OOB = 0x80000000u;
constexpr unsigned CODE_BYTES = 20352;
constexpr int SPIN_CAP = 1 << 18;
constexpr float LOG2E = 1.4426950408889634f;

__device__ __forceinline__ rsrc_t mk(const void* p, unsigned bytes) { return __builtin_amdgcn_make_buffer_rsrc((void*)p, 0, bytes, 0x00020000); }
__device__ __forceinline__ v4u ld_nt(rsrc_t r, unsigned vo, unsigned so) { return __builtin_amdgcn_raw_buffer_load_b128(r, vo, so, AUX_NT); }
__device__ __forceinline__ v4u ld_sc(rsrc_t r, unsigned vo, unsigned so) { return __builtin_amdgcn_raw_buffer_load_b128(r, vo, so, AUX_SC1); }
__device__ __forceinline__ void st_sc(rsrc_t r, unsigned vo, unsigned so, v4u v) { __builtin_amdgcn_raw_buffer_store_b128(v, r, vo, so, AUX_SC1); }
__device__ __forceinline__ float u2f(unsigned u) { return __builtin_bit_cast(float, u); }
__device__ __forceinline__ unsigned f2u(float f) { return __builtin_bit_cast(unsigned, f); }
__device__ __forceinline__ unsigned cvtpk(float lo, float hi) {
  bf16x2 r = {(__bf16)lo, (__bf16)hi};
  return __builtin_bit_cast(unsigned, r);
}
__device__ __forceinline__ float rbf(float f) { return (float)(__bf16)f; }
__device__ __forceinline__ float ex2(float f) { return __builtin_amdgcn_exp2f(f); }
__device__ __forceinline__ float dot2(unsigned a, unsigned b, float c) {
  return __builtin_amdgcn_fdot2_f32_bf16(__builtin_bit_cast(bf16x2, a), __builtin_bit_cast(bf16x2, b), c, false);
}
__device__ __forceinline__ f32x4 mfma(v4u a, v4u b, f32x4 c) {
  return __builtin_amdgcn_mfma_f32_16x16x32_bf16(__builtin_bit_cast(bf16x8, a), __builtin_bit_cast(bf16x8, b), c, 0, 0, 0);
}
template <int CTRL> __device__ __forceinline__ float dpp(float x) { return u2f(__builtin_amdgcn_mov_dpp(f2u(x), CTRL, 0xf, 0xf, true)); }
template <int CTRL> __device__ __forceinline__ unsigned dppu(unsigned x) { return __builtin_amdgcn_mov_dpp(x, CTRL, 0xf, 0xf, true); }
constexpr int XOR1 = 0xB1, XOR2 = 0x4E, XOR7 = 0x141, XOR8 = 0x128, ROR4 = 0x124, BCAST = 0x150;
__device__ __forceinline__ float add16(float a, float b) {
  auto s = __builtin_amdgcn_permlane16_swap(f2u(a), f2u(b), false, false); return u2f(s[0]) + u2f(s[1]); }
__device__ __forceinline__ float add32(float a, float b) {
  auto s = __builtin_amdgcn_permlane32_swap(f2u(a), f2u(b), false, false); return u2f(s[0]) + u2f(s[1]); }
__device__ __forceinline__ float sum16x(float x) { return add16(x, x); }
__device__ __forceinline__ float sum32x(float x) { return add32(x, x); }
__device__ __forceinline__ float max16x(float x) { auto s = __builtin_amdgcn_permlane16_swap(f2u(x), f2u(x), false, false); return fmaxf(u2f(s[0]), u2f(s[1])); }
__device__ __forceinline__ float max32x(float x) { auto s = __builtin_amdgcn_permlane32_swap(f2u(x), f2u(x), false, false); return fmaxf(u2f(s[0]), u2f(s[1])); }
__device__ __forceinline__ float rowsum(float x) { x += dpp<XOR1>(x); x += dpp<XOR2>(x); x += dpp<XOR7>(x); x += dpp<XOR8>(x); return x; }
__device__ __forceinline__ float rowmax(float x) { x = fmaxf(x, dpp<XOR1>(x)); x = fmaxf(x, dpp<XOR2>(x)); x = fmaxf(x, dpp<XOR7>(x)); x = fmaxf(x, dpp<XOR8>(x)); return x; }
#define SB() __builtin_amdgcn_sched_barrier(0)
#define MEMBAR() asm volatile("" ::: "memory")
#define LDS_FENCE() asm volatile("s_waitcnt lgkmcnt(0)" ::: "memory")
#define LDS_BARRIER() do { SB(); asm volatile("s_waitcnt lgkmcnt(0)\n\ts_barrier" ::: "memory"); SB(); } while (0)
#define VMWAIT(n) asm volatile("s_waitcnt vmcnt(%0)" :: "i"(n) : "memory")
__device__ __forceinline__ unsigned now() { return (unsigned)__builtin_amdgcn_s_memrealtime(); }
__device__ __forceinline__ unsigned lds_ld(LAS unsigned* p) { return __hip_atomic_load(p, __ATOMIC_RELAXED, __HIP_MEMORY_SCOPE_WORKGROUP); }
__device__ __forceinline__ void lds_st(LAS unsigned* p, unsigned v) { __hip_atomic_store(p, v, __ATOMIC_RELAXED, __HIP_MEMORY_SCOPE_WORKGROUP); }
__device__ __forceinline__ void lds_wait(LAS unsigned* p) {
  while (__builtin_amdgcn_readfirstlane(lds_ld(p)) == 0) __builtin_amdgcn_s_sleep(1);
  MEMBAR();
}
__device__ __forceinline__ void lds_wait4(LAS unsigned* p, int lane) {
  while (__builtin_amdgcn_ballot_w64(lds_ld(p + (lane & 3)) == 0) != 0ull) __builtin_amdgcn_s_sleep(4);
  MEMBAR();
}
__device__ __forceinline__ void lds_signal(LAS unsigned* p, int lane) {
  LDS_FENCE();
  if (lane == 0) lds_st(p, 1u);
}
__device__ __forceinline__ bool none_bad(unsigned bad) { return __builtin_amdgcn_ballot_w64(bad != 0u) == 0ull; }
__device__ __forceinline__ unsigned tagbad(const v4u& v, unsigned T) { return (v.y ^ T) | (v.w ^ T); }
__device__ __forceinline__ v4u gran(float a, float b, unsigned T) { return v4u{f2u(a), T, f2u(b), T}; }
__device__ __forceinline__ v4u afrag(unsigned dup, const v4u& m) { return v4u{dup & m.x, dup & m.y, dup & m.z, dup & m.w}; }
template <int D> __device__ __forceinline__ void poll2(rsrc_t rW, unsigned vo, unsigned so, unsigned T, v4u& r0, v4u& r1) {
#pragma unroll 1
  for (int spin = 0; spin < SPIN_CAP; ++spin) {
    MEMBAR();
    r0 = ld_sc(rW, vo, so); r1 = ld_sc(rW, vo, so + D);
    if (none_bad(tagbad(r0, T) | tagbad(r1, T))) break;
  }
}

#define WINDOW() asm volatile("s_cmp_ge_i32 %0, 18\n\ts_cbranch_scc1 1f\n\ts_waitcnt vmcnt(9)\n\ts_cmp_ge_i32 %0, 13\n\ts_cbranch_scc1 1f\n\ts_waitcnt vmcnt(7)\n\t" \
                              "s_cmp_ge_i32 %0, 9\n\ts_cbranch_scc1 1f\n\ts_waitcnt vmcnt(6)\n\ts_cmp_ge_i32 %0, 6\n\ts_cbranch_scc1 1f\n\ts_waitcnt vmcnt(5)\n\t" \
                              "s_cmp_ge_i32 %0, 4\n\ts_cbranch_scc1 1f\n\ts_waitcnt vmcnt(4)\n\ts_cmp_ge_i32 %0, 2\n\ts_cbranch_scc1 1f\n\ts_waitcnt vmcnt(3)\n1:" \
                              :: "s"(cls) : "scc", "memory")
#define PACE(n) do { const unsigned need_ = tk0 + (((unsigned)(n) * TAU) >> 4); \
    while ((lag = (int)(now() - need_)) < 0) __builtin_amdgcn_s_sleep(1); \
    cls = (int)(((unsigned)lag * (65536u / 25)) >> 16);     \
    WINDOW(); } while (0)
#define BLOCK_END(n) do { if (sw == 0 && lane == 0) { L[L_FL + F_PS] = tk0 + lag; MEMBAR(); lds_st(L + L_FL + F_PB, (unsigned)(n)); } \
    tk0 = __builtin_amdgcn_readfirstlane(lds_ld(L + L_FL + F_TK)); } while (0)

template <int N, int M> __device__ __forceinline__ f32x4 gemv(f32x4 a0, const unsigned (&xv)[N], const v4u& am, const v4u (&w)[M]) {
  f32x4 a1 = {0.f, 0.f, 0.f, 0.f};
#pragma unroll
  for (int i = 0; i < N; ++i) { if (i & 1) a1 = mfma(afrag(xv[i], am), w[i], a1); else a0 = mfma(afrag(xv[i], am), w[i], a0); }
  return a0 + a1;
}

__global__ void __launch_bounds__(384, 2)
fused_decode(const char* xg_, const char* kc_, const char* vc_, const char* wq_, const char* wo_, const char* wg_, const char* wu_, const char* wd_,
             char* out_, char* ws_) {
  v8q a8; v2q a2;
  asm volatile("s_load_dwordx16 %0, %2, 0x0\n\ts_load_dwordx4 %1, %2, 0x40" : "=&s"(a8), "=&s"(a2) : "s"(__builtin_amdgcn_kernarg_segment_ptr()));
  unsigned long long pc0;
  asm volatile("s_getpc_b64 %0" : "=s"(pc0));
  SB();
  __shared__ __attribute__((aligned(16))) unsigned smem[L_END];
  LAS unsigned* L = (LAS unsigned*)smem;
  LAS float* Lf = (LAS float*)smem;
  const int bid = blockIdx.x, xc = bid & 7, r = bid >> 3;
  int lane = threadIdx.x & 63;
  const int wave = __builtin_amdgcn_readfirstlane(threadIdx.x >> 6);
  const unsigned cv = __builtin_amdgcn_raw_buffer_load_b32(mk((const void*)(pc0 & ~255ull), CODE_BYTES), 0, min((unsigned)(r * 6 + wave) * 128u, CODE_BYTES - 128u), 0);
  const int h = bid & 31, j = bid >> 5, s = j & 3, b = j >> 2;
  unsigned cb = (b * 32 + h) * 2097152u + s * 1024u;
  unsigned vo = wave * 4096 + lane * 16;
  if (lane < 12) L[L_FL + lane] = 0u;
  asm volatile("" : "+v"(vo));
  SB();
  asm volatile("s_waitcnt lgkmcnt(0)\n\ts_barrier" : "+s"(a8), "+s"(a2), "+s"(cb) :: "memory");
  SB();
  const char* xg = (const char*)a8[0]; const char* kc = (const char*)a8[1]; const char* vc = (const char*)a8[2]; const char* wq = (const char*)a8[3];
  const char* wo = (const char*)a8[4]; const char* wg = (const char*)a8[5]; const char* wu = (const char*)a8[6]; const char* wd = (const char*)a8[7];
  char* out = (char*)a2[0]; char* ws = (char*)a2[1];

  if (__builtin_expect(wave < 4, 1)) {
    const int sw = wave;
    const unsigned tst = now();
    const char* kb = kc + cb;
    const char* vb = vc + cb;
    const unsigned span = 2097152u - s * 1024u;
    const rsrc_t rK = mk(kb, span), rV = mk(vb, span);
    v4u a[16], pre[NPRE * 16];
#pragma unroll
    for (int i = 0; i < PINFL / 2; ++i) { a[i] = ld_nt(rK, vo, i * LSTR); pre[i] = ld_nt(rV, vo, (NVB * 16 + i) * LSTR); }
    SB();
    asm volatile("" : "+v"(lane));
#pragma unroll
    for (int i = PINFL / 2; i < NPRE * 16; ++i) { VMWAIT(PINFL - 1); pre[i] = ld_nt(rV, vo, (NVB * 16 + i) * LSTR); SB(); }
#pragma unroll
    for (int i = PINFL / 2; i < 16; ++i) { VMWAIT(PINFL - 1); a[i] = ld_nt(rK, vo, i * LSTR); SB(); }
    const int g = lane >> 4, c = lane & 15;
    lds_wait(L + L_FL + F_Q);
    asm volatile("" :: "v"(cv));
    const v4u q = *(LAS v4u*)(L + L_QB + c * 4);
    unsigned tk0 = __builtin_amdgcn_readfirstlane(L[L_FL + F_TK]);
    int lag = 0, cls = 0;
    float sc[8];
#pragma unroll
    for (int i = 0; i < 8; ++i) sc[i] = 0.f;
#pragma unroll 1
    for (int blk = 0; blk < 8; ++blk) {
      const rsrc_t rN = mk(blk < 7 ? kb : vb, span);
      const unsigned sN = blk < 7 ? (blk + 1) * 16 * LSTR : 0;
      float x[16];
#pragma unroll
      for (int i = 0; i < 16; ++i) {
        const v4u k = a[i];
        float d = dot2(k.x, q.x, 0.f); d = dot2(k.y, q.y, d); d = dot2(k.z, q.z, d); d = dot2(k.w, q.w, d);
        x[i] = d;
        asm volatile("" : "+v"(x[i]));
        SB();
        if (i & 1) {
          PACE(NPRE * 16 + 7 + blk * 16 + i);
          a[i - 1] = ld_nt(rN, vo, sN + (i - 1) * LSTR);
          SB();
          WINDOW();
          a[i] = ld_nt(rN, vo, sN + i * LSTR);
          SB();
#define TRC(LO, HI, CTRL, BIT) { const float t1_ = (LO) + dpp<CTRL>(LO), t2_ = (HI) + dpp<CTRL>(HI); LO = (c & (BIT)) ? t2_ : t1_; }
          TRC(x[i - 1], x[i], XOR8, 8)
          if ((i & 3) == 3) TRC(x[(i - 3) & 15], x[i - 1], XOR7, 4)
          if ((i & 7) == 7) TRC(x[(i - 7) & 15], x[(i - 3) & 15], XOR2, 2)
          if (i == 15) TRC(x[0], x[8], XOR1, 1)
          asm volatile("" : "+v"(x[(i & 7) == 7 ? (i == 15 ? 0 : (i - 7) & 15) : (i & 3) == 3 ? (i - 3) & 15 : i - 1]));
          SB();
        }
      }
#pragma unroll
      for (int i = 0; i < 7; ++i) sc[i] = sc[i + 1];
      sc[7] = rbf(rbf(x[0]) * 0.08838834764831845f);
      BLOCK_END(blk + 1);
    }
    float m = sc[0];
#pragma unroll
    for (int i = 1; i < 8; ++i) m = fmaxf(m, sc[i]);
    m = max32x(max16x(rowmax(m)));
    float l = 0.f;
    unsigned pp[8];
#pragma unroll
    for (int i = 0; i < 8; ++i) { const float p = ex2((sc[i] - m) * LOG2E); l += p; pp[i] = cvtpk(p, p); }
    const unsigned hm = (c & 1) ? 0xffff0000u : 0x0000ffffu;
    const int mt = (c & 7) >> 1;
    const v4u am = {mt == 0 ? hm : 0u, mt == 1 ? hm : 0u, mt == 2 ? hm : 0u, mt == 3 ? hm : 0u};
    const v4u pm = {c < 8 ? am.x : 0u, c < 8 ? am.y : 0u, c < 8 ? am.z : 0u, c < 8 ? am.w : 0u};
    f32x4 acc = {0.f, 0.f, 0.f, 0.f};
#define REV4(I) ((((I) & 1) << 3) | (((I) & 2) << 1) | (((I) & 4) >> 1) | (((I) & 8) >> 3))
#define PVM(I, P, V) { acc = mfma(afrag(dppu<BCAST + REV4(I)>(P), pm), (V), acc); asm volatile("" : "+v"(acc)); SB(); }
#define PV2(I) { PVM(I, pp[0], a[I]) PVM((I) + 1, pp[0], a[(I) + 1]) PACE(NPRE * 16 + 136 + blk * 16 + (I)); \
      a[I] = ld_nt(rV, vo, sN + (I) * LSTR); SB(); WINDOW(); a[(I) + 1] = ld_nt(rV, vo, sN + ((I) + 1) * LSTR); SB(); \
      if (blk == 0) { PVM(I, pq0, pre[I]) PVM((I) + 1, pq0, pre[(I) + 1]) } else if (blk == 1) { PVM(I, pq1, pre[16 + (I)]) PVM((I) + 1, pq1, pre[17 + (I)]) } }
    const unsigned pq0 = pp[NVB], pq1 = pp[NVB + 1];
#pragma unroll 1
    for (int blk = 0; blk < NVB - 1; ++blk) {
      const unsigned sN = (blk + 1) * 16 * LSTR;
      PV2(0)
      if (blk == 0) {
        l = sum32x(sum16x(rowsum(l)));
        if (lane == 0) { Lf[L_ML + 2 * sw] = m; Lf[L_ML + 2 * sw + 1] = l; }
        lds_signal(L + L_FL + F_KD + sw, lane);
        SB();
      }
      PV2(2) PV2(4) PV2(6) PV2(8) PV2(10) PV2(12) PV2(14)
#pragma unroll
      for (int i = 0; i < NVB - 1; ++i) pp[i] = pp[i + 1];
      BLOCK_END(blk + 9);
    }
    const int hp = 4 * (h & 7) + (h >> 3);
    v4u wz[9], wy[8];
    const rsrc_t rD = mk(wd + (size_t)(144 * hp + 36 * sw) * 1152 + j * 144, 36 * 1152 - j * 144);
    const rsrc_t rO = mk(wo + (size_t)(128 * h + 32 * sw) * 1152 + j * 144, 32 * 1152 - j * 144);
    const unsigned to = (c < 9 ? (unsigned)(c * 16) : OOB) + g * 1152;
    const rsrc_t rW = mk(ws, WS_USED);
    lds_wait(L + L_FL + F_FW);
    const unsigned T = __builtin_amdgcn_readfirstlane(L[L_FL + F_T]);
    float fw = Lf[L_FW + sw];
    unsigned hx[9];
#pragma unroll
    for (int i = 0; i < 9; ++i) hx[i] = L[L_HD + (c >> 3) * HD_S + 36 * sw + g + 4 * i];
    const unsigned src = (c >> 3) ? 0x0302u : 0x0100u, sel = (c & 1) ? (src << 16) | 0x0c0cu : 0x0c0c0000u | src;
    v4u sl = {mt == 0 ? sel : 0x0c0c0c0cu, mt == 1 ? sel : 0x0c0c0c0cu, mt == 2 ? sel : 0x0c0c0c0cu, mt == 3 ? sel : 0x0c0c0c0cu};
    unsigned a_ow = (L_OW + sw * 128 + c * 8 + g * 4) * 4, a_or = (L_OW + sw * 32 + (c & 7) * 4 + (c >> 3) * 2) * 4, a_og = g == 0 ? c * 16 : OOB;
    const int yk = (sw & 1) * 16 + (lane >> 2), ykb = yk >= 9 ? 1 : 0;
    unsigned a_yr = (L_YW + ((ykb * 2 + ((lane & 3) >> 1)) * 16 + yk - ykb * 9) * 4 + (lane & 1) * 2) * 4, a_yg = (sw == 0 || lane < 8) ? lane * 16 : OOB;
    const int oo = (h >= 9 ? 1152 - 144 : 0) + 144 * j + 16 * h + (lane & 3) * 4;
    unsigned xres = 0;
    if (sw == 2 && h < 18) xres = *(const GAS unsigned*)(xg + oo);
    asm volatile("" : "+v"(fw), "+v"(sl), "+v"(a_ow), "+v"(a_or), "+v"(a_og), "+v"(a_yr), "+v"(a_yg));
    asm volatile("" : "+v"(hx[0]), "+v"(hx[1]), "+v"(hx[2]), "+v"(hx[3]), "+v"(hx[4]), "+v"(hx[5]), "+v"(hx[6]), "+v"(hx[7]), "+v"(hx[8]));
    SB();
    PVM(0, pp[0], a[0]) PVM(1, pp[0], a[1]) PVM(2, pp[0], a[2]) PVM(3, pp[0], a[3]) PVM(4, pp[0], a[4]) PVM(5, pp[0], a[5]) PVM(6, pp[0], a[6])
    PVM(7, pp[0], a[7]) PVM(8, pp[0], a[8]) PVM(9, pp[0], a[9]) PVM(10, pp[0], a[10]) PVM(11, pp[0], a[11]) PVM(12, pp[0], a[12]) PVM(13, pp[0], a[13])
#pragma unroll
    for (int i = 0; i < 3; ++i) wz[i] = ld_nt(rD, to, i * 4608);
    SB();
    PVM(14, pp[0], a[14])
#pragma unroll
    for (int i = 3; i < 6; ++i) wz[i] = ld_nt(rD, to, i * 4608);
    SB();
    PVM(15, pp[0], a[15])
    if (lane < 32) *(LAS f32x4*)((LAS char*)L + a_ow) = acc * fw;
    const unsigned tvd = now();
    LDS_BARRIER();
    {
      f32x2 t = *(LAS f32x2*)((LAS char*)L + a_or);
#pragma unroll
      for (int i = 1; i < 4; ++i) t += *(LAS f32x2*)((LAS char*)L + a_or + i * 512);
      st_sc(rW, a_og, OG_OFF + h * 8192 + sw * 2048 + j * 256, gran(t.x, t.y, T));
      SB();
    }
#pragma unroll
    for (int i = 6; i < 9; ++i) wz[i] = ld_nt(rD, to, i * 4608);
#pragma unroll
    for (int i = 0; i < 8; ++i) wy[i] = ld_nt(rO, to, i * 4608);
    SB();
    f32x4 y = gemv<9>(f32x4{0.f, 0.f, 0.f, 0.f}, hx, am, wz);
    asm volatile("" : "+v"(y));
    v4u p0, p1;
    poll2<1024>(rW, lane * 16, OG_OFF + h * 8192 + sw * 2048, T, p0, p1);
    {
      const float e = add32(add16(u2f(p0.x), u2f(p0.z)), add16(u2f(p1.x), u2f(p1.z)));
      auto eb = __builtin_amdgcn_permlane32_swap(f2u(e), f2u(e), false, false);
      unsigned pk = cvtpk(u2f(eb[0]), u2f(eb[1]));
      pk = __builtin_amdgcn_update_dpp(pk, pk, XOR8, 0xc, 0xf, false);
      f32x4 y1 = {0.f, 0.f, 0.f, 0.f};
#define YM(I, ACC) { const unsigned bc = dppu<BCAST + (I)>(pk); ACC = mfma(v4u{__builtin_amdgcn_perm(0u, bc, sl.x), __builtin_amdgcn_perm(0u, bc, sl.y), \
        __builtin_amdgcn_perm(0u, bc, sl.z), __builtin_amdgcn_perm(0u, bc, sl.w)}, wy[I], ACC); }
      YM(0, y) YM(1, y1) YM(2, y) YM(3, y1) YM(4, y) YM(5, y1) YM(6, y) YM(7, y1)
      y += y1;
    }
    *(LAS f32x4*)(Lf + L_YW + sw * 256 + lane * 4) = y;
    LDS_BARRIER();
    if (sw < 2) {
      f32x2 t = *(LAS f32x2*)((LAS char*)L + a_yr);
#pragma unroll
      for (int i = 1; i < 4; ++i) t += *(LAS f32x2*)((LAS char*)L + a_yr + i * 1024);
      st_sc(rW, a_yg, YG_OFF + bid * 1152 + sw * 1024, gran(t.x, t.y, T));
    } else if (sw == 2 && h < 18) {
      poll2<16 * 1152>(rW, (lane >> 2) * 1152 + (lane & 3) * 16, YG_OFF + j * 32 * 1152 + h * 64, T, p0, p1);
      float y0 = u2f(p0.x) + u2f(p1.x), y1 = u2f(p0.z) + u2f(p1.z);
      y0 += dpp<ROR4>(y0); y1 += dpp<ROR4>(y1);
      y0 += dpp<XOR8>(y0); y1 += dpp<XOR8>(y1);
      y0 = sum32x(add16(y0, y1));
      auto yy = __builtin_amdgcn_permlane16_swap(f2u(y0), f2u(y0), false, false);
      const unsigned res = cvtpk(u2f(xres << 16) + u2f(yy[0]), u2f(xres & 0xffff0000u) + u2f(yy[1]));
      __builtin_amdgcn_raw_buffer_store_b32(res, mk(out, 2304), lane < 4 ? (unsigned)oo : OOB, 0, 0);
      if (bid == 0 && lane == 0) __builtin_amdgcn_raw_buffer_store_b32(T, rW, E_OFF, 0, AUX_SC1);
      VMWAIT(0);
      const unsigned dd = now() - tvd;
      if (bid == PB) while (now() - tst < 8000u + 10u * dd) __builtin_amdgcn_s_sleep(2);
    }
    return;
  }

  const bool isA = wave == 4;
  const int g = lane >> 4, c = lane & 15;
  const int hp = 4 * (h & 7) + (h >> 3);
  const rsrc_t rW = mk(ws, WS_USED);
  v4u w[36];
  unsigned E = 0, ta = 0;
  const int u = (r & 15) + 16 * (xc & 3), qq = (r >> 4) + 2 * (xc >> 2);
  const int gm = j & 1, half = (j >> 1) & 1, rh = j >> 2;
  const unsigned stride = isA ? 8192 : 9216, cofs = isA ? u * 128 : hp * 288 + half * 144;
  int row0 = isA ? 144 * qq : 288 * rh;
  const rsrc_t rG = mk((isA ? wq : gm ? wu : wg) + cofs, 576 * stride - cofs);
  const unsigned gvo = (c < (isA ? 8 : 9) ? (unsigned)(c * 16) : OOB) + g * stride;
  if (isA) {
    __builtin_amdgcn_s_setprio(3);
    ta = now();
    E = __builtin_amdgcn_raw_buffer_load_b32(rW, E_OFF, 0, AUX_SC1);
  } else {
    __builtin_amdgcn_s_setprio(2);
    v3u xr[3];
    const rsrc_t rX = mk(xg, 2304);
#pragma unroll
    for (int k = 0; k < 3; ++k) xr[k] = __builtin_amdgcn_raw_buffer_load_b96(rX, (lane >> 5) * 1152 + (lane & 31) * 12, k * 384, 0);
    float xv[18];
#pragma unroll
    for (int k = 0; k < 3; ++k)
#pragma unroll
      for (int e = 0; e < 3; ++e) { xv[k * 6 + 2 * e] = u2f(xr[k][e] << 16); xv[k * 6 + 2 * e + 1] = u2f(xr[k][e] & 0xffff0000u); }
    float sum = 0.f;
#pragma unroll
    for (int i = 0; i < 18; ++i) sum += xv[i];
    const float mean = sum16x(rowsum(sum)) * (1.0f / 576.0f);
    float sq = 0.f;
#pragma unroll
    for (int i = 0; i < 18; ++i) { const float d = xv[i] - mean; sq = fmaf(d, d, sq); }
    const float var = sum16x(rowsum(sq)) * (1.0f / 576.0f);
    const float mb = rbf(mean), sd = rbf(__builtin_sqrtf(rbf(rbf(var) + 1e-5f))), rs = 1.0f / sd;
    LAS v2u* dst = (LAS v2u*)(L + L_XN + (lane >> 5) * XN_S + (lane & 31) * 6);
#pragma unroll
    for (int k = 0; k < 3; ++k)
#pragma unroll
      for (int e = 0; e < 3; ++e) {
        float n[2];
#pragma unroll
        for (int f = 0; f < 2; ++f) { const float d = rbf(xv[k * 6 + 2 * e + f] - mb); const float q0 = d * rs; n[f] = fmaf(fmaf(-q0, sd, d), rs, q0); }
        dst[k * 96 + e] = v2u{cvtpk(n[0], n[0]), cvtpk(n[1], n[1])};
      }
    lds_signal(L + L_FL + F_XN, lane);
    __builtin_amdgcn_s_setprio(0);
  }
  const unsigned hm = (c & 1) ? 0xffff0000u : 0x0000ffffu;
  const int mt = (c & 7) >> 1;
  const v4u am = {mt == 0 ? hm : 0u, mt == 1 ? hm : 0u, mt == 2 ? hm : 0u, mt == 3 ? hm : 0u};
  unsigned T = 0;
  f32x4 ga = {0.f, 0.f, 0.f, 0.f};
#pragma unroll 1
  for (int t = 0; t < (isA ? 1 : 2); ++t, row0 += 144) {
    unsigned so = row0 * stride;
#pragma unroll
    for (int i = 0; i < 36; ++i) { w[i] = ld_nt(rG, gvo, so); so += 4 * stride; }
    SB();
    if (isA) {
      T = __builtin_amdgcn_readfirstlane(E) + 0x9E3779B9u;
      if (lane == 0) L[L_FL + F_T] = T;
      lds_wait(L + L_FL + F_XN);
    }
    asm volatile("" :: "v"(cv));
    unsigned xv[36];
#pragma unroll
    for (int i = 0; i < 36; ++i) xv[i] = L[L_XN + (c >> 3) * XN_S + row0 + g + 4 * i];
    SB();
    ga = gemv<36>(ga, xv, am, w);
  }
  if (isA) {
    const float s2 = dpp<XOR8>(ga[2]), s3 = dpp<XOR8>(ga[3]);
    const unsigned o = QG_OFF + (((u >> 1) * 2 + (g >> 1)) * 4 + qq) * 1024 + ((u & 1) * 64 + (c & 7) * 8 + (g & 1) * 4 + (c >> 3) * 2) * 8;
    st_sc(rW, o, 0, gran(c < 8 ? ga[0] : s2, c < 8 ? ga[1] : s3, T));
    __builtin_amdgcn_s_sleep(32);
    float q0, q1;
#pragma unroll 1
    for (int spin = 0; spin < SPIN_CAP; ++spin) {
      MEMBAR();
#pragma unroll
      for (int i = 0; i < 4; ++i) w[i] = ld_sc(rW, lane * 16, QG_OFF + ((h * 2 + b) * 4 + i) * 1024);
      q0 = (u2f(w[0].x) + u2f(w[1].x)) + (u2f(w[2].x) + u2f(w[3].x));
      q1 = (u2f(w[0].z) + u2f(w[1].z)) + (u2f(w[2].z) + u2f(w[3].z));
      if (none_bad(tagbad(w[0], T) | tagbad(w[1], T) | tagbad(w[2], T) | tagbad(w[3], T))) break;
      __builtin_amdgcn_s_sleep(16);
    }
    L[L_QB + lane] = cvtpk(q0, q1);
    if (lane == 0) L[L_FL + F_TK] = ta + LAG0;
    lds_signal(L + L_FL + F_Q, lane);
    __builtin_amdgcn_s_setprio(0);
#pragma unroll 1
    for (int k = 0; k < 2; ++k) {
      while (__builtin_amdgcn_readfirstlane(lds_ld(L + L_FL + F_PB)) < 1u + 6u * k) __builtin_amdgcn_s_sleep(8);
      MEMBAR();
      const unsigned ps = lds_ld(L + L_FL + F_PS);
      if (bid < 8) st_sc(rW, lane == 0 ? RT_OFF + k * 256 + bid * 16 : OOB, 0, v4u{ps, T, ps, T});
#pragma unroll 1
      for (int t = 0; t < 3; ++t) {
        __builtin_amdgcn_s_sleep(64);
        MEMBAR();
        const v4u v = ld_sc(rW, (lane & 7) * 16, RT_OFF + k * 256);
        if (!none_bad(tagbad(v, T))) continue;
        int d = (int)(v.x - ps);
        d += (int)dppu<XOR1>(d); d += (int)dppu<XOR2>(d); d += (int)dppu<XOR7>(d);
        if (lane == 0) lds_st(L + L_FL + F_TK, ps + min(max(d >> 3, -500), 500) - LAGC);
        break;
      }
    }
  } else {
    st_sc(rW, c < 9 ? (unsigned)((g >> 1) * 288 + c * 32 + (g & 1) * 16) : OOB, GU_OFF + bid * 640, __builtin_bit_cast(v4u, ga));
    lds_wait4(L + L_FL + F_KD, lane);
    T = __builtin_amdgcn_readfirstlane(L[L_FL + F_T]);
    float mw[4], lw[4];
#pragma unroll
    for (int i = 0; i < 4; ++i) { mw[i] = Lf[L_ML + 2 * i]; lw[i] = Lf[L_ML + 2 * i + 1]; }
    const float mg = fmaxf(fmaxf(mw[0], mw[1]), fmaxf(mw[2], mw[3]));
    float lg = 0.f;
#pragma unroll
    for (int i = 0; i < 4; ++i) lg += lw[i] * ex2((mw[i] - mg) * LOG2E);
    VMWAIT(0);
    st_sc(rW, lane == 0 ? ML_OFF + h * 128 + j * 16 : OOB, 0, gran(mg, lg, T));
    v4u v;
#pragma unroll 1
    for (int spin = 0; spin < SPIN_CAP; ++spin) {
      __builtin_amdgcn_s_sleep(80);
      MEMBAR();
      v = ld_sc(rW, ((lane & 7) ^ (b * 4)) * 16, ML_OFF + h * 128);
      if (none_bad(tagbad(v, T))) break;
    }
    MEMBAR();
#pragma unroll
    for (int i = 0; i < 8; ++i) w[i] = ld_sc(rW, lane < 36 ? (unsigned)(lane * 16) : OOB, GU_OFF + (i * 32 + h) * 640);
#pragma unroll
    for (int hf = 0; hf < 2; ++hf) {
      v4u hv;
#pragma unroll
      for (int e = 0; e < 4; ++e) {
        const float gt = rbf(u2f(w[2 * hf][e]) + u2f(w[4 + 2 * hf][e])), up = rbf(u2f(w[1 + 2 * hf][e]) + u2f(w[5 + 2 * hf][e]));
        const float t2 = 1.5957691216057308f * (gt + 0.044715f * gt * gt * gt);
        const float hh = rbf(gt / (1.0f + ex2(-t2 * LOG2E))) * up;
        hv[e] = cvtpk(hh, hh);
      }
      if (lane < 36) *(LAS v4u*)(L + L_HD + (lane >= 18 ? HD_S - 72 : 0) + hf * 72 + lane * 4) = hv;
    }
    const float ms = u2f(v.x);
    float M = fmaxf(ms, dpp<XOR1>(ms)); M = fmaxf(M, dpp<XOR2>(M));
    float Ls = u2f(v.z) * ex2((ms - M) * LOG2E);
    Ls += dpp<XOR1>(Ls); Ls += dpp<XOR2>(Ls);
    const float inv = 1.0f / Ls;
    if (lane == 0) *(LAS f32x4*)(Lf + L_FW) = f32x4{ex2((mw[0] - M) * LOG2E) * inv, ex2((mw[1] - M) * LOG2E) * inv, ex2((mw[2] - M) * LOG2E) * inv, ex2((mw[3] - M) * LOG2E) * inv};
    lds_signal(L + L_FL + F_FW, lane);
    {
      const char* tp = (lane & 4) ? wo + (size_t)(128 * h + 42 * (lane & 3)) * 1152 + j * 144 : wd + (size_t)(144 * hp + 47 * (lane & 3)) * 1152 + j * 144;
      if (lane & 8) tp = out;
      unsigned t2 = 0;
      if (lane < 9) t2 = __builtin_nontemporal_load((const GAS unsigned*)tp);
      asm volatile("" :: "v"(t2));
    }
    const unsigned o = lane < 8 ? OG_OFF + h * 8192 + (lane >> 1) * 2048 + j * 256 + (lane & 1) * 128 : lane < 17 ? YG_OFF + bid * 1152 + (lane - 8) * 128 : OOB;
    const unsigned t = __builtin_amdgcn_raw_buffer_load_b32(rW, o, 0, AUX_SC1);
    asm volatile("" :: "v"(t));
  }
}

extern "C" void kernel_launch(void* const* d_in, const int* in_sizes, int n_in, void* d_out, int out_size, void* d_ws, size_t ws_size,
                              hipStream_t stream) {
  fused_decode<<<256, 384, 0, stream>>>((const char*)d_in[0], (const char*)d_in[1], (const char*)d_in[2], (const char*)d_in[3],
                                        (const char*)d_in[4], (const char*)d_in[6], (const char*)d_in[7], (const char*)d_in[8],
                                        (char*)d_out, (char*)d_ws);
}
```

```cpp
#include <hip/hip_runtime.h>
#include <hip/hip_bf16.h>

#define GAS __attribute__((address_space(1)))
#define LAS __attribute__((address_space(3)))
typedef unsigned v4u __attribute__((ext_vector_type(4)));
typedef unsigned v3u __attribute__((ext_vector_type(3)));
typedef unsigned v2u __attribute__((ext_vector_type(2)));
typedef float f32x4 __attribute__((ext_vector_type(4)));
typedef float f32x2 __attribute__((ext_vector_type(2)));
typedef __bf16 bf16x8 __attribute__((ext_vector_type(8)));
typedef __bf16 bf16x2 __attribute__((ext_vector_type(2)));
typedef unsigned long long v8q __attribute__((ext_vector_type(8)));
typedef unsigned long long v2q __attribute__((ext_vector_type(2)));
typedef __amdgpu_buffer_rsrc_t rsrc_t;

constexpr int LSTR = 16384;
constexpr int NPRE = 2;
constexpr int NVB = 8 - NPRE;
constexpr int PINFL = 6;
constexpr int TAU = 217;
constexpr int LAG0 = 150, LAGC = 140;

constexpr int E_OFF = 0;
constexpr int RT_OFF = 1024;
constexpr int QG_OFF = 4096;
constexpr int ML_OFF = QG_OFF + 262144;
constexpr int GU_OFF = ML_OFF + 4096;
constexpr int OG_OFF = GU_OFF + 163840;
constexpr int YG_OFF = OG_OFF + 262144;
constexpr int WS_USED = YG_OFF + 294912;

constexpr int L_XN = 0, XN_S = 580;
constexpr int L_HD = 1160, HD_S = 148;
constexpr int L_QB = L_HD + 2 * HD_S;
constexpr int L_OW = L_QB + 64;
constexpr int L_YW = L_OW + 512;
constexpr int L_ML = L_YW + 1056;
constexpr int L_FW = L_ML + 8;
constexpr int L_FL = L_FW + 4;
constexpr int F_KD = 0, F_Q = 4, F_T = 5, F_XN = 7, F_FW = 8, F_TK = 9, F_PB = 10, F_PS = 11;
constexpr int L_END = L_FL + 12;
static_assert(L_FL % 4 == 0 && L_OW % 4 == 0 && L_YW % 4 == 0 && L_QB % 4 == 0 && L_FW % 4 == 0 && L_HD % 4 == 0, "16-byte aligned LDS pieces");

constexpr int AUX_NT = 2, AUX_SC1 = 16;
constexpr unsigned OOB = 0x80000000u;
constexpr unsigned CODE_BYTES = 20352;
constexpr int SPIN_CAP = 1 << 18;
constexpr float LOG2E = 1.4426950408889634f;

__device__ __forceinline__ rsrc_t mk(const void* p, unsigned bytes) { return __builtin_amdgcn_make_buffer_rsrc((void*)p, 0, bytes, 0x00020000); }
__device__ __forceinline__ v4u ld_nt(rsrc_t r, unsigned vo, unsigned so) { return __builtin_amdgcn_raw_buffer_load_b128(r, vo, so, AUX_NT); }
__device__ __forceinline__ v4u ld_sc(rsrc_t r, unsigned vo, unsigned so) { return __builtin_amdgcn_raw_buffer_load_b128(r, vo, so, AUX_SC1); }
__device__ __forceinline__ void st_sc(rsrc_t r, unsigned vo, unsigned so, v4u v) { __builtin_amdgcn_raw_buffer_store_b128(v, r, vo, so, AUX_SC1); }
__device__ __forceinline__ float u2f(unsigned u) { return __builtin_bit_cast(float, u); }
__device__ __forceinline__ unsigned f2u(float f) { return __builtin_bit_cast(unsigned, f); }
__device__ __forceinline__ unsigned cvtpk(float lo, float hi) {
  bf16x2 r = {(__bf16)lo, (__bf16)hi};
  return __builtin_bit_cast(unsigned, r);
}
__device__ __forceinline__ float rbf(float f) { return (float)(__bf16)f; }
__device__ __forceinline__ float ex2(float f) { return __builtin_amdgcn_exp2f(f); }
__device__ __forceinline__ float dot2(unsigned a, unsigned b, float c) {
  return __builtin_amdgcn_fdot2_f32_bf16(__builtin_bit_cast(bf16x2, a), __builtin_bit_cast(bf16x2, b), c, false);
}
__device__ __forceinline__ f32x4 mfma(v4u a, v4u b, f32x4 c) {
  return __builtin_amdgcn_mfma_f32_16x16x32_bf16(__builtin_bit_cast(bf16x8, a), __builtin_bit_cast(bf16x8, b), c, 0, 0, 0);
}
template <int CTRL> __device__ __forceinline__ float dpp(float x) { return u2f(__builtin_amdgcn_mov_dpp(f2u(x), CTRL, 0xf, 0xf, true)); }
template <int CTRL> __device__ __forceinline__ unsigned dppu(unsigned x) { return __builtin_amdgcn_mov_dpp(x, CTRL, 0xf, 0xf, true); }
constexpr int XOR1 = 0xB1, XOR2 = 0x4E, XOR7 = 0x141, XOR8 = 0x128, ROR4 = 0x124, BCAST = 0x150;
__device__ __forceinline__ float add16(float a, float b) {
  auto s = __builtin_amdgcn_permlane16_swap(f2u(a), f2u(b), false, false); return u2f(s[0]) + u2f(s[1]); }
__device__ __forceinline__ float add32(float a, float b) {
  auto s = __builtin_amdgcn_permlane32_swap(f2u(a), f2u(b), false, false); return u2f(s[0]) + u2f(s[1]); }
__device__ __forceinline__ float sum16x(float x) { return add16(x, x); }
__device__ __forceinline__ float sum32x(float x) { return add32(x, x); }
__device__ __forceinline__ float max16x(float x) { auto s = __builtin_amdgcn_permlane16_swap(f2u(x), f2u(x), false, false); return fmaxf(u2f(s[0]), u2f(s[1])); }
__device__ __forceinline__ float max32x(float x) { auto s = __builtin_amdgcn_permlane32_swap(f2u(x), f2u(x), false, false); return fmaxf(u2f(s[0]), u2f(s[1])); }
__device__ __forceinline__ float rowsum(float x) { x += dpp<XOR1>(x); x += dpp<XOR2>(x); x += dpp<XOR7>(x); x += dpp<XOR8>(x); return x; }
__device__ __forceinline__ float rowmax(float x) { x = fmaxf(x, dpp<XOR1>(x)); x = fmaxf(x, dpp<XOR2>(x)); x = fmaxf(x, dpp<XOR7>(x)); x = fmaxf(x, dpp<XOR8>(x)); return x; }
#define SB() __builtin_amdgcn_sched_barrier(0)
#define MEMBAR() asm volatile("" ::: "memory")
#define LDS_FENCE() asm volatile("s_waitcnt lgkmcnt(0)" ::: "memory")
#define LDS_BARRIER() do { SB(); asm volatile("s_waitcnt lgkmcnt(0)\n\ts_barrier" ::: "memory"); SB(); } while (0)
#define VMWAIT(n) asm volatile("s_waitcnt vmcnt(%0)" :: "i"(n) : "memory")
__device__ __forceinline__ unsigned now() { return (unsigned)__builtin_amdgcn_s_memrealtime(); }
__device__ __forceinline__ unsigned lds_ld(LAS unsigned* p) { return __hip_atomic_load(p, __ATOMIC_RELAXED, __HIP_MEMORY_SCOPE_WORKGROUP); }
__device__ __forceinline__ void lds_st(LAS unsigned* p, unsigned v) { __hip_atomic_store(p, v, __ATOMIC_RELAXED, __HIP_MEMORY_SCOPE_WORKGROUP); }
__device__ __forceinline__ void lds_wait(LAS unsigned* p) {
  while (__builtin_amdgcn_readfirstlane(lds_ld(p)) == 0) __builtin_amdgcn_s_sleep(1);
  MEMBAR();
}
__device__ __forceinline__ void lds_wait4(LAS unsigned* p, int lane) {
  while (__builtin_amdgcn_ballot_w64(lds_ld(p + (lane & 3)) == 0) != 0ull) __builtin_amdgcn_s_sleep(4);
  MEMBAR();
}
__device__ __forceinline__ void lds_signal(LAS unsigned* p, int lane) {
  LDS_FENCE();
  if (lane == 0) lds_st(p, 1u);
}
__device__ __forceinline__ bool none_bad(unsigned bad) { return __builtin_amdgcn_ballot_w64(bad != 0u) == 0ull; }
__device__ __forceinline__ unsigned tagbad(const v4u& v, unsigned T) { return (v.y ^ T) | (v.w ^ T); }
__device__ __forceinline__ v4u gran(float a, float b, unsigned T) { return v4u{f2u(a), T, f2u(b), T}; }
__device__ __forceinline__ v4u afrag(unsigned dup, const v4u& m) { return v4u{dup & m.x, dup & m.y, dup & m.z, dup & m.w}; }
template <int D> __device__ __forceinline__ void poll2(rsrc_t rW, unsigned vo, unsigned so, unsigned T, v4u& r0, v4u& r1) {
#pragma unroll 1
  for (int spin = 0; spin < SPIN_CAP; ++spin) {
    MEMBAR();
    r0 = ld_sc(rW, vo, so); r1 = ld_sc(rW, vo, so + D);
    if (none_bad(tagbad(r0, T) | tagbad(r1, T))) break;
  }
}

#define WINDOW() asm volatile("s_cmp_ge_i32 %0, 18\n\ts_cbranch_scc1 1f\n\ts_waitcnt vmcnt(9)\n\ts_cmp_ge_i32 %0, 13\n\ts_cbranch_scc1 1f\n\ts_waitcnt vmcnt(7)\n\t" \
                              "s_cmp_ge_i32 %0, 9\n\ts_cbranch_scc1 1f\n\ts_waitcnt vmcnt(6)\n\ts_cmp_ge_i32 %0, 6\n\ts_cbranch_scc1 1f\n\ts_waitcnt vmcnt(5)\n\t" \
                              "s_cmp_ge_i32 %0, 4\n\ts_cbranch_scc1 1f\n\ts_waitcnt vmcnt(4)\n\ts_cmp_ge_i32 %0, 2\n\ts_cbranch_scc1 1f\n\ts_waitcnt vmcnt(3)\n1:" \
                              :: "s"(cls) : "scc", "memory")
#define PACE(n) do { const unsigned need_ = tk0 + (((unsigned)(n) * TAU) >> 4); \
    while ((lag = (int)(now() - need_)) < 0) __builtin_amdgcn_s_sleep(1); \
    cls = (int)(((unsigned)lag * (65536u / 25)) >> 16);     \
    WINDOW(); } while (0)
#define BLOCK_END(n) do { if (sw == 0 && lane == 0) { L[L_FL + F_PS] = tk0 + lag; MEMBAR(); lds_st(L + L_FL + F_PB, (unsigned)(n)); } \
    tk0 = __builtin_amdgcn_readfirstlane(lds_ld(L + L_FL + F_TK)); } while (0)

template <int N, int M> __device__ __forceinline__ f32x4 gemv(f32x4 a0, const unsigned (&xv)[N], const v4u& am, const v4u (&w)[M]) {
  f32x4 a1 = {0.f, 0.f, 0.f, 0.f};
#pragma unroll
  for (int i = 0; i < N; ++i) { if (i & 1) a1 = mfma(afrag(xv[i], am), w[i], a1); else a0 = mfma(afrag(xv[i], am), w[i], a0); }
  return a0 + a1;
}

__global__ void __launch_bounds__(384, 2)
fused_decode(const char* xg_, const char* kc_, const char* vc_, const char* wq_, const char* wo_, const char* wg_, const char* wu_, const char* wd_,
             char* out_, char* ws_) {
  v8q a8; v2q a2;
  asm volatile("s_load_dwordx16 %0, %2, 0x0\n\ts_load_dwordx4 %1, %2, 0x40" : "=&s"(a8), "=&s"(a2) : "s"(__builtin_amdgcn_kernarg_segment_ptr()));
  unsigned long long pc0;
  asm volatile("s_getpc_b64 %0" : "=s"(pc0));
  SB();
  __shared__ __attribute__((aligned(16))) unsigned smem[L_END];
  LAS unsigned* L = (LAS unsigned*)smem;
  LAS float* Lf = (LAS float*)smem;
  const int bid = blockIdx.x, xc = bid & 7, r = bid >> 3;
  int lane = threadIdx.x & 63;
  const int wave = __builtin_amdgcn_readfirstlane(threadIdx.x >> 6);
  const unsigned cv = __builtin_amdgcn_raw_buffer_load_b32(mk((const void*)(pc0 & ~255ull), CODE_BYTES), 0, min((unsigned)(r * 6 + wave) * 128u, CODE_BYTES - 128u), 0);
  const int h = bid & 31, j = bid >> 5, s = j & 3, b = j >> 2;
  unsigned cb = (b * 32 + h) * 2097152u + s * 1024u;
  unsigned vo = wave * 4096 + lane * 16;
  if (lane < 12) L[L_FL + lane] = 0u;
  asm volatile("" : "+v"(vo));
  SB();
  asm volatile("s_waitcnt lgkmcnt(0)\n\ts_barrier" : "+s"(a8), "+s"(a2), "+s"(cb) :: "memory");
  SB();
  const char* xg = (const char*)a8[0]; const char* kc = (const char*)a8[1]; const char* vc = (const char*)a8[2]; const char* wq = (const char*)a8[3];
  const char* wo = (const char*)a8[4]; const char* wg = (const char*)a8[5]; const char* wu = (const char*)a8[6]; const char* wd = (const char*)a8[7];
  char* out = (char*)a2[0]; char* ws = (char*)a2[1];

  if (__builtin_expect(wave < 4, 1)) {
    const int sw = wave;
    const char* kb = kc + cb;
    const char* vb = vc + cb;
    const unsigned span = 2097152u - s * 1024u;
    const rsrc_t rK = mk(kb, span), rV = mk(vb, span);
    v4u a[16], pre[NPRE * 16];
#pragma unroll
    for (int i = 0; i < PINFL / 2; ++i) { a[i] = ld_nt(rK, vo, i * LSTR); pre[i] = ld_nt(rV, vo, (NVB * 16 + i) * LSTR); }
    SB();
    asm volatile("" : "+v"(lane));
#pragma unroll
    for (int i = PINFL / 2; i < NPRE * 16; ++i) { VMWAIT(PINFL - 1); pre[i] = ld_nt(rV, vo, (NVB * 16 + i) * LSTR); SB(); }
#pragma unroll
    for (int i = PINFL / 2; i < 16; ++i) { VMWAIT(PINFL - 1); a[i] = ld_nt(rK, vo, i * LSTR); SB(); }
    const int g = lane >> 4, c = lane & 15;
    lds_wait(L + L_FL + F_Q);
    asm volatile("" :: "v"(cv));
    const v4u q = *(LAS v4u*)(L + L_QB + c * 4);
    unsigned tk0 = __builtin_amdgcn_readfirstlane(L[L_FL + F_TK]);
    int lag = 0, cls = 0;
    float sc[8];
#pragma unroll
    for (int i = 0; i < 8; ++i) sc[i] = 0.f;
#pragma unroll 1
    for (int blk = 0; blk < 8; ++blk) {
      const rsrc_t rN = mk(blk < 7 ? kb : vb, span);
      const unsigned sN = blk < 7 ? (blk + 1) * 16 * LSTR : 0;
      float x[16];
#pragma unroll
      for (int i = 0; i < 16; ++i) {
        const v4u k = a[i];
        float d = dot2(k.x, q.x, 0.f); d = dot2(k.y, q.y, d); d = dot2(k.z, q.z, d); d = dot2(k.w, q.w, d);
        x[i] = d;
        asm volatile("" : "+v"(x[i]));
        SB();
        if (i & 1) {
          PACE(NPRE * 16 + 7 + blk * 16 + i);
          a[i - 1] = ld_nt(rN, vo, sN + (i - 1) * LSTR);
          SB();
          WINDOW();
          a[i] = ld_nt(rN, vo, sN + i * LSTR);
          SB();
#define TRC(LO, HI, CTRL, BIT) { const float t1_ = (LO) + dpp<CTRL>(LO), t2_ = (HI) + dpp<CTRL>(HI); LO = (c & (BIT)) ? t2_ : t1_; }
          TRC(x[i - 1], x[i], XOR8, 8)
          if ((i & 3) == 3) TRC(x[(i - 3) & 15], x[i - 1], XOR7, 4)
          if ((i & 7) == 7) TRC(x[(i - 7) & 15], x[(i - 3) & 15], XOR2, 2)
          if (i == 15) TRC(x[0], x[8], XOR1, 1)
          asm volatile("" : "+v"(x[(i & 7) == 7 ? (i == 15 ? 0 : (i - 7) & 15) : (i & 3) == 3 ? (i - 3) & 15 : i - 1]));
          SB();
        }
      }
#pragma unroll
      for (int i = 0; i < 7; ++i) sc[i] = sc[i + 1];
      sc[7] = rbf(rbf(x[0]) * 0.08838834764831845f);
      BLOCK_END(blk + 1);
    }
    float m = sc[0];
#pragma unroll
    for (int i = 1; i < 8; ++i) m = fmaxf(m, sc[i]);
    m = max32x(max16x(rowmax(m)));
    float l = 0.f;
    unsigned pp[8];
#pragma unroll
    for (int i = 0; i < 8; ++i) { const float p = ex2((sc[i] - m) * LOG2E); l += p; pp[i] = cvtpk(p, p); }
    const unsigned hm = (c & 1) ? 0xffff0000u : 0x0000ffffu;
    const int mt = (c & 7) >> 1;
    const v4u am = {mt == 0 ? hm : 0u, mt == 1 ? hm : 0u, mt == 2 ? hm : 0u, mt == 3 ? hm : 0u};
    const v4u pm = {c < 8 ? am.x : 0u, c < 8 ? am.y : 0u, c < 8 ? am.z : 0u, c < 8 ? am.w : 0u};
    f32x4 acc = {0.f, 0.f, 0.f, 0.f};
#define REV4(I) ((((I) & 1) << 3) | (((I) & 2) << 1) | (((I) & 4) >> 1) | (((I) & 8) >> 3))
#define PVM(I, P, V) { acc = mfma(afrag(dppu<BCAST + REV4(I)>(P), pm), (V), acc); asm volatile("" : "+v"(acc)); SB(); }
#define PV2(I) { PVM(I, pp[0], a[I]) PVM((I) + 1, pp[0], a[(I) + 1]) PACE(NPRE * 16 + 136 + blk * 16 + (I)); \
      a[I] = ld_nt(rV, vo, sN + (I) * LSTR); SB(); WINDOW(); a[(I) + 1] = ld_nt(rV, vo, sN + ((I) + 1) * LSTR); SB(); \
      if (blk == 0) { PVM(I, pq0, pre[I]) PVM((I) + 1, pq0, pre[(I) + 1]) } else if (blk == 1) { PVM(I, pq1, pre[16 + (I)]) PVM((I) + 1, pq1, pre[17 + (I)]) } }
    const unsigned pq0 = pp[NVB], pq1 = pp[NVB + 1];
#pragma unroll 1
    for (int blk = 0; blk < NVB - 1; ++blk) {
      const unsigned sN = (blk + 1) * 16 * LSTR;
      PV2(0)
      if (blk == 0) {
        l = sum32x(sum16x(rowsum(l)));
        if (lane == 0) { Lf[L_ML + 2 * sw] = m; Lf[L_ML + 2 * sw + 1] = l; }
        lds_signal(L + L_FL + F_KD + sw, lane);
        SB();
      }
      PV2(2) PV2(4) PV2(6) PV2(8) PV2(10) PV2(12) PV2(14)
#pragma unroll
      for (int i = 0; i < NVB - 1; ++i) pp[i] = pp[i + 1];
      BLOCK_END(blk + 9);
    }
    const int hp = 4 * (h & 7) + (h >> 3);
    v4u wz[9], wy[8];
    const rsrc_t rD = mk(wd + (size_t)(144 * hp + 36 * sw) * 1152 + j * 144, 36 * 1152 - j * 144);
    const rsrc_t rO = mk(wo + (size_t)(128 * h + 32 * sw) * 1152 + j * 144, 32 * 1152 - j * 144);
    const unsigned to = (c < 9 ? (unsigned)(c * 16) : OOB) + g * 1152;
    const rsrc_t rW = mk(ws, WS_USED);
    lds_wait(L + L_FL + F_FW);
    const unsigned T = __builtin_amdgcn_readfirstlane(L[L_FL + F_T]);
    float fw = Lf[L_FW + sw];
    unsigned hx[9];
#pragma unroll
    for (int i = 0; i < 9; ++i) hx[i] = L[L_HD + (c >> 3) * HD_S + 36 * sw + g + 4 * i];
    const unsigned src = (c >> 3) ? 0x0302u : 0x0100u, sel = (c & 1) ? (src << 16) | 0x0c0cu : 0x0c0c0000u | src;
    v4u sl = {mt == 0 ? sel : 0x0c0c0c0cu, mt == 1 ? sel : 0x0c0c0c0cu, mt == 2 ? sel : 0x0c0c0c0cu, mt == 3 ? sel : 0x0c0c0c0cu};
    unsigned a_ow = (L_OW + sw * 128 + c * 8 + g * 4) * 4, a_or = (L_OW + sw * 32 + (c & 7) * 4 + (c >> 3) * 2) * 4, a_og = g == 0 ? c * 16 : OOB;
    const int yk = (sw & 1) * 16 + (lane >> 2), ykb = yk >= 9 ? 1 : 0;
    unsigned a_yr = (L_YW + ((ykb * 2 + ((lane & 3) >> 1)) * 16 + yk - ykb * 9) * 4 + (lane & 1) * 2) * 4, a_yg = (sw == 0 || lane < 8) ? lane * 16 : OOB;
    const int oo = (h >= 9 ? 1152 - 144 : 0) + 144 * j + 16 * h + (lane & 3) * 4;
    unsigned xres = 0;
    if (sw == 2 && h < 18) xres = *(const GAS unsigned*)(xg + oo);
    asm volatile("" : "+v"(fw), "+v"(sl), "+v"(a_ow), "+v"(a_or), "+v"(a_og), "+v"(a_yr), "+v"(a_yg));
    asm volatile("" : "+v"(hx[0]), "+v"(hx[1]), "+v"(hx[2]), "+v"(hx[3]), "+v"(hx[4]), "+v"(hx[5]), "+v"(hx[6]), "+v"(hx[7]), "+v"(hx[8]));
    SB();
    PVM(0, pp[0], a[0]) PVM(1, pp[0], a[1]) PVM(2, pp[0], a[2]) PVM(3, pp[0], a[3]) PVM(4, pp[0], a[4]) PVM(5, pp[0], a[5]) PVM(6, pp[0], a[6])
    PVM(7, pp[0], a[7]) PVM(8, pp[0], a[8]) PVM(9, pp[0], a[9]) PVM(10, pp[0], a[10]) PVM(11, pp[0], a[11]) PVM(12, pp[0], a[12]) PVM(13, pp[0], a[13])
#pragma unroll
    for (int i = 0; i < 3; ++i) wz[i] = ld_nt(rD, to, i * 4608);
    SB();
    PVM(14, pp[0], a[14])
#pragma unroll
    for (int i = 3; i < 6; ++i) wz[i] = ld_nt(rD, to, i * 4608);
    SB();
    PVM(15, pp[0], a[15])
    if (lane < 32) *(LAS f32x4*)((LAS char*)L + a_ow) = acc * fw;
    LDS_BARRIER();
    {
      f32x2 t = *(LAS f32x2*)((LAS char*)L + a_or);
#pragma unroll
      for (int i = 1; i < 4; ++i) t += *(LAS f32x2*)((LAS char*)L + a_or + i * 512);
      st_sc(rW, a_og, OG_OFF + h * 8192 + sw * 2048 + j * 256, gran(t.x, t.y, T));
      SB();
    }
#pragma unroll
    for (int i = 6; i < 9; ++i) wz[i] = ld_nt(rD, to, i * 4608);
#pragma unroll
    for (int i = 0; i < 8; ++i) wy[i] = ld_nt(rO, to, i * 4608);
    SB();
    f32x4 y = gemv<9>(f32x4{0.f, 0.f, 0.f, 0.f}, hx, am, wz);
    asm volatile("" : "+v"(y));
    v4u p0, p1;
    poll2<1024>(rW, lane * 16, OG_OFF + h * 8192 + sw * 2048, T, p0, p1);
    {
      const float e = add32(add16(u2f(p0.x), u2f(p0.z)), add16(u2f(p1.x), u2f(p1.z)));
      auto eb = __builtin_amdgcn_permlane32_swap(f2u(e), f2u(e), false, false);
      unsigned pk = cvtpk(u2f(eb[0]), u2f(eb[1]));
      pk = __builtin_amdgcn_update_dpp(pk, pk, XOR8, 0xc, 0xf, false);
      f32x4 y1 = {0.f, 0.f, 0.f, 0.f};
#define YM(I, ACC) { const unsigned bc = dppu<BCAST + (I)>(pk); ACC = mfma(v4u{__builtin_amdgcn_perm(0u, bc, sl.x), __builtin_amdgcn_perm(0u, bc, sl.y), \
        __builtin_amdgcn_perm(0u, bc, sl.z), __builtin_amdgcn_perm(0u, bc, sl.w)}, wy[I], ACC); }
      YM(0, y) YM(1, y1) YM(2, y) YM(3, y1) YM(4, y) YM(5, y1) YM(6, y) YM(7, y1)
      y += y1;
    }
    *(LAS f32x4*)(Lf + L_YW + sw * 256 + lane * 4) = y;
    LDS_BARRIER();
    if (sw < 2) {
      f32x2 t = *(LAS f32x2*)((LAS char*)L + a_yr);
#pragma unroll
      for (int i = 1; i < 4; ++i) t += *(LAS f32x2*)((LAS char*)L + a_yr + i * 1024);
      st_sc(rW, a_yg, YG_OFF + bid * 1152 + sw * 1024, gran(t.x, t.y, T));
    } else if (sw == 2 && h < 18) {
      poll2<16 * 1152>(rW, (lane >> 2) * 1152 + (lane & 3) * 16, YG_OFF + j * 32 * 1152 + h * 64, T, p0, p1);
      float y0 = u2f(p0.x) + u2f(p1.x), y1 = u2f(p0.z) + u2f(p1.z);
      y0 += dpp<ROR4>(y0); y1 += dpp<ROR4>(y1);
      y0 += dpp<XOR8>(y0); y1 += dpp<XOR8>(y1);
      y0 = sum32x(add16(y0, y1));
      auto yy = __builtin_amdgcn_permlane16_swap(f2u(y0), f2u(y0), false, false);
      const unsigned res = cvtpk(u2f(xres << 16) + u2f(yy[0]), u2f(xres & 0xffff0000u) + u2f(yy[1]));
      __builtin_amdgcn_raw_buffer_store_b32(res, mk(out, 2304), lane < 4 ? (unsigned)oo : OOB, 0, 0);
      if (bid == 0 && lane == 0) __builtin_amdgcn_raw_buffer_store_b32(T, rW, E_OFF, 0, AUX_SC1);
    }
    return;
  }

  const bool isA = wave == 4;
  const int g = lane >> 4, c = lane & 15;
  const int hp = 4 * (h & 7) + (h >> 3);
  const rsrc_t rW = mk(ws, WS_USED);
  v4u w[36];
  unsigned E = 0, ta = 0;
  const int u = (r & 15) + 16 * (xc & 3), qq = (r >> 4) + 2 * (xc >> 2);
  const int gm = j & 1, half = (j >> 1) & 1, rh = j >> 2;
  const unsigned stride = isA ? 8192 : 9216, cofs = isA ? u * 128 : hp * 288 + half * 144;
  int row0 = isA ? 144 * qq : 288 * rh;
  const rsrc_t rG = mk((isA ? wq : gm ? wu : wg) + cofs, 576 * stride - cofs);
  const unsigned gvo = (c < (isA ? 8 : 9) ? (unsigned)(c * 16) : OOB) + g * stride;
  if (isA) {
    __builtin_amdgcn_s_setprio(3);
    ta = now();
    E = __builtin_amdgcn_raw_buffer_load_b32(rW, E_OFF, 0, AUX_SC1);
  } else {
    __builtin_amdgcn_s_setprio(2);
    v3u xr[3];
    const rsrc_t rX = mk(xg, 2304);
#pragma unroll
    for (int k = 0; k < 3; ++k) xr[k] = __builtin_amdgcn_raw_buffer_load_b96(rX, (lane >> 5) * 1152 + (lane & 31) * 12, k * 384, 0);
    float xv[18];
#pragma unroll
    for (int k = 0; k < 3; ++k)
#pragma unroll
      for (int e = 0; e < 3; ++e) { xv[k * 6 + 2 * e] = u2f(xr[k][e] << 16); xv[k * 6 + 2 * e + 1] = u2f(xr[k][e] & 0xffff0000u); }
    float sum = 0.f;
#pragma unroll
    for (int i = 0; i < 18; ++i) sum += xv[i];
    const float mean = sum16x(rowsum(sum)) * (1.0f / 576.0f);
    float sq = 0.f;
#pragma unroll
    for (int i = 0; i < 18; ++i) { const float d = xv[i] - mean; sq = fmaf(d, d, sq); }
    const float var = sum16x(rowsum(sq)) * (1.0f / 576.0f);
    const float mb = rbf(mean), sd = rbf(__builtin_sqrtf(rbf(rbf(var) + 1e-5f))), rs = 1.0f / sd;
    LAS v2u* dst = (LAS v2u*)(L + L_XN + (lane >> 5) * XN_S + (lane & 31) * 6);
#pragma unroll
    for (int k = 0; k < 3; ++k)
#pragma unroll
      for (int e = 0; e < 3; ++e) {
        float n[2];
#pragma unroll
        for (int f = 0; f < 2; ++f) { const float d = rbf(xv[k * 6 + 2 * e + f] - mb); const float q0 = d * rs; n[f] = fmaf(fmaf(-q0, sd, d), rs, q0); }
        dst[k * 96 + e] = v2u{cvtpk(n[0], n[0]), cvtpk(n[1], n[1])};
      }
    lds_signal(L + L_FL + F_XN, lane);
    __builtin_amdgcn_s_setprio(0);
  }
  const unsigned hm = (c & 1) ? 0xffff0000u : 0x0000ffffu;
  const int mt = (c & 7) >> 1;
  const v4u am = {mt == 0 ? hm : 0u, mt == 1 ? hm : 0u, mt == 2 ? hm : 0u, mt == 3 ? hm : 0u};
  unsigned T = 0;
  f32x4 ga = {0.f, 0.f, 0.f, 0.f};
#pragma unroll 1
  for (int t = 0; t < (isA ? 1 : 2); ++t, row0 += 144) {
    unsigned so = row0 * stride;
#pragma unroll
    for (int i = 0; i < 36; ++i) { w[i] = ld_nt(rG, gvo, so); so += 4 * stride; }
    SB();
    if (isA) {
      T = __builtin_amdgcn_readfirstlane(E) + 0x9E3779B9u;
      if (lane == 0) L[L_FL + F_T] = T;
      lds_wait(L + L_FL + F_XN);
    }
    asm volatile("" :: "v"(cv));
    unsigned xv[36];
#pragma unroll
    for (int i = 0; i < 36; ++i) xv[i] = L[L_XN + (c >> 3) * XN_S + row0 + g + 4 * i];
    SB();
    ga = gemv<36>(ga, xv, am, w);
  }
  if (isA) {
    const float s2 = dpp<XOR8>(ga[2]), s3 = dpp<XOR8>(ga[3]);
    const unsigned o = QG_OFF + (((u >> 1) * 2 + (g >> 1)) * 4 + qq) * 1024 + ((u & 1) * 64 + (c & 7) * 8 + (g & 1) * 4 + (c >> 3) * 2) * 8;
    st_sc(rW, o, 0, gran(c < 8 ? ga[0] : s2, c < 8 ? ga[1] : s3, T));
    __builtin_amdgcn_s_sleep(32);
    float q0, q1;
#pragma unroll 1
    for (int spin = 0; spin < SPIN_CAP; ++spin) {
      MEMBAR();
#pragma unroll
      for (int i = 0; i < 4; ++i) w[i] = ld_sc(rW, lane * 16, QG_OFF + ((h * 2 + b) * 4 + i) * 1024);
      q0 = (u2f(w[0].x) + u2f(w[1].x)) + (u2f(w[2].x) + u2f(w[3].x));
      q1 = (u2f(w[0].z) + u2f(w[1].z)) + (u2f(w[2].z) + u2f(w[3].z));
      if (none_bad(tagbad(w[0], T) | tagbad(w[1], T) | tagbad(w[2], T) | tagbad(w[3], T))) break;
      __builtin_amdgcn_s_sleep(16);
    }
    L[L_QB + lane] = cvtpk(q0, q1);
    if (lane == 0) L[L_FL + F_TK] = ta + LAG0;
    lds_signal(L + L_FL + F_Q, lane);
    __builtin_amdgcn_s_setprio(0);
#pragma unroll 1
    for (int k = 0; k < 2; ++k) {
      while (__builtin_amdgcn_readfirstlane(lds_ld(L + L_FL + F_PB)) < 1u + 6u * k) __builtin_amdgcn_s_sleep(8);
      MEMBAR();
      const unsigned ps = lds_ld(L + L_FL + F_PS);
      if (bid < 8) st_sc(rW, lane == 0 ? RT_OFF + k * 256 + bid * 16 : OOB, 0, v4u{ps, T, ps, T});
#pragma unroll 1
      for (int t = 0; t < 3; ++t) {
        __builtin_amdgcn_s_sleep(64);
        MEMBAR();
        const v4u v = ld_sc(rW, (lane & 7) * 16, RT_OFF + k * 256);
        if (!none_bad(tagbad(v, T))) continue;
        int d = (int)(v.x - ps);
        d += (int)dppu<XOR1>(d); d += (int)dppu<XOR2>(d); d += (int)dppu<XOR7>(d);
        if (lane == 0) lds_st(L + L_FL + F_TK, ps + min(max(d >> 3, -500), 500) - LAGC);
        break;
      }
    }
  } else {
    st_sc(rW, c < 9 ? (unsigned)((g >> 1) * 288 + c * 32 + (g & 1) * 16) : OOB, GU_OFF + bid * 640, __builtin_bit_cast(v4u, ga));
    lds_wait4(L + L_FL + F_KD, lane);
    T = __builtin_amdgcn_readfirstlane(L[L_FL + F_T]);
    float mw[4], lw[4];
#pragma unroll
    for (int i = 0; i < 4; ++i) { mw[i] = Lf[L_ML + 2 * i]; lw[i] = Lf[L_ML + 2 * i + 1]; }
    const float mg = fmaxf(fmaxf(mw[0], mw[1]), fmaxf(mw[2], mw[3]));
    float lg = 0.f;
#pragma unroll
    for (int i = 0; i < 4; ++i) lg += lw[i] * ex2((mw[i] - mg) * LOG2E);
    VMWAIT(0);
    st_sc(rW, lane == 0 ? ML_OFF + h * 128 + j * 16 : OOB, 0, gran(mg, lg, T));
    v4u v;
#pragma unroll 1
    for (int spin = 0; spin < SPIN_CAP; ++spin) {
      __builtin_amdgcn_s_sleep(80);
      MEMBAR();
      v = ld_sc(rW, ((lane & 7) ^ (b * 4)) * 16, ML_OFF + h * 128);
      if (none_bad(tagbad(v, T))) break;
    }
    MEMBAR();
#pragma unroll
    for (int i = 0; i < 8; ++i) w[i] = ld_sc(rW, lane < 36 ? (unsigned)(lane * 16) : OOB, GU_OFF + (i * 32 + h) * 640);
#pragma unroll
    for (int hf = 0; hf < 2; ++hf) {
      v4u hv;
#pragma unroll
      for (int e = 0; e < 4; ++e) {
        const float gt = rbf(u2f(w[2 * hf][e]) + u2f(w[4 + 2 * hf][e])), up = rbf(u2f(w[1 + 2 * hf][e]) + u2f(w[5 + 2 * hf][e]));
        const float t2 = 1.5957691216057308f * (gt + 0.044715f * gt * gt * gt);
        const float hh = rbf(gt / (1.0f + ex2(-t2 * LOG2E))) * up;
        hv[e] = cvtpk(hh, hh);
      }
      if (lane < 36) *(LAS v4u*)(L + L_HD + (lane >= 18 ? HD_S - 72 : 0) + hf * 72 + lane * 4) = hv;
    }
    const float ms = u2f(v.x);
    float M = fmaxf(ms, dpp<XOR1>(ms)); M = fmaxf(M, dpp<XOR2>(M));
    float Ls = u2f(v.z) * ex2((ms - M) * LOG2E);
    Ls += dpp<XOR1>(Ls); Ls += dpp<XOR2>(Ls);
    const float inv = 1.0f / Ls;
    if (lane == 0) *(LAS f32x4*)(Lf + L_FW) = f32x4{ex2((mw[0] - M) * LOG2E) * inv, ex2((mw[1] - M) * LOG2E) * inv, ex2((mw[2] - M) * LOG2E) * inv, ex2((mw[3] - M) * LOG2E) * inv};
    lds_signal(L + L_FL + F_FW, lane);
    const unsigned o = lane < 8 ? OG_OFF + h * 8192 + (lane >> 1) * 2048 + j * 256 + (lane & 1) * 128 : lane < 17 ? YG_OFF + bid * 1152 + (lane - 8) * 128 : OOB;
    const unsigned t = __builtin_amdgcn_raw_buffer_load_b32(rW, o, 0, AUX_SC1);
    asm volatile("" :: "v"(t));
  }
}

extern "C" void kernel_launch(void* const* d_in, const int* in_sizes, int n_in, void* d_out, int out_size, void* d_ws, size_t ws_size,
                              hipStream_t stream) {
  fused_decode<<<256, 384, 0, stream>>>((const char*)d_in[0], (const char*)d_in[1], (const char*)d_in[2], (const char*)d_in[3],
                                        (const char*)d_in[4], (const char*)d_in[6], (const char*)d_in[7], (const char*)d_in[8],
                                        (char*)d_out, (char*)d_ws);
}
```
